```python
import torch

def _softmax(x, axis=-1):
    return torch.softmax(x, dim=axis)

def _gelu(x):
    return torch.nn.functional.gelu(x, approximate='tanh')

def _silu(x):
    return torch.nn.functional.silu(x)

def _sigmoid(x):
    return torch.sigmoid(x)

def _layernorm(x, axis=-1, eps=1e-05):
    m = x.mean(dim=axis, keepdim=True)
    v = x.var(dim=axis, keepdim=True, unbiased=False)
    return (x - m) / torch.sqrt(v + eps)

def _scatter_add(tgt, idx, src, axis=0):
    return tgt.clone().index_add_(axis, idx, src)

def _segment_sum(src, seg, n):
    out = torch.zeros((n,) + src.shape[1:], dtype=src.dtype)
    return out.index_add_(0, seg, src)

def _pad(x, pw):
    flat = tuple((v for pair in reversed(pw) for v in pair))
    return torch.nn.functional.pad(x, flat)
kernel_name = 'sequential_preLN__memory_aug_recurrent_mem_tr__glu_gated_swiglu__prompt'

def setup_inputs():

    _w = lambda a: (lambda t: t.to(torch.bfloat16) if t.is_floating_point() else t)(torch.as_tensor(a))

    def _A_setup():
        B, S, Dm = (1, 2048, 640)
        H, Dh = (32, 128)
        M, seg = (8, 32)
        seg = min(seg, S)
        S = S // seg * seg
        return {'x': _w((lambda *s: torch.randn(*s, dtype=torch.bfloat16) * 0.05)(B, S, Dm)), 'mem0': _w((lambda *s: torch.randn(*s, dtype=torch.bfloat16) * 0.2)(B, M, Dm)), 'Wq': _w((lambda *s: torch.randn(*s, dtype=torch.bfloat16) * 1.6)(Dm, H, Dh) * Dm ** (-0.5)), 'Wk': _w((lambda *s: torch.randn(*s, dtype=torch.bfloat16) * 1.6)(Dm, H, Dh) * Dm ** (-0.5)), 'Wv': _w((lambda *s: torch.randn(*s, dtype=torch.bfloat16) * 0.2)(Dm, H, Dh) * Dm ** (-0.5)), 'Wo': _w((lambda *s: torch.randn(*s, dtype=torch.bfloat16) * 0.2)(H, Dh, Dm) * (H * Dh) ** (-0.5)), 'seg_len': seg}
    _A_in = _A_setup()

    def _B_setup():
        B, S, D, F = (1, 2048, 640, 1728)
        return {'x': _w((lambda *s: torch.randn(*s, dtype=torch.bfloat16) * 0.05)(B, S, D)), 'w1': _w((lambda *s: torch.randn(*s, dtype=torch.bfloat16) * 0.2)(D, F) * (2.0 / D) ** 0.5), 'w3': _w((lambda *s: torch.randn(*s, dtype=torch.bfloat16) * 0.2)(D, F) * (2.0 / D) ** 0.5), 'w2': _w((lambda *s: torch.randn(*s, dtype=torch.bfloat16) * 0.2)(F, D) * (2.0 / F) ** 0.5)}
    _B_in = _B_setup()
    return {'A_x': _A_in['x'], 'A_mem0': _A_in['mem0'], 'A_Wq': _A_in['Wq'], 'A_Wk': _A_in['Wk'], 'A_Wv': _A_in['Wv'], 'A_Wo': _A_in['Wo'], 'A_seg_len': _A_in['seg_len'], 'B_x': _B_in['x'], 'B_w1': _B_in['w1'], 'B_w3': _B_in['w3'], 'B_w2': _B_in['w2']}

def reference(A_x, A_mem0, A_Wq, A_Wk, A_Wv, A_Wo, A_seg_len, B_x, B_w1, B_w3, B_w2):

    def _A_ref(x, mem0, Wq, Wk, Wv, Wo, seg_len):
        B, S, Dm = x.shape
        H, Dh = (Wq.shape[1], Wq.shape[2])
        M = mem0.shape[1]
        scale = Dh ** (-0.5)
        n_seg = S // seg_len
        L = M + seg_len
        idx = torch.arange(L)
        mask = (idx[:, None] < M) | (idx[:, None] >= idx[None, :])
        mem = mem0
        outs = []
        for s in range(n_seg):
            xs = x[:, s * seg_len:(s + 1) * seg_len, :].clone()
            h = torch.concatenate([mem, xs], axis=1)
            q = torch.einsum('btd,dhe->bhte', h, Wq)
            k = torch.einsum('btd,dhe->bhte', h, Wk)
            v = torch.einsum('btd,dhe->bhte', h, Wv)
            scores = torch.einsum('bhqe,bhke->bhqk', q, k) * scale
            scores = torch.where(mask[None, None], scores, -1000000000.0)
            p = _softmax(scores, axis=-1)
            o = torch.einsum('bhqk,bhke->bhqe', p, v)
            o = torch.einsum('bhte,hed->btd', o, Wo)
            mem = o[:, :M, :].clone()
            outs.append(o[:, M:, :])
        return torch.concatenate(outs, axis=1)

    def _B_ref(x, w1, w3, w2):
        gate = torch.einsum('bsd,df->bsf', x, w1)
        up = torch.einsum('bsd,df->bsf', x, w3)
        h = _silu(gate) * up
        out = torch.einsum('bsf,fd->bsd', h, w2)
        return out
    x = A_x
    h = x + _A_ref(_layernorm(x, axis=-1), A_mem0, A_Wq, A_Wk, A_Wv, A_Wo, A_seg_len)
    h2 = h + _B_ref(_layernorm(h, axis=-1), B_w1, B_w3, B_w2)
    return h2
```

```cpp
#include <hip/hip_runtime.h>
#include <hip/hip_bf16.h>
#define LAS __attribute__((address_space(3)))
#define GAS __attribute__((address_space(1)))
#define DI __device__ __forceinline__
typedef unsigned u32;
typedef unsigned short u16;
typedef u32 u32x2 __attribute__((ext_vector_type(2)));
typedef u32 u32x4 __attribute__((ext_vector_type(4)));
typedef u32 u32x8 __attribute__((ext_vector_type(8)));
typedef float f32x2 __attribute__((ext_vector_type(2)));
typedef float f32x4 __attribute__((ext_vector_type(4)));
typedef __bf16 bf16x2 __attribute__((ext_vector_type(2)));
typedef __bf16 bf16x8 __attribute__((ext_vector_type(8)));
typedef __bf16 bf16x16 __attribute__((ext_vector_type(16)));
typedef short s16x4 __attribute__((ext_vector_type(4)));
typedef LAS char* lptr;

constexpr int S_ = 2048, DM = 640, NH = 32, DH = 128, NSEG = 64, FF = 1728;

DI f32x4 smf(u32x4 a, u32x8 b, f32x4 c, int idx) {
  return __builtin_amdgcn_smfmac_f32_16x16x64_bf16(__builtin_bit_cast(bf16x8, a), __builtin_bit_cast(bf16x16, b), c, idx, 0, 0);
}
DI f32x4 mm32(u32x4 a, u32x4 b, f32x4 c) {
  return __builtin_amdgcn_mfma_f32_16x16x32_bf16(__builtin_bit_cast(bf16x8, a), __builtin_bit_cast(bf16x8, b), c, 0, 0, 0);
}
DI f32x4 mm16(u32x2 a, u32x2 b, f32x4 c) {
  return __builtin_amdgcn_mfma_f32_16x16x16bf16_1k(__builtin_bit_cast(s16x4, a), __builtin_bit_cast(s16x4, b), c, 0, 0, 0);
}
DI u32 pk2(float lo, float hi) { f32x2 v = {lo, hi}; return __builtin_bit_cast(u32, __builtin_convertvector(v, bf16x2)); }
DI float bflo(u32 v) { return __builtin_bit_cast(float, v << 16); }
DI float bfhi(u32 v) { return __builtin_bit_cast(float, v & 0xffff0000u); }
DI float rbf(float x) { return bflo(pk2(x, 0.f)); }
DI u32x2 trrd(lptr p) { return __builtin_bit_cast(u32x2, __builtin_amdgcn_ds_read_tr16_b64_v4i16((LAS s16x4*)p)); }
DI u32x4 lrd16(lptr p) { return *(LAS u32x4*)p; }
DI void lwr16(lptr p, u32x4 v) { *(LAS u32x4*)p = v; }
DI void lwr8(lptr p, u32x2 v) { *(LAS u32x2*)p = v; }
DI void lwr4(lptr p, u32 v) { *(LAS u32*)p = v; }
DI float fbits(u32 v) { return __builtin_bit_cast(float, v); }
DI u32 ubits(float v) { return __builtin_bit_cast(u32, v); }
DI float swapadd32(float a, float b) {
  auto r = __builtin_amdgcn_permlane32_swap(ubits(a), ubits(b), false, false);
  return fbits(r[0]) + fbits(r[1]);
}
DI float xmax(float x) {
  auto r = __builtin_amdgcn_permlane32_swap(ubits(x), ubits(x), false, false);
  float y = fmaxf(fbits(r[0]), fbits(r[1]));
  auto s = __builtin_amdgcn_permlane16_swap(ubits(y), ubits(y), false, false);
  return fmaxf(fbits(s[0]), fbits(s[1]));
}
DI float xsum(float x) {
  auto r = __builtin_amdgcn_permlane32_swap(ubits(x), ubits(x), false, false);
  float y = fbits(r[0]) + fbits(r[1]);
  auto s = __builtin_amdgcn_permlane16_swap(ubits(y), ubits(y), false, false);
  return fbits(s[0]) + fbits(s[1]);
}
DI u32 umax3(u32 a, u32 b, u32 c) { u32 r; asm("v_max3_u32 %0, %1, %2, %3" : "=v"(r) : "v"(a), "v"(b), "v"(c)); return r; }
DI u32 umax4(u32 m, u32x4 v) { return umax3(umax3(m, v.x, v.y), v.z, v.w); }
DI u32 xcc_id() { return (u32)__builtin_amdgcn_s_getreg(20 | (3 << 11)) & 0xfu; }
#define BAR() __builtin_amdgcn_s_barrier()
#define WAITL() asm volatile("s_waitcnt lgkmcnt(0)" ::: "memory")
#define WAITV0() asm volatile("s_waitcnt vmcnt(0)" ::: "memory")
#define CFENCE() asm volatile("" ::: "memory")
typedef __amdgpu_buffer_rsrc_t rsrc_t;
DI rsrc_t mkrsrc(const void* p, u32 bytes) { return __builtin_amdgcn_make_buffer_rsrc((void*)p, 0, bytes, 0x00020000); }
constexpr int SC1 = 16;
DI u32x4 bld16(rsrc_t r, u32 vo, u32 so) { return __builtin_amdgcn_raw_buffer_load_b128(r, vo, so, SC1); }
DI void bst16(rsrc_t r, u32 vo, u32 so, u32x4 v) {
  __builtin_amdgcn_raw_buffer_store_b128(v, r, vo, so, SC1);
  __builtin_amdgcn_sched_barrier(0); asm volatile("s_nop 1"); __builtin_amdgcn_sched_barrier(0);
}
DI void bst8(rsrc_t r, u32 vo, u32 so, u32x2 v) { __builtin_amdgcn_raw_buffer_store_b64(v, r, vo, so, SC1); }
DI void bst4(rsrc_t r, u32 vo, u32 so, u32 v) { __builtin_amdgcn_raw_buffer_store_b32(v, r, vo, so, SC1); }
DI void glds16(const void* src, lptr dst) { __builtin_amdgcn_global_load_lds((const GAS u32*)src, (LAS u32*)dst, 16, 0, 0); }

template <int B0, int B1, int B2> DI u32 swz3(u32 r) { return ((r >> B0) & 1) | (((r >> B1) & 1) << 1) | (((r >> B2) & 1) << 2); }
template <int ROWS, int B0, int B1, int B2>
DI void stage128(lptr dst, const u16* src, int ld, int wave, int lane) {
#pragma unroll 4
  for (int i = 0; i < ROWS / 16; ++i) {
    const int blk = i * 4 + wave;
    const u32 row = blk * 4 + (lane >> 4);
    const u32 ch = (lane & 15) ^ (swz3<B0, B1, B2>(row) << 1);
    glds16(src + (size_t)row * ld + ch * 8, dst + blk * 1024);
  }
}
template <int B0, int B1, int B2> DI u32 off128(u32 row, u32 col) {
  return row * 256 + ((((col >> 3) ^ (swz3<B0, B1, B2>(row) << 1))) << 4) + ((col & 4) << 1);
}
template <int ROWS, int B1, int B2>
DI void stage64(lptr dst, const u16* src, int ld, int wave, int lane) {
#pragma unroll 4
  for (int i = 0; i < ROWS / 32; ++i) {
    const int blk = i * 4 + wave;
    const u32 row = blk * 8 + (lane >> 3);
    const u32 ch = (lane & 7) ^ ((((row >> B1) & 1) | (((row >> B2) & 1) << 1)) << 1);
    glds16(src + (size_t)row * ld + ch * 8, dst + blk * 1024);
  }
}
template <int B1, int B2> DI u32 off64(u32 row, u32 col) {
  return row * 128 + (((col >> 3) ^ ((((row >> B1) & 1) | (((row >> B2) & 1) << 1)) << 1)) << 4) + ((col & 4) << 1);
}
#define X_ASM_TEXT \
"s_setprio 3\n" \
"s_getpc_b64 s[46:47]\n" \
"XCW_%=:\n" \
"v_mbcnt_lo_u32_b32 v60, -1, 0\n" \
"v_mbcnt_hi_u32_b32 v60, -1, v60\n" \
"v_lshlrev_b32 v60, 7, v60\n" \
"v_min_u32 v61, XCZ_%=-XCW_%=, v60\n" \
"global_load_dword v62, v61, s[46:47]\n" \
"v_add_u32 v60, 0x2000, v60\n" \
"v_min_u32 v61, XCZ_%=-XCW_%=, v60\n" \
"global_load_dword v62, v61, s[46:47]\n" \
"v_add_u32 v60, 0x2000, v60\n" \
"s_mov_b64 s[36:37], %[ws]\n" \
"s_and_b32 s37, s37, 0xffff\n" \
"s_mov_b32 s38, 0x800000\n" \
"s_mov_b32 s39, 0x00020000\n" \
"s_mov_b64 s[40:41], %[ws]\n" \
"s_add_u32 s40, s40, 0x800000\n" \
"s_addc_u32 s41, s41, 0\n" \
"s_and_b32 s41, s41, 0xffff\n" \
"s_mov_b32 s42, 0x9000000\n" \
"s_mov_b32 s43, 0x00020000\n" \
"s_mov_b32 s35, %[wave]\n" \
"v_mbcnt_lo_u32_b32 v48, -1, 0\n" \
"v_mbcnt_hi_u32_b32 v48, -1, v48\n" \
"s_lshl_b32 s55, s35, 6\n" \
"v_add_lshl_u32 v48, v48, s55, 4\n" \
"ds_read_b128 v[0:3], v48 offset:0\n" \
"ds_read_b128 v[4:7], v48 offset:4096\n" \
"ds_read_b128 v[8:11], v48 offset:8192\n" \
"ds_read_b128 v[12:15], v48 offset:12288\n" \
"ds_read_b128 v[16:19], v48 offset:16384\n" \
"ds_read_b128 v[32:35], v48 offset:20480\n" \
"ds_read_b128 v[36:39], v48 offset:24576\n" \
"ds_read_b128 v[40:43], v48 offset:28672\n" \
"ds_read_b128 v[44:47], v48 offset:32768\n" \
"s_waitcnt lgkmcnt(0)\n" \
"s_barrier\n" \
"v_readfirstlane_b32 s70, v32\n" \
"v_readfirstlane_b32 s71, v33\n" \
"v_readfirstlane_b32 s72, v34\n" \
"v_readfirstlane_b32 s73, v35\n" \
"v_readfirstlane_b32 s74, v36\n" \
"v_readfirstlane_b32 s75, v37\n" \
"v_readfirstlane_b32 s76, v38\n" \
"v_readfirstlane_b32 s77, v39\n" \
"v_readfirstlane_b32 s67, v40\n" \
"v_readfirstlane_b32 s69, v41\n" \
"v_readfirstlane_b32 s33, v42\n" \
"v_readfirstlane_b32 s25, v43\n" \
"v_readfirstlane_b32 s78, v44\n" \
"v_readfirstlane_b32 s79, v45\n" \
"v_readfirstlane_b32 s80, v46\n" \
"v_readfirstlane_b32 s81, v47\n" \
"s_nop 4\n" \
"s_add_u32 s68, s67, 4096\n" \
"s_bitcmp1_b32 %[par], 0\n" \
"s_cselect_b32 s52, 0x2000, 0\n" \
"s_lshl_b32 s62, s35, 10\n" \
"s_and_b32 s63, s62, 1024\n" \
"s_add_u32 s49, s52, s63\n" \
"s_bitcmp1_b32 s35, 1\n" \
"s_cselect_b32 s51, 0x6800, 0\n" \
"s_cselect_b32 s58, 0, -1\n" \
"s_cselect_b32 s59, -1, 0\n" \
"s_add_u32 s51, s51, s49\n" \
"s_add_u32 s51, s51, 0x1800\n" \
"s_add_u32 s52, s52, 0x100000\n" \
"s_mov_b32 s56, 0xcccccccc\n" \
"s_mov_b32 s57, 0xcccccccc\n" \
"s_mov_b32 s64, 0x3e0293ee\n" \
"s_mov_b32 s65, 0xff81ff81\n" \
"s_mov_b32 s66, 0x40000\n" \
"s_mov_b32 s48, 0\n" \
"s_mov_b32 s23, 0\n" \
"s_getpc_b64 s[60:61]\n" \
"PXS2_%=:\n" \
"s_add_u32 s60, s60, XS2_%=-PXS2_%=\n" \
"s_addc_u32 s61, s61, 0\n" \
"s_getpc_b64 s[28:29]\n" \
"PXSB_%=:\n" \
"s_add_u32 s28, s28, XSB_%=-PXSB_%=\n" \
"s_addc_u32 s29, s29, 0\n" \
"s_sub_u32 s28, s28, s78\n" \
"s_subb_u32 s29, s29, 0\n" \
"s_getpc_b64 s[30:31]\n" \
"PXSC_%=:\n" \
"s_add_u32 s30, s30, XSC_%=-PXSC_%=\n" \
"s_addc_u32 s31, s31, 0\n" \
"s_sub_u32 s30, s30, s79\n" \
"s_subb_u32 s31, s31, 0\n" \
"s_getpc_b64 s[16:17]\n" \
"PXSD_%=:\n" \
"s_add_u32 s16, s16, XSD_%=-PXSD_%=\n" \
"s_addc_u32 s17, s17, 0\n" \
"s_sub_u32 s16, s16, s81\n" \
"s_subb_u32 s17, s17, 0\n" \
"v_and_b32 v28, 0xff, v18\n" \
"v_and_b32 v29, 0xffffff00, v18\n" \
"v_lshl_add_u32 v28, v29, 1, v28\n" \
"s_cmp_lg_u32 s80, 0\n" \
"s_cbranch_scc1 XHS_%=\n" \
"s_sleep 24\n" \
"XHS_%=:\n" \
"s_mov_b64 s[44:45], s[72:73]\n" \
"s_and_b32 s45, s45, 0xffff\n" \
"s_mov_b32 s46, 0x500000\n" \
"s_mov_b32 s47, 0x00020000\n" \
"s_add_u32 m0, s62, 0\n" \
"s_mov_b32 s54, 0x0\n" \
"buffer_load_dwordx4 v12, s[44:47], s54 offen lds\n" \
"s_add_u32 m0, s62, 4096\n" \
"s_mov_b32 s54, 0x20000\n" \
"buffer_load_dwordx4 v12, s[44:47], s54 offen lds\n" \
"s_add_u32 m0, s62, 8192\n" \
"s_mov_b32 s54, 0x40000\n" \
"buffer_load_dwordx4 v12, s[44:47], s54 offen lds\n" \
"s_add_u32 m0, s62, 12288\n" \
"s_mov_b32 s54, 0x60000\n" \
"buffer_load_dwordx4 v12, s[44:47], s54 offen lds\n" \
"s_add_u32 m0, s62, 16384\n" \
"s_mov_b32 s54, 0x80000\n" \
"buffer_load_dwordx4 v12, s[44:47], s54 offen lds\n" \
"s_add_u32 m0, s62, 20480\n" \
"s_mov_b32 s54, 0xa0000\n" \
"buffer_load_dwordx4 v12, s[44:47], s54 offen lds\n" \
"s_add_u32 m0, s62, 24576\n" \
"s_mov_b32 s54, 0xc0000\n" \
"buffer_load_dwordx4 v12, s[44:47], s54 offen lds\n" \
"s_add_u32 m0, s62, 28672\n" \
"s_mov_b32 s54, 0xe0000\n" \
"buffer_load_dwordx4 v12, s[44:47], s54 offen lds\n" \
"s_mov_b64 s[44:45], s[70:71]\n" \
"s_and_b32 s45, s45, 0xffff\n" \
"s_mov_b32 s46, 0x500000\n" \
"s_mov_b32 s47, 0x00020000\n" \
"s_add_u32 m0, s62, 32768\n" \
"s_mov_b32 s54, 0x0\n" \
"buffer_load_dwordx4 v12, s[44:47], s54 offen lds\n" \
"s_add_u32 m0, s62, 36864\n" \
"s_mov_b32 s54, 0x20000\n" \
"buffer_load_dwordx4 v12, s[44:47], s54 offen lds\n" \
"s_add_u32 m0, s62, 40960\n" \
"s_mov_b32 s54, 0x40000\n" \
"buffer_load_dwordx4 v12, s[44:47], s54 offen lds\n" \
"s_add_u32 m0, s62, 45056\n" \
"s_mov_b32 s54, 0x60000\n" \
"buffer_load_dwordx4 v12, s[44:47], s54 offen lds\n" \
"s_add_u32 m0, s62, 49152\n" \
"s_mov_b32 s54, 0x80000\n" \
"buffer_load_dwordx4 v12, s[44:47], s54 offen lds\n" \
"s_add_u32 m0, s62, 53248\n" \
"s_mov_b32 s54, 0xa0000\n" \
"buffer_load_dwordx4 v12, s[44:47], s54 offen lds\n" \
"s_add_u32 m0, s62, 57344\n" \
"s_mov_b32 s54, 0xc0000\n" \
"buffer_load_dwordx4 v12, s[44:47], s54 offen lds\n" \
"s_add_u32 m0, s62, 61440\n" \
"s_mov_b32 s54, 0xe0000\n" \
"buffer_load_dwordx4 v12, s[44:47], s54 offen lds\n" \
"s_mov_b64 s[44:45], s[74:75]\n" \
"s_and_b32 s45, s45, 0xffff\n" \
"s_mov_b32 s46, 0x500000\n" \
"s_mov_b32 s47, 0x00020000\n" \
"s_add_u32 m0, s62, 65536\n" \
"s_mov_b32 s54, 0x0\n" \
"buffer_load_dwordx4 v13, s[44:47], s54 offen lds\n" \
"s_add_u32 m0, s62, 69632\n" \
"s_mov_b32 s54, 0x40000\n" \
"buffer_load_dwordx4 v13, s[44:47], s54 offen lds\n" \
"s_add_u32 m0, s62, 73728\n" \
"s_mov_b32 s54, 0x80000\n" \
"buffer_load_dwordx4 v13, s[44:47], s54 offen lds\n" \
"s_add_u32 m0, s62, 77824\n" \
"s_mov_b32 s54, 0xc0000\n" \
"buffer_load_dwordx4 v13, s[44:47], s54 offen lds\n" \
"s_add_u32 m0, s62, 81920\n" \
"s_mov_b32 s54, 0x100000\n" \
"buffer_load_dwordx4 v13, s[44:47], s54 offen lds\n" \
"s_add_u32 m0, s62, 86016\n" \
"s_mov_b32 s54, 0x140000\n" \
"buffer_load_dwordx4 v13, s[44:47], s54 offen lds\n" \
"s_add_u32 m0, s62, 90112\n" \
"s_mov_b32 s54, 0x180000\n" \
"buffer_load_dwordx4 v13, s[44:47], s54 offen lds\n" \
"s_add_u32 m0, s62, 94208\n" \
"s_mov_b32 s54, 0x1c0000\n" \
"buffer_load_dwordx4 v13, s[44:47], s54 offen lds\n" \
"s_mov_b64 s[44:45], s[72:73]\n" \
"s_and_b32 s45, s45, 0xffff\n" \
"s_mov_b32 s46, 0x500000\n" \
"s_mov_b32 s47, 0x00020000\n" \
"s_add_u32 m0, s62, 98304\n" \
"s_mov_b32 s54, 0x100000\n" \
"buffer_load_dwordx4 v12, s[44:47], s54 offen lds\n" \
"s_add_u32 m0, s62, 102400\n" \
"s_mov_b32 s54, 0x120000\n" \
"buffer_load_dwordx4 v12, s[44:47], s54 offen lds\n" \
"s_add_u32 m0, s62, 106496\n" \
"s_mov_b32 s54, 0x140000\n" \
"buffer_load_dwordx4 v12, s[44:47], s54 offen lds\n" \
"s_add_u32 m0, s62, 110592\n" \
"s_mov_b32 s54, 0x160000\n" \
"buffer_load_dwordx4 v12, s[44:47], s54 offen lds\n" \
"s_add_u32 m0, s62, 114688\n" \
"s_mov_b32 s54, 0x180000\n" \
"buffer_load_dwordx4 v12, s[44:47], s54 offen lds\n" \
"s_add_u32 m0, s62, 118784\n" \
"s_mov_b32 s54, 0x1a0000\n" \
"buffer_load_dwordx4 v12, s[44:47], s54 offen lds\n" \
"s_add_u32 m0, s62, 122880\n" \
"s_mov_b32 s54, 0x1c0000\n" \
"buffer_load_dwordx4 v12, s[44:47], s54 offen lds\n" \
"s_add_u32 m0, s62, 126976\n" \
"s_mov_b32 s54, 0x1e0000\n" \
"buffer_load_dwordx4 v12, s[44:47], s54 offen lds\n" \
"s_mov_b64 s[44:45], s[70:71]\n" \
"s_and_b32 s45, s45, 0xffff\n" \
"s_mov_b32 s46, 0x500000\n" \
"s_mov_b32 s47, 0x00020000\n" \
"s_add_u32 m0, s62, 131072\n" \
"s_mov_b32 s54, 0x100000\n" \
"buffer_load_dwordx4 v12, s[44:47], s54 offen lds\n" \
"s_add_u32 m0, s62, 135168\n" \
"s_mov_b32 s54, 0x120000\n" \
"buffer_load_dwordx4 v12, s[44:47], s54 offen lds\n" \
"s_add_u32 m0, s62, 139264\n" \
"s_mov_b32 s54, 0x140000\n" \
"buffer_load_dwordx4 v12, s[44:47], s54 offen lds\n" \
"s_add_u32 m0, s62, 143360\n" \
"s_mov_b32 s54, 0x160000\n" \
"buffer_load_dwordx4 v12, s[44:47], s54 offen lds\n" \
"s_add_u32 m0, s62, 147456\n" \
"s_mov_b32 s54, 0x180000\n" \
"buffer_load_dwordx4 v12, s[44:47], s54 offen lds\n" \
"s_add_u32 m0, s62, 151552\n" \
"s_mov_b32 s54, 0x1a0000\n" \
"buffer_load_dwordx4 v12, s[44:47], s54 offen lds\n" \
"s_add_u32 m0, s62, 155648\n" \
"s_mov_b32 s54, 0x1c0000\n" \
"buffer_load_dwordx4 v12, s[44:47], s54 offen lds\n" \
"s_add_u32 m0, s62, 159744\n" \
"s_mov_b32 s54, 0x1e0000\n" \
"buffer_load_dwordx4 v12, s[44:47], s54 offen lds\n" \
"s_waitcnt vmcnt(32)\n" \
"s_barrier\n" \
"v_add_u32 v19, 0x0, v14\n" \
"ds_read_b64_tr_b16 v[56:57], v19 offset:0\n" \
"ds_read_b64_tr_b16 v[58:59], v19 offset:512\n" \
"ds_read_b64_tr_b16 v[60:61], v19 offset:8192\n" \
"ds_read_b64_tr_b16 v[62:63], v19 offset:8704\n" \
"ds_read_b64_tr_b16 v[128:129], v19 offset:16384\n" \
"ds_read_b64_tr_b16 v[130:131], v19 offset:16896\n" \
"ds_read_b64_tr_b16 v[132:133], v19 offset:24576\n" \
"ds_read_b64_tr_b16 v[134:135], v19 offset:25088\n" \
"v_add_u32 v19, 0x0, v15\n" \
"ds_read_b64_tr_b16 v[64:65], v19 offset:0\n" \
"ds_read_b64_tr_b16 v[66:67], v19 offset:512\n" \
"ds_read_b64_tr_b16 v[68:69], v19 offset:8192\n" \
"ds_read_b64_tr_b16 v[70:71], v19 offset:8704\n" \
"ds_read_b64_tr_b16 v[136:137], v19 offset:16384\n" \
"ds_read_b64_tr_b16 v[138:139], v19 offset:16896\n" \
"ds_read_b64_tr_b16 v[140:141], v19 offset:24576\n" \
"ds_read_b64_tr_b16 v[142:143], v19 offset:25088\n" \
"s_waitcnt lgkmcnt(0)\n" \
"s_barrier\n" \
"s_mov_b64 s[44:45], s[72:73]\n" \
"s_and_b32 s45, s45, 0xffff\n" \
"s_mov_b32 s46, 0x500000\n" \
"s_mov_b32 s47, 0x00020000\n" \
"s_add_u32 m0, s62, 0\n" \
"s_mov_b32 s54, 0x200000\n" \
"buffer_load_dwordx4 v12, s[44:47], s54 offen lds\n" \
"s_add_u32 m0, s62, 4096\n" \
"s_mov_b32 s54, 0x220000\n" \
"buffer_load_dwordx4 v12, s[44:47], s54 offen lds\n" \
"s_add_u32 m0, s62, 8192\n" \
"s_mov_b32 s54, 0x240000\n" \
"buffer_load_dwordx4 v12, s[44:47], s54 offen lds\n" \
"s_add_u32 m0, s62, 12288\n" \
"s_mov_b32 s54, 0x260000\n" \
"buffer_load_dwordx4 v12, s[44:47], s54 offen lds\n" \
"s_add_u32 m0, s62, 16384\n" \
"s_mov_b32 s54, 0x280000\n" \
"buffer_load_dwordx4 v12, s[44:47], s54 offen lds\n" \
"s_add_u32 m0, s62, 20480\n" \
"s_mov_b32 s54, 0x2a0000\n" \
"buffer_load_dwordx4 v12, s[44:47], s54 offen lds\n" \
"s_add_u32 m0, s62, 24576\n" \
"s_mov_b32 s54, 0x2c0000\n" \
"buffer_load_dwordx4 v12, s[44:47], s54 offen lds\n" \
"s_add_u32 m0, s62, 28672\n" \
"s_mov_b32 s54, 0x2e0000\n" \
"buffer_load_dwordx4 v12, s[44:47], s54 offen lds\n" \
"s_waitcnt vmcnt(32)\n" \
"s_barrier\n" \
"v_add_u32 v19, 0x8000, v14\n" \
"ds_read_b64_tr_b16 v[88:89], v19 offset:0\n" \
"ds_read_b64_tr_b16 v[90:91], v19 offset:512\n" \
"ds_read_b64_tr_b16 v[92:93], v19 offset:8192\n" \
"ds_read_b64_tr_b16 v[94:95], v19 offset:8704\n" \
"ds_read_b64_tr_b16 v[112:113], v19 offset:16384\n" \
"ds_read_b64_tr_b16 v[114:115], v19 offset:16896\n" \
"ds_read_b64_tr_b16 v[116:117], v19 offset:24576\n" \
"ds_read_b64_tr_b16 v[118:119], v19 offset:25088\n" \
"v_add_u32 v19, 0x8000, v15\n" \
"ds_read_b64_tr_b16 v[96:97], v19 offset:0\n" \
"ds_read_b64_tr_b16 v[98:99], v19 offset:512\n" \
"ds_read_b64_tr_b16 v[100:101], v19 offset:8192\n" \
"ds_read_b64_tr_b16 v[102:103], v19 offset:8704\n" \
"ds_read_b64_tr_b16 v[120:121], v19 offset:16384\n" \
"ds_read_b64_tr_b16 v[122:123], v19 offset:16896\n" \
"ds_read_b64_tr_b16 v[124:125], v19 offset:24576\n" \
"ds_read_b64_tr_b16 v[126:127], v19 offset:25088\n" \
"s_waitcnt lgkmcnt(0)\n" \
"s_barrier\n" \
"s_mov_b64 s[44:45], s[70:71]\n" \
"s_and_b32 s45, s45, 0xffff\n" \
"s_mov_b32 s46, 0x500000\n" \
"s_mov_b32 s47, 0x00020000\n" \
"s_add_u32 m0, s62, 32768\n" \
"s_mov_b32 s54, 0x200000\n" \
"buffer_load_dwordx4 v12, s[44:47], s54 offen lds\n" \
"s_add_u32 m0, s62, 36864\n" \
"s_mov_b32 s54, 0x220000\n" \
"buffer_load_dwordx4 v12, s[44:47], s54 offen lds\n" \
"s_add_u32 m0, s62, 40960\n" \
"s_mov_b32 s54, 0x240000\n" \
"buffer_load_dwordx4 v12, s[44:47], s54 offen lds\n" \
"s_add_u32 m0, s62, 45056\n" \
"s_mov_b32 s54, 0x260000\n" \
"buffer_load_dwordx4 v12, s[44:47], s54 offen lds\n" \
"s_add_u32 m0, s62, 49152\n" \
"s_mov_b32 s54, 0x280000\n" \
"buffer_load_dwordx4 v12, s[44:47], s54 offen lds\n" \
"s_add_u32 m0, s62, 53248\n" \
"s_mov_b32 s54, 0x2a0000\n" \
"buffer_load_dwordx4 v12, s[44:47], s54 offen lds\n" \
"s_add_u32 m0, s62, 57344\n" \
"s_mov_b32 s54, 0x2c0000\n" \
"buffer_load_dwordx4 v12, s[44:47], s54 offen lds\n" \
"s_add_u32 m0, s62, 61440\n" \
"s_mov_b32 s54, 0x2e0000\n" \
"buffer_load_dwordx4 v12, s[44:47], s54 offen lds\n" \
"s_waitcnt vmcnt(32)\n" \
"s_barrier\n" \
"v_add_u32 v19, 0x10000, v16\n" \
"ds_read_b64_tr_b16 v[104:105], v19 offset:0\n" \
"ds_read_b64_tr_b16 v[106:107], v19 offset:256\n" \
"ds_read_b64_tr_b16 v[108:109], v19 offset:4096\n" \
"ds_read_b64_tr_b16 v[110:111], v19 offset:4352\n" \
"ds_read_b64_tr_b16 v[144:145], v19 offset:8192\n" \
"ds_read_b64_tr_b16 v[146:147], v19 offset:8448\n" \
"ds_read_b64_tr_b16 v[148:149], v19 offset:12288\n" \
"ds_read_b64_tr_b16 v[150:151], v19 offset:12544\n" \
"ds_read_b64_tr_b16 a[8:9], v19 offset:16384\n" \
"ds_read_b64_tr_b16 a[10:11], v19 offset:16640\n" \
"ds_read_b64_tr_b16 a[12:13], v19 offset:20480\n" \
"ds_read_b64_tr_b16 a[14:15], v19 offset:20736\n" \
"ds_read_b64_tr_b16 a[48:49], v19 offset:24576\n" \
"ds_read_b64_tr_b16 a[50:51], v19 offset:24832\n" \
"ds_read_b64_tr_b16 a[52:53], v19 offset:28672\n" \
"ds_read_b64_tr_b16 a[54:55], v19 offset:28928\n" \
"s_waitcnt lgkmcnt(0)\n" \
"s_barrier\n" \
"s_mov_b64 s[44:45], s[74:75]\n" \
"s_and_b32 s45, s45, 0xffff\n" \
"s_mov_b32 s46, 0x500000\n" \
"s_mov_b32 s47, 0x00020000\n" \
"s_add_u32 m0, s62, 65536\n" \
"s_mov_b32 s54, 0x200000\n" \
"buffer_load_dwordx4 v13, s[44:47], s54 offen lds\n" \
"s_add_u32 m0, s62, 69632\n" \
"s_mov_b32 s54, 0x240000\n" \
"buffer_load_dwordx4 v13, s[44:47], s54 offen lds\n" \
"s_add_u32 m0, s62, 73728\n" \
"s_mov_b32 s54, 0x280000\n" \
"buffer_load_dwordx4 v13, s[44:47], s54 offen lds\n" \
"s_add_u32 m0, s62, 77824\n" \
"s_mov_b32 s54, 0x2c0000\n" \
"buffer_load_dwordx4 v13, s[44:47], s54 offen lds\n" \
"s_add_u32 m0, s62, 81920\n" \
"s_mov_b32 s54, 0x300000\n" \
"buffer_load_dwordx4 v13, s[44:47], s54 offen lds\n" \
"s_add_u32 m0, s62, 86016\n" \
"s_mov_b32 s54, 0x340000\n" \
"buffer_load_dwordx4 v13, s[44:47], s54 offen lds\n" \
"s_add_u32 m0, s62, 90112\n" \
"s_mov_b32 s54, 0x380000\n" \
"buffer_load_dwordx4 v13, s[44:47], s54 offen lds\n" \
"s_add_u32 m0, s62, 94208\n" \
"s_mov_b32 s54, 0x3c0000\n" \
"buffer_load_dwordx4 v13, s[44:47], s54 offen lds\n" \
"s_waitcnt vmcnt(32)\n" \
"s_barrier\n" \
"v_add_u32 v19, 0x18000, v14\n" \
"ds_read_b64_tr_b16 v[40:41], v19 offset:0\n" \
"ds_read_b64_tr_b16 v[42:43], v19 offset:512\n" \
"ds_read_b64_tr_b16 v[44:45], v19 offset:8192\n" \
"ds_read_b64_tr_b16 v[46:47], v19 offset:8704\n" \
"ds_read_b64_tr_b16 a[32:33], v19 offset:16384\n" \
"ds_read_b64_tr_b16 a[34:35], v19 offset:16896\n" \
"ds_read_b64_tr_b16 a[36:37], v19 offset:24576\n" \
"ds_read_b64_tr_b16 a[38:39], v19 offset:25088\n" \
"v_add_u32 v19, 0x18000, v15\n" \
"ds_read_b64_tr_b16 a[0:1], v19 offset:0\n" \
"ds_read_b64_tr_b16 a[2:3], v19 offset:512\n" \
"ds_read_b64_tr_b16 a[4:5], v19 offset:8192\n" \
"ds_read_b64_tr_b16 a[6:7], v19 offset:8704\n" \
"ds_read_b64_tr_b16 a[40:41], v19 offset:16384\n" \
"ds_read_b64_tr_b16 a[42:43], v19 offset:16896\n" \
"ds_read_b64_tr_b16 a[44:45], v19 offset:24576\n" \
"ds_read_b64_tr_b16 a[46:47], v19 offset:25088\n" \
"s_waitcnt lgkmcnt(0)\n" \
"s_barrier\n" \
"s_mov_b64 s[44:45], s[72:73]\n" \
"s_and_b32 s45, s45, 0xffff\n" \
"s_mov_b32 s46, 0x500000\n" \
"s_mov_b32 s47, 0x00020000\n" \
"s_add_u32 m0, s62, 98304\n" \
"s_mov_b32 s54, 0x300000\n" \
"buffer_load_dwordx4 v12, s[44:47], s54 offen lds\n" \
"s_add_u32 m0, s62, 102400\n" \
"s_mov_b32 s54, 0x320000\n" \
"buffer_load_dwordx4 v12, s[44:47], s54 offen lds\n" \
"s_add_u32 m0, s62, 106496\n" \
"s_mov_b32 s54, 0x340000\n" \
"buffer_load_dwordx4 v12, s[44:47], s54 offen lds\n" \
"s_add_u32 m0, s62, 110592\n" \
"s_mov_b32 s54, 0x360000\n" \
"buffer_load_dwordx4 v12, s[44:47], s54 offen lds\n" \
"s_add_u32 m0, s62, 114688\n" \
"s_mov_b32 s54, 0x380000\n" \
"buffer_load_dwordx4 v12, s[44:47], s54 offen lds\n" \
"s_add_u32 m0, s62, 118784\n" \
"s_mov_b32 s54, 0x3a0000\n" \
"buffer_load_dwordx4 v12, s[44:47], s54 offen lds\n" \
"s_add_u32 m0, s62, 122880\n" \
"s_mov_b32 s54, 0x3c0000\n" \
"buffer_load_dwordx4 v12, s[44:47], s54 offen lds\n" \
"s_add_u32 m0, s62, 126976\n" \
"s_mov_b32 s54, 0x3e0000\n" \
"buffer_load_dwordx4 v12, s[44:47], s54 offen lds\n" \
"s_waitcnt vmcnt(32)\n" \
"s_barrier\n" \
"v_add_u32 v19, 0x20000, v14\n" \
"ds_read_b64_tr_b16 v[72:73], v19 offset:0\n" \
"ds_read_b64_tr_b16 v[74:75], v19 offset:512\n" \
"ds_read_b64_tr_b16 v[76:77], v19 offset:8192\n" \
"ds_read_b64_tr_b16 v[78:79], v19 offset:8704\n" \
"ds_read_b64_tr_b16 a[16:17], v19 offset:16384\n" \
"ds_read_b64_tr_b16 a[18:19], v19 offset:16896\n" \
"ds_read_b64_tr_b16 a[20:21], v19 offset:24576\n" \
"ds_read_b64_tr_b16 a[22:23], v19 offset:25088\n" \
"v_add_u32 v19, 0x20000, v15\n" \
"ds_read_b64_tr_b16 v[80:81], v19 offset:0\n" \
"ds_read_b64_tr_b16 v[82:83], v19 offset:512\n" \
"ds_read_b64_tr_b16 v[84:85], v19 offset:8192\n" \
"ds_read_b64_tr_b16 v[86:87], v19 offset:8704\n" \
"ds_read_b64_tr_b16 a[24:25], v19 offset:16384\n" \
"ds_read_b64_tr_b16 a[26:27], v19 offset:16896\n" \
"ds_read_b64_tr_b16 a[28:29], v19 offset:24576\n" \
"ds_read_b64_tr_b16 a[30:31], v19 offset:25088\n" \
"s_waitcnt lgkmcnt(0)\n" \
"s_barrier\n" \
"s_mov_b64 s[44:45], s[70:71]\n" \
"s_and_b32 s45, s45, 0xffff\n" \
"s_mov_b32 s46, 0x500000\n" \
"s_mov_b32 s47, 0x00020000\n" \
"s_add_u32 m0, s62, 131072\n" \
"s_mov_b32 s54, 0x300000\n" \
"buffer_load_dwordx4 v12, s[44:47], s54 offen lds\n" \
"s_add_u32 m0, s62, 135168\n" \
"s_mov_b32 s54, 0x320000\n" \
"buffer_load_dwordx4 v12, s[44:47], s54 offen lds\n" \
"s_add_u32 m0, s62, 139264\n" \
"s_mov_b32 s54, 0x340000\n" \
"buffer_load_dwordx4 v12, s[44:47], s54 offen lds\n" \
"s_add_u32 m0, s62, 143360\n" \
"s_mov_b32 s54, 0x360000\n" \
"buffer_load_dwordx4 v12, s[44:47], s54 offen lds\n" \
"s_add_u32 m0, s62, 147456\n" \
"s_mov_b32 s54, 0x380000\n" \
"buffer_load_dwordx4 v12, s[44:47], s54 offen lds\n" \
"s_add_u32 m0, s62, 151552\n" \
"s_mov_b32 s54, 0x3a0000\n" \
"buffer_load_dwordx4 v12, s[44:47], s54 offen lds\n" \
"s_add_u32 m0, s62, 155648\n" \
"s_mov_b32 s54, 0x3c0000\n" \
"buffer_load_dwordx4 v12, s[44:47], s54 offen lds\n" \
"s_add_u32 m0, s62, 159744\n" \
"s_mov_b32 s54, 0x3e0000\n" \
"buffer_load_dwordx4 v12, s[44:47], s54 offen lds\n" \
"s_waitcnt vmcnt(32)\n" \
"s_barrier\n" \
"v_add_u32 v19, 0x0, v14\n" \
"ds_read_b64_tr_b16 a[72:73], v19 offset:0\n" \
"ds_read_b64_tr_b16 a[74:75], v19 offset:512\n" \
"ds_read_b64_tr_b16 a[76:77], v19 offset:8192\n" \
"ds_read_b64_tr_b16 a[78:79], v19 offset:8704\n" \
"ds_read_b64_tr_b16 a[112:113], v19 offset:16384\n" \
"ds_read_b64_tr_b16 a[114:115], v19 offset:16896\n" \
"ds_read_b64_tr_b16 a[116:117], v19 offset:24576\n" \
"ds_read_b64_tr_b16 a[118:119], v19 offset:25088\n" \
"v_add_u32 v19, 0x0, v15\n" \
"ds_read_b64_tr_b16 a[80:81], v19 offset:0\n" \
"ds_read_b64_tr_b16 a[82:83], v19 offset:512\n" \
"ds_read_b64_tr_b16 a[84:85], v19 offset:8192\n" \
"ds_read_b64_tr_b16 a[86:87], v19 offset:8704\n" \
"ds_read_b64_tr_b16 a[120:121], v19 offset:16384\n" \
"ds_read_b64_tr_b16 a[122:123], v19 offset:16896\n" \
"ds_read_b64_tr_b16 a[124:125], v19 offset:24576\n" \
"ds_read_b64_tr_b16 a[126:127], v19 offset:25088\n" \
"s_waitcnt lgkmcnt(0)\n" \
"s_barrier\n" \
"s_mov_b64 s[44:45], s[76:77]\n" \
"s_and_b32 s45, s45, 0xffff\n" \
"s_mov_b32 s46, 0x14000\n" \
"s_mov_b32 s47, 0x00020000\n" \
"s_add_u32 m0, s62, 0\n" \
"s_mov_b32 s54, 0x100\n" \
"buffer_load_dwordx4 v10, s[44:47], s54 offen lds\n" \
"s_add_u32 m0, s62, 4096\n" \
"s_mov_b32 s54, 0x2900\n" \
"buffer_load_dwordx4 v10, s[44:47], s54 offen lds\n" \
"s_add_u32 m0, s62, 8192\n" \
"s_mov_b32 s54, 0x5100\n" \
"buffer_load_dwordx4 v10, s[44:47], s54 offen lds\n" \
"s_add_u32 m0, s62, 12288\n" \
"s_mov_b32 s54, 0x7900\n" \
"buffer_load_dwordx4 v10, s[44:47], s54 offen lds\n" \
"s_add_u32 m0, s62, 16384\n" \
"s_mov_b32 s54, 0xa100\n" \
"buffer_load_dwordx4 v10, s[44:47], s54 offen lds\n" \
"s_add_u32 m0, s62, 20480\n" \
"s_mov_b32 s54, 0xc900\n" \
"buffer_load_dwordx4 v10, s[44:47], s54 offen lds\n" \
"s_add_u32 m0, s62, 24576\n" \
"s_mov_b32 s54, 0xf100\n" \
"buffer_load_dwordx4 v10, s[44:47], s54 offen lds\n" \
"s_add_u32 m0, s62, 28672\n" \
"s_mov_b32 s54, 0x11900\n" \
"buffer_load_dwordx4 v10, s[44:47], s54 offen lds\n" \
"s_waitcnt vmcnt(32)\n" \
"s_barrier\n" \
"v_add_u32 v19, 0x8000, v14\n" \
"ds_read_b64_tr_b16 a[56:57], v19 offset:0\n" \
"ds_read_b64_tr_b16 a[58:59], v19 offset:512\n" \
"ds_read_b64_tr_b16 a[60:61], v19 offset:8192\n" \
"ds_read_b64_tr_b16 a[62:63], v19 offset:8704\n" \
"ds_read_b64_tr_b16 a[96:97], v19 offset:16384\n" \
"ds_read_b64_tr_b16 a[98:99], v19 offset:16896\n" \
"ds_read_b64_tr_b16 a[100:101], v19 offset:24576\n" \
"ds_read_b64_tr_b16 a[102:103], v19 offset:25088\n" \
"v_add_u32 v19, 0x8000, v15\n" \
"ds_read_b64_tr_b16 a[64:65], v19 offset:0\n" \
"ds_read_b64_tr_b16 a[66:67], v19 offset:512\n" \
"ds_read_b64_tr_b16 a[68:69], v19 offset:8192\n" \
"ds_read_b64_tr_b16 a[70:71], v19 offset:8704\n" \
"ds_read_b64_tr_b16 a[104:105], v19 offset:16384\n" \
"ds_read_b64_tr_b16 a[106:107], v19 offset:16896\n" \
"ds_read_b64_tr_b16 a[108:109], v19 offset:24576\n" \
"ds_read_b64_tr_b16 a[110:111], v19 offset:25088\n" \
"s_waitcnt lgkmcnt(0)\n" \
"s_barrier\n" \
"s_add_u32 m0, s62, 32768\n" \
"s_mov_b32 s54, 0x300\n" \
"buffer_load_dwordx4 v10, s[44:47], s54 offen lds\n" \
"s_add_u32 m0, s62, 36864\n" \
"s_mov_b32 s54, 0x2b00\n" \
"buffer_load_dwordx4 v10, s[44:47], s54 offen lds\n" \
"s_add_u32 m0, s62, 40960\n" \
"s_mov_b32 s54, 0x5300\n" \
"buffer_load_dwordx4 v10, s[44:47], s54 offen lds\n" \
"s_add_u32 m0, s62, 45056\n" \
"s_mov_b32 s54, 0x7b00\n" \
"buffer_load_dwordx4 v10, s[44:47], s54 offen lds\n" \
"s_add_u32 m0, s62, 49152\n" \
"s_mov_b32 s54, 0xa300\n" \
"buffer_load_dwordx4 v10, s[44:47], s54 offen lds\n" \
"s_add_u32 m0, s62, 53248\n" \
"s_mov_b32 s54, 0xcb00\n" \
"buffer_load_dwordx4 v10, s[44:47], s54 offen lds\n" \
"s_add_u32 m0, s62, 57344\n" \
"s_mov_b32 s54, 0xf300\n" \
"buffer_load_dwordx4 v10, s[44:47], s54 offen lds\n" \
"s_add_u32 m0, s62, 61440\n" \
"s_mov_b32 s54, 0x11b00\n" \
"buffer_load_dwordx4 v10, s[44:47], s54 offen lds\n" \
"s_waitcnt vmcnt(32)\n" \
"s_barrier\n" \
"v_add_u32 v19, 0x10000, v16\n" \
"ds_read_b64_tr_b16 a[88:89], v19 offset:0\n" \
"ds_read_b64_tr_b16 a[90:91], v19 offset:256\n" \
"ds_read_b64_tr_b16 a[92:93], v19 offset:4096\n" \
"ds_read_b64_tr_b16 a[94:95], v19 offset:4352\n" \
"ds_read_b64_tr_b16 a[128:129], v19 offset:8192\n" \
"ds_read_b64_tr_b16 a[130:131], v19 offset:8448\n" \
"ds_read_b64_tr_b16 a[132:133], v19 offset:12288\n" \
"ds_read_b64_tr_b16 a[134:135], v19 offset:12544\n" \
"ds_read_b64_tr_b16 a[168:169], v19 offset:16384\n" \
"ds_read_b64_tr_b16 a[170:171], v19 offset:16640\n" \
"ds_read_b64_tr_b16 a[172:173], v19 offset:20480\n" \
"ds_read_b64_tr_b16 a[174:175], v19 offset:20736\n" \
"ds_read_b64_tr_b16 a[208:209], v19 offset:24576\n" \
"ds_read_b64_tr_b16 a[210:211], v19 offset:24832\n" \
"ds_read_b64_tr_b16 a[212:213], v19 offset:28672\n" \
"ds_read_b64_tr_b16 a[214:215], v19 offset:28928\n" \
"s_waitcnt lgkmcnt(0)\n" \
"s_barrier\n" \
"s_add_u32 m0, s62, 65536\n" \
"s_mov_b32 s54, 0x0\n" \
"buffer_load_dwordx4 v11, s[44:47], s54 offen lds\n" \
"s_add_u32 m0, s62, 69632\n" \
"s_mov_b32 s54, 0x5000\n" \
"buffer_load_dwordx4 v11, s[44:47], s54 offen lds\n" \
"s_add_u32 m0, s62, 73728\n" \
"s_mov_b32 s54, 0xa000\n" \
"buffer_load_dwordx4 v11, s[44:47], s54 offen lds\n" \
"s_add_u32 m0, s62, 77824\n" \
"s_mov_b32 s54, 0xf000\n" \
"buffer_load_dwordx4 v11, s[44:47], s54 offen lds\n" \
"s_waitcnt vmcnt(28)\n" \
"s_barrier\n" \
"v_add_u32 v19, 0x18000, v14\n" \
"ds_read_b64_tr_b16 a[152:153], v19 offset:0\n" \
"ds_read_b64_tr_b16 a[154:155], v19 offset:512\n" \
"ds_read_b64_tr_b16 a[156:157], v19 offset:8192\n" \
"ds_read_b64_tr_b16 a[158:159], v19 offset:8704\n" \
"ds_read_b64_tr_b16 a[192:193], v19 offset:16384\n" \
"ds_read_b64_tr_b16 a[194:195], v19 offset:16896\n" \
"ds_read_b64_tr_b16 a[196:197], v19 offset:24576\n" \
"ds_read_b64_tr_b16 a[198:199], v19 offset:25088\n" \
"v_add_u32 v19, 0x18000, v15\n" \
"ds_read_b64_tr_b16 a[160:161], v19 offset:0\n" \
"ds_read_b64_tr_b16 a[162:163], v19 offset:512\n" \
"ds_read_b64_tr_b16 a[164:165], v19 offset:8192\n" \
"ds_read_b64_tr_b16 a[166:167], v19 offset:8704\n" \
"ds_read_b64_tr_b16 a[200:201], v19 offset:16384\n" \
"ds_read_b64_tr_b16 a[202:203], v19 offset:16896\n" \
"ds_read_b64_tr_b16 a[204:205], v19 offset:24576\n" \
"ds_read_b64_tr_b16 a[206:207], v19 offset:25088\n" \
"s_waitcnt lgkmcnt(0)\n" \
"s_barrier\n" \
"s_mov_b64 s[44:45], s[72:73]\n" \
"s_and_b32 s45, s45, 0xffff\n" \
"s_mov_b32 s46, 0x500000\n" \
"s_mov_b32 s47, 0x00020000\n" \
"s_add_u32 m0, s62, 98304\n" \
"s_mov_b32 s54, 0x400000\n" \
"buffer_load_dwordx4 v12, s[44:47], s54 offen lds\n" \
"s_add_u32 m0, s62, 102400\n" \
"s_mov_b32 s54, 0x420000\n" \
"buffer_load_dwordx4 v12, s[44:47], s54 offen lds\n" \
"s_add_u32 m0, s62, 106496\n" \
"s_mov_b32 s54, 0x440000\n" \
"buffer_load_dwordx4 v12, s[44:47], s54 offen lds\n" \
"s_add_u32 m0, s62, 110592\n" \
"s_mov_b32 s54, 0x460000\n" \
"buffer_load_dwordx4 v12, s[44:47], s54 offen lds\n" \
"s_add_u32 m0, s62, 114688\n" \
"s_mov_b32 s54, 0x480000\n" \
"buffer_load_dwordx4 v12, s[44:47], s54 offen lds\n" \
"s_add_u32 m0, s62, 118784\n" \
"s_mov_b32 s54, 0x4a0000\n" \
"buffer_load_dwordx4 v12, s[44:47], s54 offen lds\n" \
"s_add_u32 m0, s62, 122880\n" \
"s_mov_b32 s54, 0x4c0000\n" \
"buffer_load_dwordx4 v12, s[44:47], s54 offen lds\n" \
"s_add_u32 m0, s62, 126976\n" \
"s_mov_b32 s54, 0x4e0000\n" \
"buffer_load_dwordx4 v12, s[44:47], s54 offen lds\n" \
"s_waitcnt vmcnt(28)\n" \
"s_barrier\n" \
"v_add_u32 v19, 0x20000, v14\n" \
"ds_read_b64_tr_b16 a[136:137], v19 offset:0\n" \
"ds_read_b64_tr_b16 a[138:139], v19 offset:512\n" \
"ds_read_b64_tr_b16 a[140:141], v19 offset:8192\n" \
"ds_read_b64_tr_b16 a[142:143], v19 offset:8704\n" \
"ds_read_b64_tr_b16 a[176:177], v19 offset:16384\n" \
"ds_read_b64_tr_b16 a[178:179], v19 offset:16896\n" \
"ds_read_b64_tr_b16 a[180:181], v19 offset:24576\n" \
"ds_read_b64_tr_b16 a[182:183], v19 offset:25088\n" \
"v_add_u32 v19, 0x20000, v15\n" \
"ds_read_b64_tr_b16 a[144:145], v19 offset:0\n" \
"ds_read_b64_tr_b16 a[146:147], v19 offset:512\n" \
"ds_read_b64_tr_b16 a[148:149], v19 offset:8192\n" \
"ds_read_b64_tr_b16 a[150:151], v19 offset:8704\n" \
"ds_read_b64_tr_b16 a[184:185], v19 offset:16384\n" \
"ds_read_b64_tr_b16 a[186:187], v19 offset:16896\n" \
"ds_read_b64_tr_b16 a[188:189], v19 offset:24576\n" \
"ds_read_b64_tr_b16 a[190:191], v19 offset:25088\n" \
"s_waitcnt lgkmcnt(0)\n" \
"s_barrier\n" \
"s_mov_b64 s[44:45], s[70:71]\n" \
"s_and_b32 s45, s45, 0xffff\n" \
"s_mov_b32 s46, 0x500000\n" \
"s_mov_b32 s47, 0x00020000\n" \
"s_add_u32 m0, s62, 131072\n" \
"s_mov_b32 s54, 0x400000\n" \
"buffer_load_dwordx4 v12, s[44:47], s54 offen lds\n" \
"s_add_u32 m0, s62, 135168\n" \
"s_mov_b32 s54, 0x420000\n" \
"buffer_load_dwordx4 v12, s[44:47], s54 offen lds\n" \
"s_add_u32 m0, s62, 139264\n" \
"s_mov_b32 s54, 0x440000\n" \
"buffer_load_dwordx4 v12, s[44:47], s54 offen lds\n" \
"s_add_u32 m0, s62, 143360\n" \
"s_mov_b32 s54, 0x460000\n" \
"buffer_load_dwordx4 v12, s[44:47], s54 offen lds\n" \
"s_add_u32 m0, s62, 147456\n" \
"s_mov_b32 s54, 0x480000\n" \
"buffer_load_dwordx4 v12, s[44:47], s54 offen lds\n" \
"s_add_u32 m0, s62, 151552\n" \
"s_mov_b32 s54, 0x4a0000\n" \
"buffer_load_dwordx4 v12, s[44:47], s54 offen lds\n" \
"s_add_u32 m0, s62, 155648\n" \
"s_mov_b32 s54, 0x4c0000\n" \
"buffer_load_dwordx4 v12, s[44:47], s54 offen lds\n" \
"s_add_u32 m0, s62, 159744\n" \
"s_mov_b32 s54, 0x4e0000\n" \
"buffer_load_dwordx4 v12, s[44:47], s54 offen lds\n" \
"s_waitcnt vmcnt(28)\n" \
"s_barrier\n" \
"v_add_u32 v19, 0x0, v28\n" \
"ds_read_b32 v232, v19 offset:0\n" \
"ds_read_b32 v233, v19 offset:256\n" \
"ds_read_b32 v234, v19 offset:512\n" \
"ds_read_b32 v235, v19 offset:768\n" \
"ds_read_b32 v236, v19 offset:16384\n" \
"ds_read_b32 v237, v19 offset:16640\n" \
"ds_read_b32 v238, v19 offset:16896\n" \
"ds_read_b32 v239, v19 offset:17152\n" \
"ds_read_b32 v240, v19 offset:1024\n" \
"ds_read_b32 v241, v19 offset:1280\n" \
"ds_read_b32 v242, v19 offset:1536\n" \
"ds_read_b32 v243, v19 offset:1792\n" \
"ds_read_b32 v244, v19 offset:17408\n" \
"ds_read_b32 v245, v19 offset:17664\n" \
"ds_read_b32 v246, v19 offset:17920\n" \
"ds_read_b32 v247, v19 offset:18176\n" \
"ds_read_b32 v248, v19 offset:4096\n" \
"ds_read_b32 v249, v19 offset:4352\n" \
"ds_read_b32 v250, v19 offset:4608\n" \
"ds_read_b32 v251, v19 offset:4864\n" \
"ds_read_b32 v252, v19 offset:20480\n" \
"ds_read_b32 v253, v19 offset:20736\n" \
"ds_read_b32 v254, v19 offset:20992\n" \
"ds_read_b32 v255, v19 offset:21248\n" \
"ds_read_b32 v20, v19 offset:5120\n" \
"ds_read_b32 v21, v19 offset:5376\n" \
"ds_read_b32 v22, v19 offset:5632\n" \
"ds_read_b32 v23, v19 offset:5888\n" \
"ds_read_b32 v24, v19 offset:21504\n" \
"ds_read_b32 v25, v19 offset:21760\n" \
"ds_read_b32 v26, v19 offset:22016\n" \
"ds_read_b32 v27, v19 offset:22272\n" \
"s_waitcnt lgkmcnt(0)\n" \
"s_barrier\n" \
"s_mov_b64 s[44:45], s[74:75]\n" \
"s_and_b32 s45, s45, 0xffff\n" \
"s_mov_b32 s46, 0x500000\n" \
"s_mov_b32 s47, 0x00020000\n" \
"s_add_u32 m0, s62, 0\n" \
"s_mov_b32 s54, 0x400000\n" \
"buffer_load_dwordx4 v13, s[44:47], s54 offen lds\n" \
"s_add_u32 m0, s62, 4096\n" \
"s_mov_b32 s54, 0x440000\n" \
"buffer_load_dwordx4 v13, s[44:47], s54 offen lds\n" \
"s_add_u32 m0, s62, 8192\n" \
"s_mov_b32 s54, 0x480000\n" \
"buffer_load_dwordx4 v13, s[44:47], s54 offen lds\n" \
"s_add_u32 m0, s62, 12288\n" \
"s_mov_b32 s54, 0x4c0000\n" \
"buffer_load_dwordx4 v13, s[44:47], s54 offen lds\n" \
"s_mov_b32 s54, 0x05040100\n" \
"s_mov_b32 s55, 0x07060302\n" \
"v_perm_b32 v152, v234, v232, s54\n" \
"v_perm_b32 v160, v234, v232, s55\n" \
"v_perm_b32 v168, v235, v233, s54\n" \
"v_perm_b32 v176, v235, v233, s55\n" \
"v_perm_b32 v153, v238, v236, s54\n" \
"v_perm_b32 v161, v238, v236, s55\n" \
"v_perm_b32 v169, v239, v237, s54\n" \
"v_perm_b32 v177, v239, v237, s55\n" \
"v_perm_b32 v154, v242, v240, s54\n" \
"v_perm_b32 v162, v242, v240, s55\n" \
"v_perm_b32 v170, v243, v241, s54\n" \
"v_perm_b32 v178, v243, v241, s55\n" \
"v_perm_b32 v155, v246, v244, s54\n" \
"v_perm_b32 v163, v246, v244, s55\n" \
"v_perm_b32 v171, v247, v245, s54\n" \
"v_perm_b32 v179, v247, v245, s55\n" \
"v_perm_b32 v156, v250, v248, s54\n" \
"v_perm_b32 v164, v250, v248, s55\n" \
"v_perm_b32 v172, v251, v249, s54\n" \
"v_perm_b32 v180, v251, v249, s55\n" \
"v_perm_b32 v157, v254, v252, s54\n" \
"v_perm_b32 v165, v254, v252, s55\n" \
"v_perm_b32 v173, v255, v253, s54\n" \
"v_perm_b32 v181, v255, v253, s55\n" \
"v_perm_b32 v158, v22, v20, s54\n" \
"v_perm_b32 v166, v22, v20, s55\n" \
"v_perm_b32 v174, v23, v21, s54\n" \
"v_perm_b32 v182, v23, v21, s55\n" \
"v_perm_b32 v159, v26, v24, s54\n" \
"v_perm_b32 v167, v26, v24, s55\n" \
"v_perm_b32 v175, v27, v25, s54\n" \
"v_perm_b32 v183, v27, v25, s55\n" \
"s_waitcnt vmcnt(24)\n" \
"s_barrier\n" \
"v_add_u32 v19, 0x8000, v17\n" \
"ds_read_b64 v[232:233], v19 offset:0\n" \
"ds_read_b64 v[234:235], v19 offset:512\n" \
"ds_read_b64 v[236:237], v19 offset:16384\n" \
"ds_read_b64 v[238:239], v19 offset:16896\n" \
"ds_read_b64 v[240:241], v19 offset:1024\n" \
"ds_read_b64 v[242:243], v19 offset:1536\n" \
"ds_read_b64 v[244:245], v19 offset:17408\n" \
"ds_read_b64 v[246:247], v19 offset:17920\n" \
"ds_read_b64 v[248:249], v19 offset:4096\n" \
"ds_read_b64 v[250:251], v19 offset:4608\n" \
"ds_read_b64 v[252:253], v19 offset:20480\n" \
"ds_read_b64 v[254:255], v19 offset:20992\n" \
"ds_read_b64 v[20:21], v19 offset:5120\n" \
"ds_read_b64 v[22:23], v19 offset:5632\n" \
"ds_read_b64 v[24:25], v19 offset:21504\n" \
"ds_read_b64 v[26:27], v19 offset:22016\n" \
"s_waitcnt lgkmcnt(0)\n" \
"s_barrier\n" \
"s_mov_b32 s54, 0x05040100\n" \
"s_mov_b32 s55, 0x07060302\n" \
"v_perm_b32 v184, v234, v232, s54\n" \
"v_perm_b32 v192, v234, v232, s55\n" \
"v_perm_b32 v200, v235, v233, s54\n" \
"v_perm_b32 v208, v235, v233, s55\n" \
"v_perm_b32 v185, v238, v236, s54\n" \
"v_perm_b32 v193, v238, v236, s55\n" \
"v_perm_b32 v201, v239, v237, s54\n" \
"v_perm_b32 v209, v239, v237, s55\n" \
"v_perm_b32 v186, v242, v240, s54\n" \
"v_perm_b32 v194, v242, v240, s55\n" \
"v_perm_b32 v202, v243, v241, s54\n" \
"v_perm_b32 v210, v243, v241, s55\n" \
"v_perm_b32 v187, v246, v244, s54\n" \
"v_perm_b32 v195, v246, v244, s55\n" \
"v_perm_b32 v203, v247, v245, s54\n" \
"v_perm_b32 v211, v247, v245, s55\n" \
"v_perm_b32 v188, v250, v248, s54\n" \
"v_perm_b32 v196, v250, v248, s55\n" \
"v_perm_b32 v204, v251, v249, s54\n" \
"v_perm_b32 v212, v251, v249, s55\n" \
"v_perm_b32 v189, v254, v252, s54\n" \
"v_perm_b32 v197, v254, v252, s55\n" \
"v_perm_b32 v205, v255, v253, s54\n" \
"v_perm_b32 v213, v255, v253, s55\n" \
"v_perm_b32 v190, v22, v20, s54\n" \
"v_perm_b32 v198, v22, v20, s55\n" \
"v_perm_b32 v206, v23, v21, s54\n" \
"v_perm_b32 v214, v23, v21, s55\n" \
"v_perm_b32 v191, v26, v24, s54\n" \
"v_perm_b32 v199, v26, v24, s55\n" \
"v_perm_b32 v207, v27, v25, s54\n" \
"v_perm_b32 v215, v27, v25, s55\n" \
"s_waitcnt vmcnt(20)\n" \
"s_barrier\n" \
"v_add_u32 v19, 0x10000, v18\n" \
"ds_read_b32 v232, v19 offset:0\n" \
"ds_read_b32 v233, v19 offset:256\n" \
"ds_read_b32 v234, v19 offset:8192\n" \
"ds_read_b32 v235, v19 offset:8448\n" \
"ds_read_b32 v236, v19 offset:512\n" \
"ds_read_b32 v237, v19 offset:768\n" \
"ds_read_b32 v238, v19 offset:8704\n" \
"ds_read_b32 v239, v19 offset:8960\n" \
"ds_read_b32 v240, v19 offset:2048\n" \
"ds_read_b32 v241, v19 offset:2304\n" \
"ds_read_b32 v242, v19 offset:10240\n" \
"ds_read_b32 v243, v19 offset:10496\n" \
"ds_read_b32 v244, v19 offset:2560\n" \
"ds_read_b32 v245, v19 offset:2816\n" \
"ds_read_b32 v246, v19 offset:10752\n" \
"ds_read_b32 v247, v19 offset:11008\n" \
"s_waitcnt lgkmcnt(0)\n" \
"s_barrier\n" \
"s_mov_b32 s54, 0x05040100\n" \
"s_mov_b32 s55, 0x07060302\n" \
"v_perm_b32 v216, v233, v232, s54\n" \
"v_perm_b32 v224, v233, v232, s55\n" \
"v_perm_b32 v217, v235, v234, s54\n" \
"v_perm_b32 v225, v235, v234, s55\n" \
"v_perm_b32 v218, v237, v236, s54\n" \
"v_perm_b32 v226, v237, v236, s55\n" \
"v_perm_b32 v219, v239, v238, s54\n" \
"v_perm_b32 v227, v239, v238, s55\n" \
"v_perm_b32 v220, v241, v240, s54\n" \
"v_perm_b32 v228, v241, v240, s55\n" \
"v_perm_b32 v221, v243, v242, s54\n" \
"v_perm_b32 v229, v243, v242, s55\n" \
"v_perm_b32 v222, v245, v244, s54\n" \
"v_perm_b32 v230, v245, v244, s55\n" \
"v_perm_b32 v223, v247, v246, s54\n" \
"v_perm_b32 v231, v247, v246, s55\n" \
"s_waitcnt vmcnt(12)\n" \
"s_barrier\n" \
"v_add_u32 v19, 0x18000, v14\n" \
"ds_read_b64_tr_b16 a[224:225], v19 offset:0\n" \
"ds_read_b64_tr_b16 a[226:227], v19 offset:512\n" \
"ds_read_b64_tr_b16 a[228:229], v19 offset:8192\n" \
"ds_read_b64_tr_b16 a[230:231], v19 offset:8704\n" \
"ds_read_b64_tr_b16 v[240:241], v19 offset:16384\n" \
"ds_read_b64_tr_b16 v[242:243], v19 offset:16896\n" \
"ds_read_b64_tr_b16 v[244:245], v19 offset:24576\n" \
"ds_read_b64_tr_b16 v[246:247], v19 offset:25088\n" \
"v_add_u32 v19, 0x18000, v15\n" \
"ds_read_b64_tr_b16 a[232:233], v19 offset:0\n" \
"ds_read_b64_tr_b16 a[234:235], v19 offset:512\n" \
"ds_read_b64_tr_b16 a[236:237], v19 offset:8192\n" \
"ds_read_b64_tr_b16 a[238:239], v19 offset:8704\n" \
"ds_read_b64_tr_b16 v[248:249], v19 offset:16384\n" \
"ds_read_b64_tr_b16 v[250:251], v19 offset:16896\n" \
"ds_read_b64_tr_b16 v[252:253], v19 offset:24576\n" \
"ds_read_b64_tr_b16 v[254:255], v19 offset:25088\n" \
"s_waitcnt lgkmcnt(0)\n" \
"s_barrier\n" \
"s_waitcnt vmcnt(4)\n" \
"s_barrier\n" \
"v_add_u32 v19, 0x20000, v14\n" \
"ds_read_b64_tr_b16 a[216:217], v19 offset:0\n" \
"ds_read_b64_tr_b16 a[218:219], v19 offset:512\n" \
"ds_read_b64_tr_b16 a[220:221], v19 offset:8192\n" \
"ds_read_b64_tr_b16 a[222:223], v19 offset:8704\n" \
"ds_read_b64_tr_b16 a[248:249], v19 offset:16384\n" \
"ds_read_b64_tr_b16 a[250:251], v19 offset:16896\n" \
"ds_read_b64_tr_b16 a[252:253], v19 offset:24576\n" \
"ds_read_b64_tr_b16 a[254:255], v19 offset:25088\n" \
"v_add_u32 v19, 0x20000, v15\n" \
"ds_read_b64_tr_b16 v[32:33], v19 offset:0\n" \
"ds_read_b64_tr_b16 v[34:35], v19 offset:512\n" \
"ds_read_b64_tr_b16 v[36:37], v19 offset:8192\n" \
"ds_read_b64_tr_b16 v[38:39], v19 offset:8704\n" \
"ds_read_b64_tr_b16 v[232:233], v19 offset:16384\n" \
"ds_read_b64_tr_b16 v[234:235], v19 offset:16896\n" \
"ds_read_b64_tr_b16 v[236:237], v19 offset:24576\n" \
"ds_read_b64_tr_b16 v[238:239], v19 offset:25088\n" \
"s_waitcnt lgkmcnt(0)\n" \
"s_barrier\n" \
"s_waitcnt vmcnt(0)\n" \
"s_barrier\n" \
"v_add_u32 v19, 0x0, v16\n" \
"ds_read_b64_tr_b16 a[240:241], v19 offset:0\n" \
"ds_read_b64_tr_b16 a[242:243], v19 offset:256\n" \
"ds_read_b64_tr_b16 a[244:245], v19 offset:4096\n" \
"ds_read_b64_tr_b16 a[246:247], v19 offset:4352\n" \
"ds_read_b64_tr_b16 v[20:21], v19 offset:8192\n" \
"ds_read_b64_tr_b16 v[22:23], v19 offset:8448\n" \
"ds_read_b64_tr_b16 v[24:25], v19 offset:12288\n" \
"ds_read_b64_tr_b16 v[26:27], v19 offset:12544\n" \
"s_waitcnt lgkmcnt(0)\n" \
"s_barrier\n" \
"v_mov_b64 v[10:11], 0\n" \
"ds_write_b128 v9, v[88:91] offset:0\n" \
"ds_write_b128 v9, v[92:95] offset:1024\n" \
"ds_write_b128 v9, v[96:99] offset:2048\n" \
"ds_write_b128 v9, v[100:103] offset:3072\n" \
"ds_write_b128 v9, v[56:59] offset:4096\n" \
"ds_write_b128 v9, v[60:63] offset:5120\n" \
"ds_write_b128 v9, v[64:67] offset:6144\n" \
"ds_write_b128 v9, v[68:71] offset:7168\n" \
"ds_write_b128 v9, v[104:107] offset:8192\n" \
"ds_write_b128 v9, v[108:111] offset:9216\n" \
"ds_write_b128 v9, v[112:115] offset:10240\n" \
"ds_write_b128 v9, v[116:119] offset:11264\n" \
"ds_write_b128 v9, v[120:123] offset:12288\n" \
"ds_write_b128 v9, v[124:127] offset:13312\n" \
"ds_write_b128 v9, v[128:131] offset:14336\n" \
"ds_write_b128 v9, v[132:135] offset:15360\n" \
"ds_write_b128 v9, v[136:139] offset:16384\n" \
"ds_write_b128 v9, v[140:143] offset:17408\n" \
"ds_write_b128 v9, v[144:147] offset:18432\n" \
"ds_write_b128 v9, v[148:151] offset:19456\n" \
"ds_write_b128 v9, v[72:75] offset:20480\n" \
"ds_write_b128 v9, v[76:79] offset:21504\n" \
"ds_write_b128 v9, v[80:83] offset:22528\n" \
"ds_write_b128 v9, v[84:87] offset:23552\n" \
"ds_write_b128 v9, v[40:43] offset:24576\n" \
"ds_write_b128 v9, v[44:47] offset:25600\n" \
"ds_write_b128 v9, v[32:35] offset:26624\n" \
"ds_write_b128 v9, v[36:39] offset:27648\n" \
"s_waitcnt lgkmcnt(0)\n" \
"s_mov_b32 s44, 0x80000\n" \
"s_add_u32 s45, s62, 17408\n" \
"s_lshr_b32 s26, s62, 2\n" \
"s_add_u32 s26, s26, 41984\n" \
"s_mov_b32 s27, 0\n" \
"s_cmp_eq_u32 %[lead], 0\n" \
"s_cselect_b32 s20, 0x7ff00000, 0\n" \
"s_add_u32 s20, s20, 268\n" \
"s_mov_b32 s22, 0\n" \
"v_mov_b32 v12, v40\n" \
"v_mov_b32 v13, v41\n" \
"v_mov_b32 v14, v42\n" \
"v_mov_b32 v15, v43\n" \
"v_mov_b32 v16, v44\n" \
"v_mov_b32 v17, v45\n" \
"v_mov_b32 v18, v46\n" \
"v_mov_b32 v19, v47\n" \
"v_mov_b64 v[28:29], 0\n" \
"v_mov_b64 v[30:31], 0\n" \
"v_mov_b64 v[32:33], 0\n" \
"v_mov_b64 v[34:35], 0\n" \
"v_mov_b64 v[36:37], 0\n" \
"v_mov_b64 v[38:39], 0\n" \
"v_mov_b64 v[40:41], 0\n" \
"v_mov_b64 v[42:43], 0\n" \
"v_mov_b64 v[44:45], 0\n" \
"v_mov_b64 v[46:47], 0\n" \
"s_mov_b32 s25, 6000\n" \
"s_mov_b32 s19, 0x7ff00000\n" \
"v_mov_b32 v54, 0\n" \
"s_add_u32 s55, s48, 1\n" \
"s_min_u32 s55, s55, 64\n" \
"v_readfirstlane_b32 s54, v54\n" \
"s_cmp_ge_u32 s54, s55\n" \
"s_cbranch_scc1 XK0D_%=\n" \
"XK0_%=:\n" \
"buffer_load_dword v54, off, s[40:43], %[fkv] sc1\n" \
"s_waitcnt vmcnt(0)\n" \
"v_readfirstlane_b32 s54, v54\n" \
"s_cmp_ge_u32 s54, s55\n" \
"s_cbranch_scc1 XK0R_%=\n" \
"s_sleep 1\n" \
"s_sub_u32 s44, s44, 1\n" \
"s_cbranch_scc0 XK0_%=\n" \
"s_mov_b32 s44, 0\n" \
"XK0R_%=:\n" \
"s_add_u32 m0, s45, 0\n" \
"s_nop 0\n" \
"buffer_load_dwordx4 v1, s[40:43], s67 offen sc1 lds\n" \
"s_add_u32 m0, s45, 4096\n" \
"s_nop 0\n" \
"buffer_load_dwordx4 v1, s[40:43], s68 offen sc1 lds\n" \
"s_add_u32 m0, s45, 8192\n" \
"s_nop 0\n" \
"buffer_load_dwordx4 v1, s[40:43], s69 offen sc1 lds\n" \
"XK0D_%=:\n" \
"s_bitcmp1_b64 %[dpar], s48\n" \
"s_cselect_b32 s54, 0x2000, 0\n" \
"s_add_u32 s67, s67, 0x200000\n" \
"s_xor_b32 s67, s67, s54\n" \
"s_add_u32 s68, s68, 0x200000\n" \
"s_xor_b32 s68, s68, s54\n" \
"s_add_u32 s69, s69, 0x200000\n" \
"s_xor_b32 s69, s69, s54\n" \
"s_sub_u32 s54, s45, s62\n" \
"v_add_u32 v5, s54, v1\n" \
"s_xor_b32 s54, s54, 12288\n" \
"s_add_u32 s45, s54, s62\n" \
"s_add_u32 s23, s23, 1\n" \
"s_cmp_eq_u32 s23, 4\n" \
"s_cselect_b32 s23, 2, s23\n" \
"s_bitcmp1_b32 %[par], s23\n" \
"s_cselect_b32 s53, 0x2000, 0\n" \
"s_lshl_b32 s54, s23, 21\n" \
"s_add_u32 s53, s53, s54\n" \
"s_branch XS2_%=\n" \
".p2align 6\n" \
"s_nop 1\n" \
"s_nop 1\n" \
"s_nop 1\n" \
"s_nop 1\n" \
"s_nop 1\n" \
"s_nop 1\n" \
"s_nop 1\n" \
"s_nop 1\n" \
"s_nop 1\n" \
"s_nop 1\n" \
"s_nop 1\n" \
"s_nop 1\n" \
"s_nop 1\n" \
"s_nop 1\n" \
"s_nop 1\n" \
"s_nop 1\n" \
"s_nop 1\n" \
"s_nop 1\n" \
"s_nop 1\n" \
"s_nop 1\n" \
"s_nop 1\n" \
"s_nop 1\n" \
"s_nop 1\n" \
"s_nop 1\n" \
"s_nop 1\n" \
"s_nop 1\n" \
"s_nop 1\n" \
"s_nop 1\n" \
"s_nop 1\n" \
"s_nop 1\n" \
"s_nop 1\n" \
"s_nop 1\n" \
"s_nop 1\n" \
"s_nop 1\n" \
"s_nop 1\n" \
"s_nop 1\n" \
"s_nop 1\n" \
"s_nop 1\n" \
"s_nop 1\n" \
"s_nop 1\n" \
"s_nop 1\n" \
"s_nop 1\n" \
"s_nop 1\n" \
"s_nop 1\n" \
"s_nop 1\n" \
"s_nop 1\n" \
"s_nop 1\n" \
"s_nop 1\n" \
"s_nop 1\n" \
"s_nop 1\n" \
"s_nop 1\n" \
"s_nop 1\n" \
"s_nop 1\n" \
"s_nop 1\n" \
"s_nop 1\n" \
"s_nop 1\n" \
"s_nop 1\n" \
"s_nop 1\n" \
"s_nop 1\n" \
"s_nop 1\n" \
"s_nop 1\n" \
"s_nop 1\n" \
"s_nop 1\n" \
"s_nop 1\n" \
"XS2_%=:\n" \
"s_mov_b32 m0, s63\n" \
"s_mov_b64 exec, s[58:59]\n" \
"buffer_load_dwordx4 v2, s[36:39], s49 offen sc1 lds\n" \
"s_setpc_b64 s[28:29]\n" \
"s_nop 1\n" \
"s_nop 1\n" \
"s_nop 1\n" \
"s_nop 1\n" \
"s_nop 1\n" \
"s_nop 1\n" \
"s_nop 1\n" \
"s_nop 1\n" \
"s_nop 1\n" \
"s_nop 1\n" \
"s_nop 1\n" \
"s_nop 1\n" \
"s_nop 1\n" \
"s_nop 1\n" \
"s_nop 1\n" \
"s_nop 1\n" \
"s_nop 1\n" \
"s_nop 1\n" \
"s_nop 1\n" \
"s_nop 1\n" \
"s_nop 1\n" \
"s_nop 1\n" \
"s_nop 1\n" \
"s_nop 1\n" \
"s_nop 1\n" \
"s_nop 1\n" \
"s_nop 1\n" \
"s_nop 1\n" \
"s_nop 1\n" \
"s_nop 1\n" \
"s_nop 1\n" \
"s_nop 1\n" \
"s_nop 1\n" \
"s_nop 1\n" \
"s_nop 1\n" \
"s_nop 1\n" \
"s_nop 1\n" \
"s_nop 1\n" \
"s_nop 1\n" \
"s_nop 1\n" \
"XSB_%=:\n" \
"buffer_load_dwordx4 v2, s[36:39], s49 offen offset:2048 sc1 lds\n" \
"s_add_u32 m0, s63, 4096\n" \
"s_add_u32 s54, s49, 4096\n" \
"s_setpc_b64 s[16:17]\n" \
"s_nop 1\n" \
"s_nop 1\n" \
"s_nop 1\n" \
"s_nop 1\n" \
"s_nop 1\n" \
"s_nop 1\n" \
"s_nop 1\n" \
"s_nop 1\n" \
"s_nop 1\n" \
"s_nop 1\n" \
"s_nop 1\n" \
"s_nop 1\n" \
"s_nop 1\n" \
"s_nop 1\n" \
"s_nop 1\n" \
"s_nop 1\n" \
"s_nop 1\n" \
"s_nop 1\n" \
"s_nop 1\n" \
"s_nop 1\n" \
"s_nop 1\n" \
"s_nop 1\n" \
"s_nop 1\n" \
"s_nop 1\n" \
"s_nop 1\n" \
"s_nop 1\n" \
"s_nop 1\n" \
"s_nop 1\n" \
"s_nop 1\n" \
"s_nop 1\n" \
"s_nop 1\n" \
"s_nop 1\n" \
"s_nop 1\n" \
"s_nop 1\n" \
"s_nop 1\n" \
"s_nop 1\n" \
"s_nop 1\n" \
"s_nop 1\n" \
"s_nop 1\n" \
"s_nop 1\n" \
"XSD_%=:\n" \
"buffer_load_dwordx4 v2, s[36:39], s54 offen sc1 lds\n" \
"s_mov_b64 exec, -1\n" \
"s_add_u32 m0, s62, 6144\n" \
"s_setpc_b64 s[30:31]\n" \
"s_nop 1\n" \
"s_nop 1\n" \
"s_nop 1\n" \
"s_nop 1\n" \
"s_nop 1\n" \
"s_nop 1\n" \
"s_nop 1\n" \
"s_nop 1\n" \
"s_nop 1\n" \
"s_nop 1\n" \
"s_nop 1\n" \
"s_nop 1\n" \
"s_nop 1\n" \
"s_nop 1\n" \
"s_nop 1\n" \
"s_nop 1\n" \
"s_nop 1\n" \
"s_nop 1\n" \
"s_nop 1\n" \
"s_nop 1\n" \
"s_nop 1\n" \
"s_nop 1\n" \
"s_nop 1\n" \
"s_nop 1\n" \
"s_nop 1\n" \
"s_nop 1\n" \
"s_nop 1\n" \
"s_nop 1\n" \
"s_nop 1\n" \
"s_nop 1\n" \
"s_nop 1\n" \
"s_nop 1\n" \
"s_nop 1\n" \
"s_nop 1\n" \
"s_nop 1\n" \
"s_nop 1\n" \
"s_nop 1\n" \
"s_nop 1\n" \
"s_nop 1\n" \
"s_nop 1\n" \
"XSC_%=:\n" \
"buffer_load_dwordx4 v2, s[36:39], s51 offen sc1 lds\n" \
"s_mov_b32 m0, s26\n" \
"s_nop 7\n" \
"buffer_load_dword off, s[40:43], %[fkv] sc1 lds\n" \
"buffer_store_dwordx4 v[48:51], v1, s[40:43], s19 offen sc1\n" \
"s_mov_b32 s19, 0x7ff00000\n" \
"s_waitcnt vmcnt(5)\n" \
"s_barrier\n" \
"ds_read_b128 v[48:51], v1 offset:0\n" \
"ds_read_b128 v[52:55], v1 offset:1024\n" \
"s_waitcnt lgkmcnt(1)\n" \
"v_smfmac_f32_16x16x64_bf16 v[28:31], v[48:51], v[88:95], v0\n" \
"v_smfmac_f32_16x16x64_bf16 v[32:35], v[48:51], v[96:103], v0\n" \
"ds_read_b128 v[88:91], v5 offset:0\n" \
"v_smfmac_f32_16x16x64_bf16 v[36:39], v[48:51], v[56:63], v0\n" \
"ds_read_b128 v[92:95], v5 offset:1024\n" \
"v_smfmac_f32_16x16x64_bf16 v[40:43], v[48:51], v[64:71], v0\n" \
"ds_read_b128 v[96:99], v5 offset:2048\n" \
"v_smfmac_f32_16x16x64_bf16 v[44:47], v[48:51], v[104:111], v0\n" \
"ds_read_b128 v[100:103], v5 offset:3072\n" \
"s_waitcnt lgkmcnt(4)\n" \
"v_smfmac_f32_16x16x64_bf16 v[28:31], v[52:55], v[112:119], v0\n" \
"s_waitcnt vmcnt(4)\n" \
"s_barrier\n" \
"ds_read_b128 v[56:59], v1 offset:2048\n" \
"v_smfmac_f32_16x16x64_bf16 v[32:35], v[52:55], v[120:127], v0\n" \
"ds_read_b128 v[60:63], v1 offset:3072\n" \
"v_smfmac_f32_16x16x64_bf16 v[36:39], v[52:55], v[128:135], v0\n" \
"ds_read_b128 v[104:107], v5 offset:4096\n" \
"v_smfmac_f32_16x16x64_bf16 v[40:43], v[52:55], v[136:143], v0\n" \
"ds_read_b128 v[108:111], v5 offset:5120\n" \
"v_smfmac_f32_16x16x64_bf16 v[44:47], v[52:55], v[144:151], v0\n" \
"ds_read_b128 v[112:115], v5 offset:6144\n" \
"s_waitcnt lgkmcnt(4)\n" \
"v_smfmac_f32_16x16x64_bf16 v[28:31], v[56:59], v[72:79], v0\n" \
"ds_read_b128 v[116:119], v5 offset:7168\n" \
"v_smfmac_f32_16x16x64_bf16 v[32:35], v[56:59], v[80:87], v0\n" \
"s_waitcnt vmcnt(3)\n" \
"s_barrier\n" \
"ds_read_b128 v[64:67], v1 offset:4096\n" \
"v_smfmac_f32_16x16x64_bf16 v[36:39], v[56:59], v[12:19], v0\n" \
"ds_read_b128 v[68:71], v1 offset:5120\n" \
"s_waitcnt lgkmcnt(6)\n" \
"v_smfmac_f32_16x16x64_bf16 v[28:31], v[60:63], a[16:23], v0\n" \
"ds_read_b128 v[136:139], v9 offset:26624\n" \
"v_smfmac_f32_16x16x64_bf16 v[32:35], v[60:63], a[24:31], v0\n" \
"ds_read_b128 v[140:143], v9 offset:27648\n" \
"v_smfmac_f32_16x16x64_bf16 v[36:39], v[60:63], a[32:39], v0\n" \
"ds_read_b128 v[120:123], v5 offset:8192\n" \
"s_waitcnt lgkmcnt(4)\n" \
"v_smfmac_f32_16x16x64_bf16 v[28:31], v[64:67], a[56:63], v0\n" \
"ds_read_b128 v[124:127], v5 offset:9216\n" \
"v_smfmac_f32_16x16x64_bf16 v[32:35], v[64:67], a[64:71], v0\n" \
"ds_read_b128 v[128:131], v5 offset:10240\n" \
"v_smfmac_f32_16x16x64_bf16 v[36:39], v[64:67], a[72:79], v0\n" \
"ds_read_b128 v[132:135], v5 offset:11264\n" \
"s_waitcnt lgkmcnt(6)\n" \
"v_smfmac_f32_16x16x64_bf16 v[28:31], v[68:71], a[96:103], v0\n" \
"s_waitcnt vmcnt(2)\n" \
"s_barrier\n" \
"ds_read_b128 v[72:75], v1 offset:6144\n" \
"v_smfmac_f32_16x16x64_bf16 v[32:35], v[68:71], a[104:111], v0\n" \
"ds_read_b128 v[76:79], v1 offset:7168\n" \
"v_smfmac_f32_16x16x64_bf16 v[36:39], v[68:71], a[112:119], v0\n" \
"ds_read_b128 v[80:83], v1 offset:8192\n" \
"v_smfmac_f32_16x16x64_bf16 v[40:43], v[56:59], a[0:7], v0\n" \
"ds_read_b128 v[84:87], v1 offset:9216\n" \
"v_smfmac_f32_16x16x64_bf16 v[40:43], v[60:63], a[40:47], v0\n" \
"s_waitcnt lgkmcnt(3)\n" \
"v_smfmac_f32_16x16x64_bf16 v[28:31], v[72:75], a[136:143], v0\n" \
"s_waitcnt lgkmcnt(2)\n" \
"v_smfmac_f32_16x16x64_bf16 v[28:31], v[76:79], a[176:183], v0\n" \
"s_waitcnt lgkmcnt(1)\n" \
"v_smfmac_f32_16x16x64_bf16 v[28:31], v[80:83], a[216:223], v0\n" \
"s_waitcnt lgkmcnt(0)\n" \
"v_smfmac_f32_16x16x64_bf16 v[28:31], v[84:87], a[248:255], v0\n" \
"v_smfmac_f32_16x16x64_bf16 v[32:35], v[72:75], a[144:151], v0\n" \
"v_smfmac_f32_16x16x64_bf16 v[32:35], v[76:79], a[184:191], v0\n" \
"s_waitcnt vmcnt(1)\n" \
"v_smfmac_f32_16x16x64_bf16 v[32:35], v[80:83], v[136:143], v0\n" \
"v_add_f32 v28, v28, v30\n" \
"v_add_f32 v29, v29, v31\n" \
"v_smfmac_f32_16x16x64_bf16 v[32:35], v[84:87], v[232:239], v0\n" \
"v_cmp_u_f32 vcc, v28, v29\n" \
"v_mul_f32 v28, s64, v28\n" \
"v_smfmac_f32_16x16x64_bf16 v[36:39], v[72:75], a[152:159], v0\n" \
"v_mul_f32 v29, s64, v29\n" \
"v_cvt_pk_bf16_f32 v28, v28, v29\n" \
"v_smfmac_f32_16x16x64_bf16 v[36:39], v[76:79], a[192:199], v0\n" \
"ds_write_b16 v6, v28 offset:0\n" \
"v_smfmac_f32_16x16x64_bf16 v[36:39], v[80:83], a[224:231], v0\n" \
"s_cbranch_vccnz XAGR_%=\n" \
"XAGOK_%=:\n" \
"ds_write_b16_d16_hi v6, v28 offset:288\n" \
"v_smfmac_f32_16x16x64_bf16 v[36:39], v[84:87], v[240:247], v0\n" \
"v_add_f32 v32, v32, v34\n" \
"v_add_f32 v33, v33, v35\n" \
"v_smfmac_f32_16x16x64_bf16 v[40:43], v[64:67], a[80:87], v0\n" \
"v_mul_f32 v32, s64, v32\n" \
"v_mul_f32 v33, s64, v33\n" \
"v_smfmac_f32_16x16x64_bf16 v[40:43], v[68:71], a[120:127], v0\n" \
"v_cvt_pk_bf16_f32 v32, v32, v33\n" \
"v_smfmac_f32_16x16x64_bf16 v[40:43], v[72:75], a[160:167], v0\n" \
"ds_write_b16 v6, v32 offset:8\n" \
"v_smfmac_f32_16x16x64_bf16 v[40:43], v[76:79], a[200:207], v0\n" \
"ds_write_b16_d16_hi v6, v32 offset:296\n" \
"v_smfmac_f32_16x16x64_bf16 v[40:43], v[80:83], a[232:239], v0\n" \
"v_add_f32 v36, v36, v38\n" \
"v_add_f32 v37, v37, v39\n" \
"v_smfmac_f32_16x16x64_bf16 v[40:43], v[84:87], v[248:255], v0\n" \
"v_cvt_pk_bf16_f32 v28, v36, v37\n" \
"v_smfmac_f32_16x16x64_bf16 v[44:47], v[56:59], a[8:15], v0\n" \
"ds_write_b16 v6, v28 offset:2304\n" \
"v_smfmac_f32_16x16x64_bf16 v[44:47], v[60:63], a[48:55], v0\n" \
"ds_write_b16_d16_hi v6, v28 offset:2592\n" \
"v_smfmac_f32_16x16x64_bf16 v[44:47], v[64:67], a[88:95], v0\n" \
"s_waitcnt lgkmcnt(2)\n" \
"s_barrier\n" \
"ds_read_b128 v[136:139], v7 offset:0\n" \
"v_smfmac_f32_16x16x64_bf16 v[44:47], v[68:71], a[128:135], v0\n" \
"v_add_f32 v40, v40, v42\n" \
"v_add_f32 v41, v41, v43\n" \
"v_smfmac_f32_16x16x64_bf16 v[44:47], v[72:75], a[168:175], v0\n" \
"v_cvt_pk_bf16_f32 v29, v40, v41\n" \
"v_smfmac_f32_16x16x64_bf16 v[44:47], v[76:79], a[208:215], v0\n" \
"ds_write_b16 v6, v29 offset:2312\n" \
"v_smfmac_f32_16x16x64_bf16 v[44:47], v[80:83], a[240:247], v0\n" \
"ds_write_b16_d16_hi v6, v29 offset:2600\n" \
"v_smfmac_f32_16x16x64_bf16 v[44:47], v[84:87], v[20:27], v0\n" \
"ds_read_b128 v[140:143], v7 offset:64\n" \
"s_waitcnt lgkmcnt(3)\n" \
"v_mfma_f32_16x16x32_bf16 v[12:15], v[88:91], v[136:139], 0\n" \
"ds_read_b128 v[144:147], v7 offset:128\n" \
"v_mov_b64 v[80:81], 0\n" \
"v_mfma_f32_16x16x32_bf16 v[32:35], v[104:107], v[136:139], 0\n" \
"ds_read_b128 v[148:151], v7 offset:192\n" \
"s_waitcnt lgkmcnt(2)\n" \
"v_mfma_f32_16x16x32_bf16 v[12:15], v[92:95], v[140:143], v[12:15]\n" \
"s_barrier\n" \
"ds_read_b128 v[48:51], v7 offset:2304\n" \
"v_add_f32 v44, v44, v46\n" \
"v_mfma_f32_16x16x32_bf16 v[32:35], v[108:111], v[140:143], v[32:35]\n" \
"ds_read_b128 v[52:55], v7 offset:2368\n" \
"v_add_f32 v45, v45, v47\n" \
"s_waitcnt lgkmcnt(3)\n" \
"v_mfma_f32_16x16x32_bf16 v[12:15], v[96:99], v[144:147], v[12:15]\n" \
"ds_read_b128 v[56:59], v7 offset:2432\n" \
"v_cvt_pk_bf16_f32 v30, v44, v45\n" \
"s_waitcnt lgkmcnt(3)\n" \
"v_mfma_f32_16x16x32_bf16 v[12:15], v[100:103], v[148:151], v[12:15]\n" \
"ds_write_b32 v8, v30\n" \
"v_mfma_f32_16x16x32_bf16 v[32:35], v[112:115], v[144:147], v[32:35]\n" \
"ds_read_b128 v[60:63], v7 offset:2496\n" \
"v_and_b32 v76, 4294966527, v8\n" \
"s_add_u32 m0, s45, 0\n" \
"v_mfma_f32_16x16x32_bf16 v[32:35], v[116:119], v[148:151], v[32:35]\n" \
"buffer_load_dwordx4 v1, s[40:43], s67 offen sc1 lds\n" \
"v_mov_b64 v[70:71], 0\n" \
"s_waitcnt lgkmcnt(3)\n" \
"v_mfma_f32_16x16x32_bf16 v[36:39], v[48:51], v[136:139], 0\n" \
"v_max3_f32 v5, v12, v13, v14\n" \
"v_mov_b64 v[68:69], 0\n" \
"s_add_u32 m0, s45, 4096\n" \
"v_mfma_f32_16x16x32_bf16 v[36:39], v[52:55], v[140:143], v[36:39]\n" \
"v_mov_b64 v[86:87], 0\n" \
"v_mov_b64 v[84:85], 0\n" \
"s_waitcnt lgkmcnt(0)\n" \
"v_mfma_f32_16x16x32_bf16 v[36:39], v[56:59], v[144:147], v[36:39]\n" \
"v_max3_f32 v5, v5, v15, v32\n" \
"v_max3_f32 v5, v5, v33, v34\n" \
"v_mfma_f32_16x16x32_bf16 v[36:39], v[60:63], v[148:151], v[36:39]\n" \
"v_max_f32 v5, v5, v35\n" \
"v_mov_b64 v[82:83], 0\n" \
"s_cmp_ge_u32 s48, 63\n" \
"s_cbranch_scc1 XFIN_%=\n" \
"v_mfma_f32_32x32x4_2b_bf16 v[88:119], v[10:11], v[10:11], 0\n" \
"s_barrier\n" \
"ds_read2st64_b32 v[64:65], v76 offset0:0 offset1:1\n" \
"ds_read2st64_b32 v[66:67], v76 offset0:2 offset1:3\n" \
"s_nop 0\n" \
"v_max3_f32 v78, v5, v36, v37\n" \
"v_max3_f32 v5, v5, v36, v37\n" \
"s_nop 1\n" \
"v_permlane32_swap_b32 v5, v78\n" \
"v_max_f32 v79, v5, v78\n" \
"v_max_f32 v78, v5, v78\n" \
"buffer_load_dwordx4 v1, s[40:43], s68 offen sc1 lds\n" \
"s_add_u32 m0, s45, 8192\n" \
"v_permlane16_swap_b32 v79, v78\n" \
"v_max_f32 v78, v78, v79\n" \
"v_pk_add_f32 v[12:13], v[12:13], v[78:79] op_sel_hi:[1,0] neg_lo:[0,1] neg_hi:[0,1]\n" \
"v_pk_add_f32 v[14:15], v[14:15], v[78:79] op_sel_hi:[1,0] neg_lo:[0,1] neg_hi:[0,1]\n" \
"v_pk_add_f32 v[32:33], v[32:33], v[78:79] op_sel_hi:[1,0] neg_lo:[0,1] neg_hi:[0,1]\n" \
"v_pk_add_f32 v[34:35], v[34:35], v[78:79] op_sel_hi:[1,0] neg_lo:[0,1] neg_hi:[0,1]\n" \
"v_pk_add_f32 v[36:37], v[36:37], v[78:79] op_sel_hi:[1,0] neg_lo:[0,1] neg_hi:[0,1]\n" \
"v_exp_f32 v12, v12\n" \
"v_exp_f32 v13, v13\n" \
"v_exp_f32 v14, v14\n" \
"v_exp_f32 v15, v15\n" \
"v_exp_f32 v32, v32\n" \
"v_cvt_pk_bf16_f32 v72, v12, v13\n" \
"v_exp_f32 v33, v33\n" \
"v_cvt_pk_bf16_f32 v73, v14, v15\n" \
"v_exp_f32 v34, v34\n" \
"v_pk_add_f32 v[12:13], v[12:13], v[14:15]\n" \
"v_exp_f32 v35, v35\n" \
"v_cvt_pk_bf16_f32 v74, v32, v33\n" \
"v_cvt_pk_bf16_f32 v75, v34, v35\n" \
"v_pk_add_f32 v[32:33], v[32:33], v[34:35]\n" \
"s_waitcnt lgkmcnt(0)\n" \
"v_pk_add_f32 v[12:13], v[12:13], v[32:33]\n" \
"v_mfma_f32_16x16x32_bf16 v[136:139], v[120:123], v[72:75], 0\n" \
"v_exp_f32 v36, v36\n" \
"v_mfma_f32_16x16x32_bf16 v[144:147], v[128:131], v[72:75], 0\n" \
"v_exp_f32 v37, v37\n" \
"v_mfma_f32_16x16x32_bf16 v[140:143], v[124:127], v[72:75], 0\n" \
"v_cvt_pk_bf16_f32 v68, v36, v37\n" \
"v_cvt_pk_bf16_f32 v71, v36, v37\n" \
"v_mfma_f32_16x16x32_bf16 v[148:151], v[132:135], v[72:75], 0\n" \
"v_add_f32 v12, v12, v13\n" \
"v_add_f32 v36, v36, v37\n" \
"v_mfma_f32_16x16x16_bf16 v[136:139], v[64:65], v[68:69], v[136:139]\n" \
"v_add_f32 v5, v12, v36\n" \
"v_add_f32 v78, v12, v36\n" \
"v_mfma_f32_16x16x16_bf16 v[144:147], v[66:67], v[68:69], v[144:147]\n" \
"buffer_load_dwordx4 v1, s[40:43], s69 offen sc1 lds\n" \
"v_mfma_f32_16x16x16_bf16 v[140:143], v[64:65], v[70:71], v[140:143]\n" \
"v_permlane32_swap_b32 v5, v78\n" \
"v_mfma_f32_16x16x16_bf16 v[148:151], v[66:67], v[70:71], v[148:151]\n" \
"v_add_f32 v79, v5, v78\n" \
"v_add_f32 v78, v5, v78\n" \
"v_cndmask_b32 v136, v136, v144, s[56:57]\n" \
"v_cndmask_b32 v137, v137, v145, s[56:57]\n" \
"v_permlane16_swap_b32 v79, v78\n" \
"v_cndmask_b32 v138, v138, v146, s[56:57]\n" \
"v_add_f32 v78, v78, v79\n" \
"v_cndmask_b32 v139, v139, v147, s[56:57]\n" \
"v_rcp_f32 v78, v78\n" \
"v_cndmask_b32 v140, v140, v148, s[56:57]\n" \
"v_cndmask_b32 v141, v141, v149, s[56:57]\n" \
"v_cndmask_b32 v142, v142, v150, s[56:57]\n" \
"v_cndmask_b32 v143, v143, v151, s[56:57]\n" \
"v_pk_mul_f32 v[136:137], v[136:137], v[78:79] op_sel_hi:[1,0]\n" \
"v_pk_mul_f32 v[138:139], v[138:139], v[78:79] op_sel_hi:[1,0]\n" \
"v_pk_mul_f32 v[140:141], v[140:141], v[78:79] op_sel_hi:[1,0]\n" \
"v_pk_mul_f32 v[142:143], v[142:143], v[78:79] op_sel_hi:[1,0]\n" \
"v_cvt_pk_bf16_f32 v40, v136, v137\n" \
"v_cvt_pk_bf16_f32 v41, v138, v139\n" \
"v_cvt_pk_bf16_f32 v42, v140, v141\n" \
"v_cvt_pk_bf16_f32 v43, v142, v143\n" \
"s_add_u32 s54, s53, 0x40000\n" \
"s_add_u32 s55, s53, 0x80000\n" \
"v_smfmac_f32_16x16x64_bf16 v[80:83], v[40:43], v[216:223], v0\n" \
"buffer_load_dwordx2 v[52:53], off, s[36:39], s52 sc1\n" \
"v_smfmac_f32_16x16x64_bf16 v[84:87], v[40:43], v[224:231], v0\n" \
"v_smfmac_f32_16x16x64_bf16 v[88:91], v[40:43], v[152:159], v0\n" \
"buffer_load_dword v55, off, s[40:43], s20 sc1\n" \
"v_smfmac_f32_16x16x64_bf16 v[92:95], v[40:43], v[160:167], v0\n" \
"v_add_f32 v80, v80, v82\n" \
"v_add_f32 v81, v81, v83\n" \
"v_smfmac_f32_16x16x64_bf16 v[96:99], v[40:43], v[168:175], v0\n" \
"v_cvt_pk_bf16_f32 v80, v80, v81\n" \
"v_add_f32 v84, v84, v86\n" \
"v_smfmac_f32_16x16x64_bf16 v[100:103], v[40:43], v[176:183], v0\n" \
"v_add_f32 v85, v85, v87\n" \
"v_cvt_pk_bf16_f32 v81, v84, v85\n" \
"buffer_store_dwordx2 v[80:81], v4, s[36:39], s53 offen sc1\n" \
"v_add_f32 v88, v88, v90\n" \
"v_smfmac_f32_16x16x64_bf16 v[104:107], v[40:43], v[184:191], v0\n" \
"v_add_f32 v89, v89, v91\n" \
"v_cvt_pk_bf16_f32 v88, v88, v89\n" \
"v_smfmac_f32_16x16x64_bf16 v[108:111], v[40:43], v[192:199], v0\n" \
"v_add_f32 v92, v92, v94\n" \
"v_add_f32 v93, v93, v95\n" \
"v_smfmac_f32_16x16x64_bf16 v[112:115], v[40:43], v[200:207], v0\n" \
"v_cvt_pk_bf16_f32 v89, v92, v93\n" \
"buffer_store_dwordx2 v[88:89], v4, s[36:39], s54 offen sc1\n" \
"v_add_f32 v96, v96, v98\n" \
"v_smfmac_f32_16x16x64_bf16 v[116:119], v[40:43], v[208:215], v0\n" \
"v_add_f32 v97, v97, v99\n" \
"v_cvt_pk_bf16_f32 v90, v96, v97\n" \
"v_add_f32 v100, v100, v102\n" \
"v_add_f32 v101, v101, v103\n" \
"v_cvt_pk_bf16_f32 v91, v100, v101\n" \
"buffer_store_dwordx2 v[90:91], v4, s[36:39], s55 offen sc1\n" \
"v_pk_add_f32 v[104:105], v[104:105], v[106:107]\n" \
"v_pk_add_f32 v[108:109], v[108:109], v[110:111]\n" \
"v_pk_add_f32 v[112:113], v[112:113], v[114:115]\n" \
"v_pk_add_f32 v[116:117], v[116:117], v[118:119]\n" \
"v_cvt_pk_bf16_f32 v104, v104, v105\n" \
"v_cvt_pk_bf16_f32 v105, v108, v109\n" \
"v_cvt_pk_bf16_f32 v106, v112, v113\n" \
"v_cvt_pk_bf16_f32 v107, v116, v117\n" \
"buffer_store_dwordx4 v[104:107], v3, s[36:39], s55 offen sc1\n" \
"v_mov_b32 v54, s26\n" \
"ds_read_b32 v54, v54\n" \
"v_mov_b32 v48, v28\n" \
"v_mov_b32 v49, v29\n" \
"v_mov_b32 v50, v30\n" \
"s_mov_b32 s19, s33\n" \
"s_add_u32 s48, s48, 1\n" \
"ds_read_b128 v[88:91], v9 offset:0\n" \
"ds_read_b128 v[92:95], v9 offset:1024\n" \
"ds_read_b128 v[96:99], v9 offset:2048\n" \
"ds_read_b128 v[100:103], v9 offset:3072\n" \
"ds_read_b128 v[56:59], v9 offset:4096\n" \
"ds_read_b128 v[60:63], v9 offset:5120\n" \
"ds_read_b128 v[64:67], v9 offset:6144\n" \
"ds_read_b128 v[68:71], v9 offset:7168\n" \
"ds_read_b128 v[104:107], v9 offset:8192\n" \
"ds_read_b128 v[108:111], v9 offset:9216\n" \
"ds_read_b128 v[112:115], v9 offset:10240\n" \
"ds_read_b128 v[116:119], v9 offset:11264\n" \
"ds_read_b128 v[120:123], v9 offset:12288\n" \
"ds_read_b128 v[124:127], v9 offset:13312\n" \
"ds_read_b128 v[128:131], v9 offset:14336\n" \
"ds_read_b128 v[132:135], v9 offset:15360\n" \
"ds_read_b128 v[136:139], v9 offset:16384\n" \
"ds_read_b128 v[140:143], v9 offset:17408\n" \
"ds_read_b128 v[144:147], v9 offset:18432\n" \
"ds_read_b128 v[148:151], v9 offset:19456\n" \
"ds_read_b128 v[72:75], v9 offset:20480\n" \
"ds_read_b128 v[76:79], v9 offset:21504\n" \
"ds_read_b128 v[80:83], v9 offset:22528\n" \
"ds_read_b128 v[84:87], v9 offset:23552\n" \
"ds_read_b128 v[12:15], v9 offset:24576\n" \
"ds_read_b128 v[16:19], v9 offset:25600\n" \
"v_mov_b64 v[28:29], 0\n" \
"v_mov_b64 v[30:31], 0\n" \
"v_mov_b64 v[32:33], 0\n" \
"v_mov_b64 v[34:35], 0\n" \
"v_mov_b64 v[36:37], 0\n" \
"v_mov_b64 v[38:39], 0\n" \
"v_mov_b64 v[40:41], 0\n" \
"v_mov_b64 v[42:43], 0\n" \
"v_mov_b64 v[44:45], 0\n" \
"v_mov_b64 v[46:47], 0\n" \
"s_add_u32 s33, s33, 0x200000\n" \
"s_sub_u32 s54, s48, 1\n" \
"s_bitcmp1_b64 %[dpar], s54\n" \
"s_cselect_b32 s54, 0x2000, 0\n" \
"s_xor_b32 s33, s33, s54\n" \
"s_mov_b32 s27, 0\n" \
"s_waitcnt vmcnt(4)\n" \
"s_cmp_lt_u32 s48, 61\n" \
"s_cbranch_scc1 XPE_%=\n" \
"buffer_store_dwordx4 v[48:51], v1, s[40:43], s19 offen sc1\n" \
"s_mov_b32 s19, 0x7ff00000\n" \
"XPE_%=:\n" \
"s_waitcnt lgkmcnt(15)\n" \
"s_add_u32 s55, s48, 1\n" \
"s_min_u32 s55, s55, 64\n" \
"v_readfirstlane_b32 s54, v54\n" \
"s_cmp_ge_u32 s54, s55\n" \
"s_cbranch_scc1 XK1D_%=\n" \
"XK1_%=:\n" \
"buffer_load_dword v54, off, s[40:43], %[fkv] sc1\n" \
"s_waitcnt vmcnt(0)\n" \
"v_readfirstlane_b32 s54, v54\n" \
"s_cmp_ge_u32 s54, s55\n" \
"s_cbranch_scc1 XK1R_%=\n" \
"s_sleep 1\n" \
"s_sub_u32 s44, s44, 1\n" \
"s_cbranch_scc0 XK1_%=\n" \
"s_mov_b32 s44, 0\n" \
"XK1R_%=:\n" \
"s_add_u32 m0, s45, 0\n" \
"s_nop 0\n" \
"buffer_load_dwordx4 v1, s[40:43], s67 offen sc1 lds\n" \
"s_add_u32 m0, s45, 4096\n" \
"s_nop 0\n" \
"buffer_load_dwordx4 v1, s[40:43], s68 offen sc1 lds\n" \
"s_add_u32 m0, s45, 8192\n" \
"s_nop 0\n" \
"buffer_load_dwordx4 v1, s[40:43], s69 offen sc1 lds\n" \
"XK1D_%=:\n" \
"s_cmp_lg_u32 s48, 1\n" \
"s_cbranch_scc1 XTX_%=\n" \
"XTW_%=:\n" \
"buffer_load_dwordx2 v[52:53], off, s[36:39], s52 sc1\n" \
"s_waitcnt vmcnt(0)\n" \
"v_readfirstlane_b32 s55, v53\n" \
"s_cmp_lg_u32 s55, 0xff81ff81\n" \
"s_cbranch_scc1 XTX_%=\n" \
"s_sleep 1\n" \
"s_sub_u32 s44, s44, 1\n" \
"s_cbranch_scc0 XTW_%=\n" \
"s_mov_b32 s44, 0\n" \
"XTX_%=:\n" \
"s_add_u32 s49, s49, 0x10000\n" \
"s_bitset0_b32 s49, 19\n" \
"s_add_u32 s51, s51, 0x10000\n" \
"s_bitset0_b32 s51, 19\n" \
"s_add_u32 s52, s52, 128\n" \
"v_readfirstlane_b32 s54, v52\n" \
"v_readfirstlane_b32 s55, v53\n" \
"s_cmp_lg_u32 s55, 0xff81ff81\n" \
"s_cselect_b32 s24, s54, s24\n" \
"s_cselect_b32 s25, s55, s25\n" \
"s_mul_i32 s54, %[per], s25\n" \
"s_lshr_b32 s54, s54, 14\n" \
"s_add_u32 s24, s24, s54\n" \
"s_mov_b32 s34, s24\n" \
"s_bitcmp1_b64 %[dpar], s48\n" \
"s_cselect_b32 s54, 0x2000, 0\n" \
"s_add_u32 s67, s67, 0x200000\n" \
"s_xor_b32 s67, s67, s54\n" \
"s_add_u32 s68, s68, 0x200000\n" \
"s_xor_b32 s68, s68, s54\n" \
"s_add_u32 s69, s69, 0x200000\n" \
"s_xor_b32 s69, s69, s54\n" \
"s_sub_u32 s54, s45, s62\n" \
"v_add_u32 v5, s54, v1\n" \
"s_xor_b32 s54, s54, 12288\n" \
"s_add_u32 s45, s54, s62\n" \
"s_add_u32 s23, s23, 1\n" \
"s_cmp_eq_u32 s23, 4\n" \
"s_cselect_b32 s23, 2, s23\n" \
"s_bitcmp1_b32 %[par], s23\n" \
"s_cselect_b32 s53, 0x2000, 0\n" \
"s_lshl_b32 s54, s23, 21\n" \
"s_add_u32 s53, s53, s54\n" \
"s_cmp_lg_u32 %[lead], 0\n" \
"s_cbranch_scc1 XMS_%=\n" \
"XMR_%=:\n" \
"s_sub_u32 s18, 11600, s25\n" \
"s_mul_i32 s18, s18, 1021\n" \
"s_lshr_b32 s18, s18, 14\n" \
"s_min_u32 s18, s18, 420\n" \
"s_waitcnt lgkmcnt(0)\n" \
"XT2_%=:\n" \
"s_memrealtime s[70:71]\n" \
"s_nop 1\n" \
"s_memrealtime s[72:73]\n" \
"s_nop 1\n" \
"s_memrealtime s[74:75]\n" \
"s_nop 1\n" \
"s_memrealtime s[76:77]\n" \
"s_nop 1\n" \
"s_memrealtime s[78:79]\n" \
"s_nop 1\n" \
"s_memrealtime s[80:81]\n" \
"s_nop 1\n" \
"s_memrealtime s[82:83]\n" \
"s_nop 1\n" \
"s_memrealtime s[84:85]\n" \
"s_waitcnt lgkmcnt(0)\n" \
"s_add_u32 s70, s70, s72\n" \
"s_add_u32 s74, s74, s76\n" \
"s_add_u32 s78, s78, s80\n" \
"s_add_u32 s82, s82, s84\n" \
"s_add_u32 s70, s70, s74\n" \
"s_add_u32 s78, s78, s82\n" \
"s_add_u32 s70, s70, s78\n" \
"s_sub_u32 s70, s34, s70\n" \
"s_add_u32 s71, s70, 800000\n" \
"s_cmp_lt_u32 s71, 799850\n" \
"s_cbranch_scc1 XBUMP_%=\n" \
"s_cmp_lt_u32 s70, 8000\n" \
"s_cselect_b32 s70, s70, 0\n" \
"s_cmp_gt_u32 s70, 120\n" \
"s_cbranch_scc1 XT2_%=\n" \
"s_mul_i32 s70, s70, s18\n" \
"s_lshr_b32 s70, s70, 8\n" \
"s_and_b32 s70, s70, -4\n" \
"s_sub_u32 s46, s60, s70\n" \
"s_subb_u32 s47, s61, 0\n" \
"s_setpc_b64 s[46:47]\n" \
"XMS_%=:\n" \
"s_and_b32 s54, s48, 3\n" \
"s_cmp_lg_u32 s54, 2\n" \
"s_cbranch_scc1 XMR_%=\n" \
"s_cmp_lt_u32 s48, 6\n" \
"s_cbranch_scc1 XMR_%=\n" \
"v_readfirstlane_b32 s54, v55\n" \
"s_add_u32 s20, s20, 4\n" \
"s_cmp_eq_u32 s54, 0\n" \
"s_cbranch_scc1 XMR_%=\n" \
"s_add_u32 s25, s54, s22\n" \
"v_mov_b32 v52, s24\n" \
"v_mov_b32 v53, s25\n" \
"s_mov_b64 exec, 1\n" \
"buffer_store_dwordx2 v[52:53], off, s[36:39], s52 sc1\n" \
"s_mov_b64 exec, -1\n" \
"s_branch XMR_%=\n" \
"XAGR_%=:\n" \
"v_max3_u32 v12, v48, v49, v50\n" \
"v_max3_u32 v12, v12, v51, v52\n" \
"v_max3_u32 v12, v12, v53, v54\n" \
"v_max3_u32 v12, v12, v55, v56\n" \
"v_max3_u32 v12, v12, v57, v58\n" \
"v_max3_u32 v12, v12, v59, v60\n" \
"v_max3_u32 v12, v12, v61, v62\n" \
"v_max3_u32 v12, v12, v63, v64\n" \
"v_max3_u32 v12, v12, v65, v66\n" \
"v_max3_u32 v12, v12, v67, v68\n" \
"v_max3_u32 v12, v12, v69, v70\n" \
"v_max3_u32 v12, v12, v71, v72\n" \
"v_max3_u32 v12, v12, v73, v74\n" \
"v_max3_u32 v12, v12, v75, v76\n" \
"v_max3_u32 v12, v12, v77, v78\n" \
"v_max3_u32 v12, v12, v79, v80\n" \
"v_max3_u32 v12, v12, v81, v82\n" \
"v_max3_u32 v12, v12, v83, v84\n" \
"v_max3_u32 v12, v12, v85, v86\n" \
"v_max_u32 v12, v12, v87\n" \
"v_cmp_eq_u32 vcc, s65, v12\n" \
"s_cbranch_vccz XAGOK_%=\n" \
"s_sub_u32 s66, s66, 1\n" \
"s_cbranch_scc0 XAGM_%=\n" \
"s_mov_b32 s66, 0\n" \
"s_mov_b32 s65, 1\n" \
"s_branch XAGOK_%=\n" \
"XAGM_%=:\n" \
"s_mul_i32 s54, %[fq], s25\n" \
"s_lshr_b32 s54, s54, 14\n" \
"s_add_u32 s34, s34, s54\n" \
"s_cmp_eq_u32 %[lead], 0\n" \
"s_cbranch_scc1 XAGN_%=\n" \
"s_mov_b32 s24, s34\n" \
"s_cmp_lt_u32 s48, 3\n" \
"s_cselect_b32 s27, 1, s27\n" \
"s_cmp_eq_u32 s27, 0\n" \
"s_cselect_b32 s54, %[inc], 0\n" \
"s_add_u32 s25, s25, s54\n" \
"s_add_u32 s22, s22, s54\n" \
"s_mov_b32 s27, 1\n" \
"v_mov_b32 v52, s24\n" \
"v_mov_b32 v53, s25\n" \
"s_mov_b64 exec, 1\n" \
"buffer_store_dwordx2 v[52:53], off, s[36:39], s52 sc1\n" \
"s_mov_b64 exec, -1\n" \
"XAGN_%=:\n" \
"s_waitcnt lgkmcnt(0)\n" \
"s_nop 7\n" \
"s_nop 1\n" \
"ds_read_b128 v[88:91], v9 offset:0\n" \
"ds_read_b128 v[92:95], v9 offset:1024\n" \
"ds_read_b128 v[96:99], v9 offset:2048\n" \
"ds_read_b128 v[100:103], v9 offset:3072\n" \
"ds_read_b128 v[56:59], v9 offset:4096\n" \
"ds_read_b128 v[60:63], v9 offset:5120\n" \
"ds_read_b128 v[64:67], v9 offset:6144\n" \
"ds_read_b128 v[68:71], v9 offset:7168\n" \
"ds_read_b128 v[104:107], v9 offset:8192\n" \
"ds_read_b128 v[108:111], v9 offset:9216\n" \
"ds_read_b128 v[112:115], v9 offset:10240\n" \
"ds_read_b128 v[116:119], v9 offset:11264\n" \
"ds_read_b128 v[120:123], v9 offset:12288\n" \
"ds_read_b128 v[124:127], v9 offset:13312\n" \
"ds_read_b128 v[128:131], v9 offset:14336\n" \
"ds_read_b128 v[132:135], v9 offset:15360\n" \
"ds_read_b128 v[136:139], v9 offset:16384\n" \
"ds_read_b128 v[140:143], v9 offset:17408\n" \
"ds_read_b128 v[144:147], v9 offset:18432\n" \
"ds_read_b128 v[148:151], v9 offset:19456\n" \
"ds_read_b128 v[72:75], v9 offset:20480\n" \
"ds_read_b128 v[76:79], v9 offset:21504\n" \
"ds_read_b128 v[80:83], v9 offset:22528\n" \
"ds_read_b128 v[84:87], v9 offset:23552\n" \
"ds_read_b128 v[12:15], v9 offset:24576\n" \
"ds_read_b128 v[16:19], v9 offset:25600\n" \
"v_mov_b64 v[28:29], 0\n" \
"v_mov_b64 v[30:31], 0\n" \
"v_mov_b64 v[32:33], 0\n" \
"v_mov_b64 v[34:35], 0\n" \
"v_mov_b64 v[36:37], 0\n" \
"v_mov_b64 v[38:39], 0\n" \
"v_mov_b64 v[40:41], 0\n" \
"v_mov_b64 v[42:43], 0\n" \
"v_mov_b64 v[44:45], 0\n" \
"v_mov_b64 v[46:47], 0\n" \
"s_waitcnt lgkmcnt(0)\n" \
"s_barrier\n" \
"s_branch XT2_%=\n" \
"XBUMP_%=:\n" \
"s_mul_i32 s54, %[fq], s25\n" \
"s_lshr_b32 s54, s54, 14\n" \
"s_add_u32 s34, s34, s54\n" \
"s_cmp_eq_u32 %[lead], 0\n" \
"s_cbranch_scc1 XT2_%=\n" \
"s_mov_b32 s24, s34\n" \
"s_cmp_lt_u32 s48, 3\n" \
"s_cselect_b32 s27, 1, s27\n" \
"s_cmp_eq_u32 s27, 0\n" \
"s_cselect_b32 s54, %[inc], 0\n" \
"s_add_u32 s25, s25, s54\n" \
"s_add_u32 s22, s22, s54\n" \
"s_mov_b32 s27, 1\n" \
"v_mov_b32 v52, s24\n" \
"v_mov_b32 v53, s25\n" \
"s_mov_b64 exec, 1\n" \
"buffer_store_dwordx2 v[52:53], off, s[36:39], s52 sc1\n" \
"s_mov_b64 exec, -1\n" \
"s_branch XT2_%=\n" \
"XFIN_%=:\n" \
"s_nop 7\n" \
"s_nop 1\n" \
"buffer_store_dwordx4 v[28:31], v1, s[40:43], s33 offen sc1\n" \
"s_waitcnt vmcnt(0)\n" \
"s_min_u32 s66, s66, s44\n" \
"s_cmp_lg_u32 s66, 0\n" \
"s_cbranch_scc1 XE_%=\n" \
"v_mov_b32 v0, 1\n" \
"buffer_store_dword v0, off, s[40:43], 0 offset:256 sc1\n" \
"XE_%=:\n" \
"XCZ_%=:\n" \
"s_endpgm\n"
#define X_ASM_CLOB "v255", "a255", "s16", "s17", "s18", "s19", "s20", "s21", "s22", "s23", "s24", "s25", "s26", "s27", "s28", "s29", "s30", "s31", "s33", "s34", "s35", "s36", "s37", "s38", "s39", "s40", "s41", "s42", "s43", "s44", "s45", "s46", "s47", "s48", "s49", "s50", "s51", "s52", "s53", "s54", "s55", "s56", "s57", "s58", "s59", "s60", "s61", "s62", "s63", "s64", "s65", "s66", "s67", "s68", "s69", "s70", "s71", "s72", "s73", "s74", "s75", "s76", "s77", "s78", "s79", "s80", "s81", "s82", "s83", "s84", "s85", "s86", "s87", "s88", "s89", "s90", "s91", "s92", "s93", "s94", "s95", "s96", "s97", "s98", "s99", "vcc", "scc", "memory"
#define Y_ASM_TEXT \
"s_setprio 3\n" \
"s_mov_b64 s[36:37], %[ws]\n" \
"s_and_b32 s37, s37, 0xffff\n" \
"s_mov_b32 s38, 0x800000\n" \
"s_mov_b32 s39, 0x00020000\n" \
"s_mov_b64 s[40:41], %[ws]\n" \
"s_add_u32 s40, s40, 0x800000\n" \
"s_addc_u32 s41, s41, 0\n" \
"s_and_b32 s41, s41, 0xffff\n" \
"s_mov_b32 s42, 0x9000000\n" \
"s_mov_b32 s43, 0x00020000\n" \
"v_mov_b32 v0, %[l16]\n" \
"v_mov_b32 v1, %[agst]\n" \
"v_mov_b32 v4, %[sel0]\n" \
"v_mov_b32 v5, %[sel1]\n" \
"v_mov_b32 v6, %[sel2]\n" \
"v_mov_b32 v7, %[sel3]\n" \
"s_bitcmp1_b32 %[par], 0\n" \
"s_cselect_b32 s49, 0x2000, 0\n" \
"s_add_u32 s52, s49, 0x100000\n" \
"s_add_u32 s49, s49, 0x10000\n" \
"s_mov_b32 s65, 0xff81ff81\n" \
"s_mov_b32 s66, 0x100000\n" \
"s_mov_b32 s48, 0\n" \
"s_mov_b32 s23, 0\n" \
"s_getpc_b64 s[60:61]\n" \
"PYS_%=:\n" \
"s_add_u32 s60, s60, YS_%=-PYS_%=\n" \
"s_addc_u32 s61, s61, 0\n" \
"s_mov_b32 s24, 0\n" \
"s_mov_b32 s25, 6000\n" \
"s_add_u32 s20, %[ms], 264\n" \
"s_sleep 1\n" \
"s_memtime s[46:47]\n" \
"s_memrealtime s[70:71]\n" \
"s_nop 1\n" \
"s_memrealtime s[72:73]\n" \
"s_nop 1\n" \
"s_memrealtime s[74:75]\n" \
"s_nop 1\n" \
"s_memrealtime s[76:77]\n" \
"s_nop 1\n" \
"s_memrealtime s[78:79]\n" \
"s_nop 1\n" \
"s_memrealtime s[80:81]\n" \
"s_nop 1\n" \
"s_memrealtime s[82:83]\n" \
"s_nop 1\n" \
"s_memrealtime s[84:85]\n" \
"s_waitcnt lgkmcnt(0)\n" \
"s_add_u32 s70, s70, s72\n" \
"s_add_u32 s74, s74, s76\n" \
"s_add_u32 s78, s78, s80\n" \
"s_add_u32 s82, s82, s84\n" \
"s_add_u32 s70, s70, s74\n" \
"s_add_u32 s78, s78, s82\n" \
"s_add_u32 s70, s70, s78\n" \
"s_mov_b32 s21, s70\n" \
"s_mov_b32 s22, s46\n" \
"s_sleep 70\n" \
"v_mov_b32 v10, 5000\n" \
"v_mov_b32 v11, 9000\n" \
"s_memtime s[46:47]\n" \
"s_memrealtime s[70:71]\n" \
"s_nop 1\n" \
"s_memrealtime s[72:73]\n" \
"s_nop 1\n" \
"s_memrealtime s[74:75]\n" \
"s_nop 1\n" \
"s_memrealtime s[76:77]\n" \
"s_nop 1\n" \
"s_memrealtime s[78:79]\n" \
"s_nop 1\n" \
"s_memrealtime s[80:81]\n" \
"s_nop 1\n" \
"s_memrealtime s[82:83]\n" \
"s_nop 1\n" \
"s_memrealtime s[84:85]\n" \
"s_waitcnt lgkmcnt(0)\n" \
"s_add_u32 s70, s70, s72\n" \
"s_add_u32 s74, s74, s76\n" \
"s_add_u32 s78, s78, s80\n" \
"s_add_u32 s82, s82, s84\n" \
"s_add_u32 s70, s70, s74\n" \
"s_add_u32 s78, s78, s82\n" \
"s_add_u32 s70, s70, s78\n" \
"s_sub_u32 s54, s70, s21\n" \
"s_sub_u32 s55, s46, s22\n" \
"v_cvt_f32_u32 v8, s55\n" \
"v_cvt_f32_u32 v9, s54\n" \
"v_rcp_f32 v8, v8\n" \
"v_mul_f32 v9, 0x46800000, v9\n" \
"v_mul_f32 v8, v8, v9\n" \
"v_cvt_u32_f32 v8, v8\n" \
"v_med3_u32 v8, v8, v10, v11\n" \
"s_mov_b64 exec, 1\n" \
"buffer_atomic_umax v8, off, s[40:43], s20 sc1\n" \
"s_mov_b64 exec, -1\n" \
"s_add_u32 s20, s20, 4\n" \
"s_mov_b32 s21, s70\n" \
"s_mov_b32 s22, s46\n" \
"s_sleep 12\n" \
"buffer_load_dword v44, off, s[40:43], 0 offset:264 sc1\n" \
"s_waitcnt vmcnt(0)\n" \
"v_readfirstlane_b32 s25, v44\n" \
"s_mul_i32 s25, s25, %[sf]\n" \
"s_lshr_b32 s25, s25, 10\n" \
"s_max_u32 s25, s25, 5600\n" \
"YLOOP_%=:\n" \
"s_add_u32 s23, s23, 1\n" \
"s_cmp_eq_u32 s23, 4\n" \
"s_cselect_b32 s23, 2, s23\n" \
"s_bitcmp1_b32 %[par], s23\n" \
"s_cselect_b32 s53, 0x2000, 0\n" \
"s_lshl_b32 s54, s23, 21\n" \
"s_add_u32 s53, s53, s54\n" \
"s_add_u32 s53, s53, %[yoff]\n" \
"s_add_u32 s62, s53, 4096\n" \
"s_cmp_eq_u32 s48, 0\n" \
"s_cbranch_scc1 YS_%=\n" \
"buffer_load_dwordx2 v[44:45], off, s[36:39], s52 sc1\n" \
"YW_%=:\n" \
"s_mul_i32 s33, %[c1], s25\n" \
"s_lshr_b32 s33, s33, 14\n" \
"s_add_u32 s33, s33, s24\n" \
"YC_%=:\n" \
"s_sleep 1\n" \
"s_memrealtime s[70:71]\n" \
"s_waitcnt lgkmcnt(0)\n" \
"s_lshl_b32 s70, s70, 3\n" \
"s_sub_u32 s70, s33, s70\n" \
"s_sub_u32 s70, s70, 250\n" \
"s_cmp_lt_u32 s70, 8000\n" \
"s_cbranch_scc1 YC_%=\n" \
"s_mov_b32 s34, s24\n" \
"buffer_load_dwordx2 v[44:45], off, s[36:39], s52 sc1\n" \
"s_waitcnt vmcnt(0)\n" \
"v_readfirstlane_b32 s54, v44\n" \
"v_readfirstlane_b32 s55, v45\n" \
"s_cmp_lg_u32 s55, 0xff81ff81\n" \
"s_cselect_b32 s24, s54, s24\n" \
"s_cselect_b32 s25, s55, s25\n" \
"s_cmp_lg_u32 s34, s24\n" \
"s_cbranch_scc1 YW_%=\n" \
"s_mul_i32 s33, %[c1], s25\n" \
"s_lshr_b32 s33, s33, 14\n" \
"s_add_u32 s33, s33, s24\n" \
"s_sub_u32 s18, 11600, s25\n" \
"s_mul_i32 s18, s18, 1021\n" \
"s_lshr_b32 s18, s18, 14\n" \
"s_min_u32 s18, s18, 420\n" \
"s_waitcnt lgkmcnt(0)\n" \
"YT_%=:\n" \
"s_memrealtime s[70:71]\n" \
"s_nop 1\n" \
"s_memrealtime s[72:73]\n" \
"s_nop 1\n" \
"s_memrealtime s[74:75]\n" \
"s_nop 1\n" \
"s_memrealtime s[76:77]\n" \
"s_nop 1\n" \
"s_memrealtime s[78:79]\n" \
"s_nop 1\n" \
"s_memrealtime s[80:81]\n" \
"s_nop 1\n" \
"s_memrealtime s[82:83]\n" \
"s_nop 1\n" \
"s_memrealtime s[84:85]\n" \
"s_waitcnt lgkmcnt(0)\n" \
"s_add_u32 s70, s70, s72\n" \
"s_add_u32 s74, s74, s76\n" \
"s_add_u32 s78, s78, s80\n" \
"s_add_u32 s82, s82, s84\n" \
"s_add_u32 s70, s70, s74\n" \
"s_add_u32 s78, s78, s82\n" \
"s_add_u32 s70, s70, s78\n" \
"s_sub_u32 s70, s33, s70\n" \
"s_cmp_lt_u32 s70, 8000\n" \
"s_cselect_b32 s70, s70, 0\n" \
"s_cmp_gt_u32 s70, 120\n" \
"s_cbranch_scc1 YT_%=\n" \
"s_mul_i32 s70, s70, s18\n" \
"s_lshr_b32 s70, s70, 8\n" \
"s_and_b32 s70, s70, -4\n" \
"s_sub_u32 s46, s60, s70\n" \
"s_subb_u32 s47, s61, 0\n" \
"s_setpc_b64 s[46:47]\n" \
"s_nop 1\n" \
"s_nop 1\n" \
"s_nop 1\n" \
"s_nop 1\n" \
"s_nop 1\n" \
"s_nop 1\n" \
"s_nop 1\n" \
"s_nop 1\n" \
"s_nop 1\n" \
"s_nop 1\n" \
"s_nop 1\n" \
"s_nop 1\n" \
"s_nop 1\n" \
"s_nop 1\n" \
"s_nop 1\n" \
"s_nop 1\n" \
"s_nop 1\n" \
"s_nop 1\n" \
"s_nop 1\n" \
"s_nop 1\n" \
"s_nop 1\n" \
"s_nop 1\n" \
"s_nop 1\n" \
"s_nop 1\n" \
"s_nop 1\n" \
"s_nop 1\n" \
"s_nop 1\n" \
"s_nop 1\n" \
"s_nop 1\n" \
"s_nop 1\n" \
"s_nop 1\n" \
"s_nop 1\n" \
"s_nop 1\n" \
"s_nop 1\n" \
"s_nop 1\n" \
"s_nop 1\n" \
"s_nop 1\n" \
"s_nop 1\n" \
"s_nop 1\n" \
"s_nop 1\n" \
"s_nop 1\n" \
"s_nop 1\n" \
"s_nop 1\n" \
"s_nop 1\n" \
"s_nop 1\n" \
"s_nop 1\n" \
"s_nop 1\n" \
"s_nop 1\n" \
"s_nop 1\n" \
"s_nop 1\n" \
"s_nop 1\n" \
"s_nop 1\n" \
"s_nop 1\n" \
"s_nop 1\n" \
"s_nop 1\n" \
"s_nop 1\n" \
"s_nop 1\n" \
"s_nop 1\n" \
"s_nop 1\n" \
"s_nop 1\n" \
"s_nop 1\n" \
"s_nop 1\n" \
"s_nop 1\n" \
"s_nop 1\n" \
"YS_%=:\n" \
"buffer_load_dwordx4 v[8:11], v0, s[36:39], s53 offen offset:0 sc1\n" \
"buffer_load_dwordx4 v[12:15], v0, s[36:39], s53 offen offset:1024 sc1\n" \
"buffer_load_dwordx4 v[16:19], v0, s[36:39], s53 offen offset:2048 sc1\n" \
"buffer_load_dwordx4 v[20:23], v0, s[36:39], s53 offen offset:3072 sc1\n" \
"buffer_load_dwordx4 v[24:27], v0, s[36:39], s62 offen offset:0 sc1\n" \
"buffer_load_dwordx4 v[28:31], v0, s[36:39], s62 offen offset:1024 sc1\n" \
"buffer_load_dwordx4 v[32:35], v0, s[36:39], s62 offen offset:2048 sc1\n" \
"buffer_load_dwordx4 v[36:39], v0, s[36:39], s62 offen offset:3072 sc1\n" \
"s_waitcnt vmcnt(7)\n" \
"v_mfma_f32_16x16x32_bf16 v[40:43], v[4:7], v[8:11], 0\n" \
"s_waitcnt vmcnt(6)\n" \
"v_mfma_f32_16x16x32_bf16 v[40:43], v[4:7], v[12:15], v[40:43]\n" \
"s_waitcnt vmcnt(5)\n" \
"v_mfma_f32_16x16x32_bf16 v[40:43], v[4:7], v[16:19], v[40:43]\n" \
"s_waitcnt vmcnt(4)\n" \
"v_mfma_f32_16x16x32_bf16 v[40:43], v[4:7], v[20:23], v[40:43]\n" \
"s_waitcnt vmcnt(3)\n" \
"v_mfma_f32_16x16x32_bf16 v[40:43], v[4:7], v[24:27], v[40:43]\n" \
"s_waitcnt vmcnt(2)\n" \
"v_mfma_f32_16x16x32_bf16 v[40:43], v[4:7], v[28:31], v[40:43]\n" \
"s_waitcnt vmcnt(1)\n" \
"v_mfma_f32_16x16x32_bf16 v[40:43], v[4:7], v[32:35], v[40:43]\n" \
"s_waitcnt vmcnt(0)\n" \
"v_mfma_f32_16x16x32_bf16 v[40:43], v[4:7], v[36:39], v[40:43]\n" \
"s_nop 7\n" \
"v_cmp_u_f32 vcc, v40, v41\n" \
"v_add_f32_dpp v42, v40, v40 row_ror:8 row_mask:0xf bank_mask:0xf\n" \
"v_add_f32_dpp v43, v41, v41 row_ror:8 row_mask:0xf bank_mask:0xf\n" \
"v_cvt_pk_bf16_f32 v40, v42, v43\n" \
"s_cbranch_vccnz YR_%=\n" \
"YOK_%=:\n" \
"buffer_store_dword v40, v1, s[36:39], s49 offen sc1\n" \
"s_cmp_lt_u32 s48, 60\n" \
"s_cbranch_scc1 YH_%=\n" \
"s_add_u32 s54, s48, 1\n" \
"v_mov_b32 v46, s54\n" \
"v_lshlrev_b32 v47, 10, v0\n" \
"s_mov_b64 exec, 15\n" \
"buffer_store_dword v46, v47, s[40:43], %[hint] offen sc1\n" \
"s_mov_b64 exec, -1\n" \
"YH_%=:\n" \
"s_cmp_lg_u32 s48, 0\n" \
"s_cbranch_scc1 Y1_%=\n" \
"s_cmp_eq_u32 %[sf], 0\n" \
"s_cbranch_scc1 Y0_%=\n" \
"s_memrealtime s[70:71]\n" \
"s_waitcnt lgkmcnt(0)\n" \
"s_lshl_b32 s24, s70, 3\n" \
"s_mul_i32 s54, %[per], s25\n" \
"s_lshr_b32 s54, s54, 14\n" \
"s_sub_u32 s24, s24, s54\n" \
"s_add_u32 s24, s24, 466\n" \
"v_mov_b32 v44, s24\n" \
"v_mov_b32 v45, s25\n" \
"s_mov_b64 exec, 1\n" \
"buffer_store_dwordx2 v[44:45], off, s[36:39], s52 sc1\n" \
"s_mov_b64 exec, -1\n" \
"Y0_%=:\n" \
"s_sleep 22\n" \
"buffer_load_dwordx2 v[44:45], off, s[36:39], s52 sc1\n" \
"s_waitcnt vmcnt(0)\n" \
"v_readfirstlane_b32 s54, v44\n" \
"v_readfirstlane_b32 s55, v45\n" \
"s_cmp_lg_u32 s55, 0xff81ff81\n" \
"s_cselect_b32 s24, s54, s24\n" \
"s_cselect_b32 s25, s55, s25\n" \
"s_cbranch_scc1 Y2_%=\n" \
"s_sub_u32 s66, s66, 1\n" \
"s_cbranch_scc0 Y0_%=\n" \
"s_mov_b32 s66, 0\n" \
"Y1_%=:\n" \
"buffer_load_dwordx2 v[44:45], off, s[36:39], s52 sc1\n" \
"s_waitcnt vmcnt(0)\n" \
"v_readfirstlane_b32 s54, v44\n" \
"v_readfirstlane_b32 s55, v45\n" \
"s_cmp_lg_u32 s55, 0xff81ff81\n" \
"s_cselect_b32 s24, s54, s24\n" \
"s_cselect_b32 s25, s55, s25\n" \
"Y2_%=:\n" \
"s_mul_i32 s54, %[per], s25\n" \
"s_lshr_b32 s54, s54, 14\n" \
"s_add_u32 s24, s24, s54\n" \
"s_sleep 3\n" \
"s_add_u32 s54, s48, 1\n" \
"v_mov_b32 v46, s54\n" \
"v_lshlrev_b32 v47, 10, v0\n" \
"s_mov_b64 exec, 15\n" \
"buffer_store_dword v46, v47, s[40:43], %[hint] offen sc1\n" \
"s_mov_b64 exec, -1\n" \
"s_sleep 7\n" \
"v_mov_b32 v16, s65\n" \
"v_mov_b32 v17, s65\n" \
"v_mov_b32 v18, s65\n" \
"v_mov_b32 v19, s65\n" \
"s_cmp_lg_u32 s48, 0\n" \
"s_cbranch_scc1 YZ_%=\n" \
"s_bitcmp1_b32 %[par], 3\n" \
"s_cselect_b32 s53, 0x2000, 0\n" \
"s_add_u32 s53, s53, %[yoff]\n" \
"s_add_u32 s53, s53, 0x600000\n" \
"s_add_u32 s62, s53, 4096\n" \
"YZ_%=:\n" \
"buffer_store_dwordx4 v[16:19], v0, s[36:39], s53 offen offset:0 sc1\n" \
"buffer_store_dwordx4 v[16:19], v0, s[36:39], s53 offen offset:1024 sc1\n" \
"buffer_store_dwordx4 v[16:19], v0, s[36:39], s53 offen offset:2048 sc1\n" \
"buffer_store_dwordx4 v[16:19], v0, s[36:39], s53 offen offset:3072 sc1\n" \
"buffer_store_dwordx4 v[16:19], v0, s[36:39], s62 offen offset:0 sc1\n" \
"buffer_store_dwordx4 v[16:19], v0, s[36:39], s62 offen offset:1024 sc1\n" \
"buffer_store_dwordx4 v[16:19], v0, s[36:39], s62 offen offset:2048 sc1\n" \
"buffer_store_dwordx4 v[16:19], v0, s[36:39], s62 offen offset:3072 sc1\n" \
"s_add_u32 s54, s49, 0x20000\n" \
"s_bitset0_b32 s54, 19\n" \
"buffer_store_dword v16, v1, s[36:39], s54 offen sc1\n" \
"s_barrier\n" \
"s_add_u32 s48, s48, 1\n" \
"s_and_b32 s54, s48, 3\n" \
"s_or_b32 s54, s54, %[ms]\n" \
"s_cbranch_scc1 YM_%=\n" \
"v_mov_b32 v10, 5000\n" \
"v_mov_b32 v11, 9000\n" \
"s_memtime s[46:47]\n" \
"s_memrealtime s[70:71]\n" \
"s_nop 1\n" \
"s_memrealtime s[72:73]\n" \
"s_nop 1\n" \
"s_memrealtime s[74:75]\n" \
"s_nop 1\n" \
"s_memrealtime s[76:77]\n" \
"s_nop 1\n" \
"s_memrealtime s[78:79]\n" \
"s_nop 1\n" \
"s_memrealtime s[80:81]\n" \
"s_nop 1\n" \
"s_memrealtime s[82:83]\n" \
"s_nop 1\n" \
"s_memrealtime s[84:85]\n" \
"s_waitcnt lgkmcnt(0)\n" \
"s_add_u32 s70, s70, s72\n" \
"s_add_u32 s74, s74, s76\n" \
"s_add_u32 s78, s78, s80\n" \
"s_add_u32 s82, s82, s84\n" \
"s_add_u32 s70, s70, s74\n" \
"s_add_u32 s78, s78, s82\n" \
"s_add_u32 s70, s70, s78\n" \
"s_sub_u32 s54, s70, s21\n" \
"s_sub_u32 s55, s46, s22\n" \
"v_cvt_f32_u32 v8, s55\n" \
"v_cvt_f32_u32 v9, s54\n" \
"v_rcp_f32 v8, v8\n" \
"v_mul_f32 v9, 0x46800000, v9\n" \
"v_mul_f32 v8, v8, v9\n" \
"v_cvt_u32_f32 v8, v8\n" \
"v_med3_u32 v8, v8, v10, v11\n" \
"s_mov_b64 exec, 1\n" \
"buffer_atomic_umax v8, off, s[40:43], s20 sc1\n" \
"s_mov_b64 exec, -1\n" \
"s_add_u32 s20, s20, 4\n" \
"s_mov_b32 s21, s70\n" \
"s_mov_b32 s22, s46\n" \
"YM_%=:\n" \
"s_add_u32 s49, s49, 0x10000\n" \
"s_bitset0_b32 s49, 19\n" \
"s_add_u32 s52, s52, 128\n" \
"s_cmp_lt_u32 s48, 63\n" \
"s_cbranch_scc1 YLOOP_%=\n" \
"s_branch YEND_%=\n" \
"YR_%=:\n" \
"v_max3_u32 v44, v8, v9, v10\n" \
"v_max3_u32 v44, v44, v11, v12\n" \
"v_max3_u32 v44, v44, v13, v14\n" \
"v_max3_u32 v44, v44, v15, v16\n" \
"v_max3_u32 v44, v44, v17, v18\n" \
"v_max3_u32 v44, v44, v19, v20\n" \
"v_max3_u32 v44, v44, v21, v22\n" \
"v_max3_u32 v44, v44, v23, v24\n" \
"v_max3_u32 v44, v44, v25, v26\n" \
"v_max3_u32 v44, v44, v27, v28\n" \
"v_max3_u32 v44, v44, v29, v30\n" \
"v_max3_u32 v44, v44, v31, v32\n" \
"v_max3_u32 v44, v44, v33, v34\n" \
"v_max3_u32 v44, v44, v35, v36\n" \
"v_max3_u32 v44, v44, v37, v38\n" \
"v_max_u32 v44, v44, v39\n" \
"v_cmp_eq_u32 vcc, s65, v44\n" \
"s_cbranch_vccz YOK_%=\n" \
"s_sub_u32 s66, s66, 0x10\n" \
"s_cbranch_scc1 YD_%=\n" \
"s_cmp_eq_u32 s48, 0\n" \
"s_cbranch_scc1 YS_%=\n" \
"s_mov_b32 s34, s24\n" \
"buffer_load_dwordx2 v[44:45], off, s[36:39], s52 sc1\n" \
"s_waitcnt vmcnt(0)\n" \
"v_readfirstlane_b32 s54, v44\n" \
"v_readfirstlane_b32 s55, v45\n" \
"s_cmp_lg_u32 s55, 0xff81ff81\n" \
"s_cselect_b32 s24, s54, s24\n" \
"s_cselect_b32 s25, s55, s25\n" \
"s_cmp_lg_u32 s34, s24\n" \
"s_cbranch_scc1 YW_%=\n" \
"s_branch YS_%=\n" \
"YD_%=:\n" \
"s_mov_b32 s66, 0\n" \
"s_branch YOK_%=\n" \
"YEND_%=:\n" \
"s_mul_i32 s33, s25, 1750\n" \
"s_lshr_b32 s33, s33, 14\n" \
"s_add_u32 s33, s33, s24\n" \
"YL_%=:\n" \
"s_memrealtime s[70:71]\n" \
"s_waitcnt lgkmcnt(0)\n" \
"s_lshl_b32 s70, s70, 3\n" \
"s_sub_u32 s70, s33, s70\n" \
"s_cmp_lt_u32 s70, 8000\n" \
"s_cbranch_scc1 YL_%=\n" \
"v_mov_b32 v46, 64\n" \
"v_lshlrev_b32 v47, 10, v0\n" \
"s_mov_b64 exec, 15\n" \
"buffer_store_dword v46, v47, s[40:43], %[hint] offen sc1\n" \
"s_mov_b64 exec, -1\n" \
"s_waitcnt vmcnt(0)\n" \
"s_cmp_lg_u32 s66, 0\n" \
"s_cbranch_scc1 YE_%=\n" \
"v_mov_b32 v0, 1\n" \
"buffer_store_dword v0, off, s[40:43], 0 offset:256 sc1\n" \
"YE_%=:\n" \
"s_endpgm\n"
#define Y_ASM_CLOB "v0", "v1", "v2", "v3", "v4", "v5", "v6", "v7", "v8", "v9", "v10", "v11", "v12", "v13", "v14", "v15", "v16", "v17", "v18", "v19", "v20", "v21", "v22", "v23", "v24", "v25", "v26", "v27", "v28", "v29", "v30", "v31", "v32", "v33", "v34", "v35", "v36", "v37", "v38", "v39", "v40", "v41", "v42", "v43", "v44", "v45", "v46", "v47", "s16", "s17", "s18", "s19", "s20", "s21", "s22", "s23", "s24", "s25", "s26", "s27", "s28", "s29", "s30", "s31", "s33", "s34", "s35", "s36", "s37", "s38", "s39", "s40", "s41", "s42", "s43", "s44", "s45", "s46", "s47", "s48", "s49", "s50", "s51", "s52", "s53", "s54", "s55", "s56", "s57", "s58", "s59", "s60", "s61", "s62", "s63", "s64", "s65", "s66", "s67", "s68", "s69", "s70", "s71", "s72", "s73", "s74", "s75", "s76", "s77", "s78", "s79", "s80", "s81", "s82", "s83", "s84", "s85", "s86", "s87", "s88", "s89", "s90", "s91", "s92", "s93", "s94", "s95", "s96", "s97", "s98", "s99", "vcc", "scc", "memory"
#define KV_ASM_TEXT \
"s_getpc_b64 s[46:47]\n" \
"KCW_%=:\n" \
"v_mbcnt_lo_u32_b32 v60, -1, 0\n" \
"v_mbcnt_hi_u32_b32 v60, -1, v60\n" \
"v_lshlrev_b32 v60, 7, v60\n" \
"v_min_u32 v61, KCZ_%=-KCW_%=, v60\n" \
"global_load_dword v62, v61, s[46:47]\n" \
"v_add_u32 v60, 0x2000, v60\n" \
"v_min_u32 v61, KCZ_%=-KCW_%=, v60\n" \
"global_load_dword v62, v61, s[46:47]\n" \
"v_add_u32 v60, 0x2000, v60\n" \
"s_mov_b64 s[40:41], %[ws]\n" \
"s_add_u32 s40, s40, 0x800000\n" \
"s_addc_u32 s41, s41, 0\n" \
"s_and_b32 s41, s41, 0xffff\n" \
"s_mov_b32 s42, 0x9000000\n" \
"s_mov_b32 s43, 0x00020000\n" \
"s_mov_b32 s33, %[wave]\n" \
"v_mbcnt_lo_u32_b32 v48, -1, 0\n" \
"v_mbcnt_hi_u32_b32 v48, -1, v48\n" \
"s_lshl_b32 s48, s33, 6\n" \
"v_add_lshl_u32 v48, v48, s48, 4\n" \
"ds_read_b128 v[0:3], v48 offset:0\n" \
"ds_read_b128 v[4:7], v48 offset:4096\n" \
"ds_read_b128 v[32:35], v48 offset:8192\n" \
"ds_read_b128 v[36:39], v48 offset:12288\n" \
"ds_read_b128 v[40:43], v48 offset:16384\n" \
"s_waitcnt lgkmcnt(0)\n" \
"s_barrier\n" \
"v_readfirstlane_b32 s70, v32\n" \
"v_readfirstlane_b32 s71, v33\n" \
"v_readfirstlane_b32 s72, v34\n" \
"v_readfirstlane_b32 s73, v35\n" \
"v_readfirstlane_b32 s51, v36\n" \
"v_readfirstlane_b32 s56, v37\n" \
"v_readfirstlane_b32 s55, v38\n" \
"v_readfirstlane_b32 s53, v39\n" \
"s_nop 4\n" \
"s_lshl_b32 s34, s33, 10\n" \
"s_cmp_eq_u32 s33, 0\n" \
"s_cselect_b32 s51, s51, 0x7ff00000\n" \
"s_mov_b32 s50, 0x80000\n" \
"s_mov_b32 s35, 0\n" \
"s_mov_b64 s[36:37], s[70:71]\n" \
"s_and_b32 s37, s37, 0xffff\n" \
"s_mov_b32 s38, 0x500000\n" \
"s_mov_b32 s39, 0x00020000\n" \
"s_mov_b64 s[44:45], s[72:73]\n" \
"s_and_b32 s45, s45, 0xffff\n" \
"s_mov_b32 s46, 0x500000\n" \
"s_mov_b32 s47, 0x00020000\n" \
"s_add_u32 m0, s34, 0\n" \
"s_mov_b32 s48, 0x0\n" \
"buffer_load_dwordx4 v1, s[36:39], s48 offen lds\n" \
"s_add_u32 m0, s34, 4096\n" \
"s_mov_b32 s48, 0x20000\n" \
"buffer_load_dwordx4 v1, s[36:39], s48 offen lds\n" \
"s_add_u32 m0, s34, 8192\n" \
"s_mov_b32 s48, 0x40000\n" \
"buffer_load_dwordx4 v1, s[36:39], s48 offen lds\n" \
"s_add_u32 m0, s34, 12288\n" \
"s_mov_b32 s48, 0x60000\n" \
"buffer_load_dwordx4 v1, s[36:39], s48 offen lds\n" \
"s_add_u32 m0, s34, 16384\n" \
"s_mov_b32 s48, 0x0\n" \
"buffer_load_dwordx4 v1, s[44:47], s48 offen lds\n" \
"s_add_u32 m0, s34, 20480\n" \
"s_mov_b32 s48, 0x20000\n" \
"buffer_load_dwordx4 v1, s[44:47], s48 offen lds\n" \
"s_add_u32 m0, s34, 24576\n" \
"s_mov_b32 s48, 0x40000\n" \
"buffer_load_dwordx4 v1, s[44:47], s48 offen lds\n" \
"s_add_u32 m0, s34, 28672\n" \
"s_mov_b32 s48, 0x60000\n" \
"buffer_load_dwordx4 v1, s[44:47], s48 offen lds\n" \
"s_add_u32 m0, s34, 32768\n" \
"s_mov_b32 s48, 0x80000\n" \
"buffer_load_dwordx4 v1, s[36:39], s48 offen lds\n" \
"s_add_u32 m0, s34, 36864\n" \
"s_mov_b32 s48, 0xa0000\n" \
"buffer_load_dwordx4 v1, s[36:39], s48 offen lds\n" \
"s_add_u32 m0, s34, 40960\n" \
"s_mov_b32 s48, 0xc0000\n" \
"buffer_load_dwordx4 v1, s[36:39], s48 offen lds\n" \
"s_add_u32 m0, s34, 45056\n" \
"s_mov_b32 s48, 0xe0000\n" \
"buffer_load_dwordx4 v1, s[36:39], s48 offen lds\n" \
"s_add_u32 m0, s34, 49152\n" \
"s_mov_b32 s48, 0x80000\n" \
"buffer_load_dwordx4 v1, s[44:47], s48 offen lds\n" \
"s_add_u32 m0, s34, 53248\n" \
"s_mov_b32 s48, 0xa0000\n" \
"buffer_load_dwordx4 v1, s[44:47], s48 offen lds\n" \
"s_add_u32 m0, s34, 57344\n" \
"s_mov_b32 s48, 0xc0000\n" \
"buffer_load_dwordx4 v1, s[44:47], s48 offen lds\n" \
"s_add_u32 m0, s34, 61440\n" \
"s_mov_b32 s48, 0xe0000\n" \
"buffer_load_dwordx4 v1, s[44:47], s48 offen lds\n" \
"s_add_u32 m0, s34, 65536\n" \
"s_mov_b32 s48, 0x100000\n" \
"buffer_load_dwordx4 v1, s[36:39], s48 offen lds\n" \
"s_add_u32 m0, s34, 69632\n" \
"s_mov_b32 s48, 0x120000\n" \
"buffer_load_dwordx4 v1, s[36:39], s48 offen lds\n" \
"s_add_u32 m0, s34, 73728\n" \
"s_mov_b32 s48, 0x140000\n" \
"buffer_load_dwordx4 v1, s[36:39], s48 offen lds\n" \
"s_add_u32 m0, s34, 77824\n" \
"s_mov_b32 s48, 0x160000\n" \
"buffer_load_dwordx4 v1, s[36:39], s48 offen lds\n" \
"s_add_u32 m0, s34, 81920\n" \
"s_mov_b32 s48, 0x100000\n" \
"buffer_load_dwordx4 v1, s[44:47], s48 offen lds\n" \
"s_add_u32 m0, s34, 86016\n" \
"s_mov_b32 s48, 0x120000\n" \
"buffer_load_dwordx4 v1, s[44:47], s48 offen lds\n" \
"s_add_u32 m0, s34, 90112\n" \
"s_mov_b32 s48, 0x140000\n" \
"buffer_load_dwordx4 v1, s[44:47], s48 offen lds\n" \
"s_add_u32 m0, s34, 94208\n" \
"s_mov_b32 s48, 0x160000\n" \
"buffer_load_dwordx4 v1, s[44:47], s48 offen lds\n" \
"s_add_u32 m0, s34, 98304\n" \
"s_mov_b32 s48, 0x180000\n" \
"buffer_load_dwordx4 v1, s[36:39], s48 offen lds\n" \
"s_add_u32 m0, s34, 102400\n" \
"s_mov_b32 s48, 0x1a0000\n" \
"buffer_load_dwordx4 v1, s[36:39], s48 offen lds\n" \
"s_add_u32 m0, s34, 106496\n" \
"s_mov_b32 s48, 0x1c0000\n" \
"buffer_load_dwordx4 v1, s[36:39], s48 offen lds\n" \
"s_add_u32 m0, s34, 110592\n" \
"s_mov_b32 s48, 0x1e0000\n" \
"buffer_load_dwordx4 v1, s[36:39], s48 offen lds\n" \
"s_add_u32 m0, s34, 114688\n" \
"s_mov_b32 s48, 0x180000\n" \
"buffer_load_dwordx4 v1, s[44:47], s48 offen lds\n" \
"s_add_u32 m0, s34, 118784\n" \
"s_mov_b32 s48, 0x1a0000\n" \
"buffer_load_dwordx4 v1, s[44:47], s48 offen lds\n" \
"s_add_u32 m0, s34, 122880\n" \
"s_mov_b32 s48, 0x1c0000\n" \
"buffer_load_dwordx4 v1, s[44:47], s48 offen lds\n" \
"s_add_u32 m0, s34, 126976\n" \
"s_mov_b32 s48, 0x1e0000\n" \
"buffer_load_dwordx4 v1, s[44:47], s48 offen lds\n" \
"s_add_u32 m0, s34, 131072\n" \
"s_mov_b32 s48, 0x200000\n" \
"buffer_load_dwordx4 v1, s[36:39], s48 offen lds\n" \
"s_add_u32 m0, s34, 135168\n" \
"s_mov_b32 s48, 0x220000\n" \
"buffer_load_dwordx4 v1, s[36:39], s48 offen lds\n" \
"s_add_u32 m0, s34, 139264\n" \
"s_mov_b32 s48, 0x240000\n" \
"buffer_load_dwordx4 v1, s[36:39], s48 offen lds\n" \
"s_add_u32 m0, s34, 143360\n" \
"s_mov_b32 s48, 0x260000\n" \
"buffer_load_dwordx4 v1, s[36:39], s48 offen lds\n" \
"s_add_u32 m0, s34, 147456\n" \
"s_mov_b32 s48, 0x200000\n" \
"buffer_load_dwordx4 v1, s[44:47], s48 offen lds\n" \
"s_add_u32 m0, s34, 151552\n" \
"s_mov_b32 s48, 0x220000\n" \
"buffer_load_dwordx4 v1, s[44:47], s48 offen lds\n" \
"s_add_u32 m0, s34, 155648\n" \
"s_mov_b32 s48, 0x240000\n" \
"buffer_load_dwordx4 v1, s[44:47], s48 offen lds\n" \
"s_add_u32 m0, s34, 159744\n" \
"s_mov_b32 s48, 0x260000\n" \
"buffer_load_dwordx4 v1, s[44:47], s48 offen lds\n" \
"s_waitcnt vmcnt(32)\n" \
"s_barrier\n" \
"v_add_u32 v20, 0x0, v4\n" \
"ds_read_b64_tr_b16 a[0:1], v20 offset:0\n" \
"ds_read_b64_tr_b16 a[2:3], v20 offset:1024\n" \
"ds_read_b64_tr_b16 a[16:17], v20 offset:8192\n" \
"ds_read_b64_tr_b16 a[18:19], v20 offset:9216\n" \
"v_add_u32 v20, 0x0, v5\n" \
"ds_read_b64_tr_b16 a[4:5], v20 offset:0\n" \
"ds_read_b64_tr_b16 a[6:7], v20 offset:1024\n" \
"ds_read_b64_tr_b16 a[20:21], v20 offset:8192\n" \
"ds_read_b64_tr_b16 a[22:23], v20 offset:9216\n" \
"v_add_u32 v20, 0x0, v6\n" \
"ds_read_b64_tr_b16 a[8:9], v20 offset:0\n" \
"ds_read_b64_tr_b16 a[10:11], v20 offset:1024\n" \
"ds_read_b64_tr_b16 a[24:25], v20 offset:8192\n" \
"ds_read_b64_tr_b16 a[26:27], v20 offset:9216\n" \
"v_add_u32 v20, 0x0, v7\n" \
"ds_read_b64_tr_b16 a[12:13], v20 offset:0\n" \
"ds_read_b64_tr_b16 a[14:15], v20 offset:1024\n" \
"ds_read_b64_tr_b16 a[28:29], v20 offset:8192\n" \
"ds_read_b64_tr_b16 a[30:31], v20 offset:9216\n" \
"s_waitcnt lgkmcnt(0)\n" \
"s_barrier\n" \
"s_add_u32 m0, s34, 0\n" \
"s_mov_b32 s48, 0x280000\n" \
"buffer_load_dwordx4 v1, s[36:39], s48 offen lds\n" \
"s_add_u32 m0, s34, 4096\n" \
"s_mov_b32 s48, 0x2a0000\n" \
"buffer_load_dwordx4 v1, s[36:39], s48 offen lds\n" \
"s_add_u32 m0, s34, 8192\n" \
"s_mov_b32 s48, 0x2c0000\n" \
"buffer_load_dwordx4 v1, s[36:39], s48 offen lds\n" \
"s_add_u32 m0, s34, 12288\n" \
"s_mov_b32 s48, 0x2e0000\n" \
"buffer_load_dwordx4 v1, s[36:39], s48 offen lds\n" \
"s_add_u32 m0, s34, 16384\n" \
"s_mov_b32 s48, 0x280000\n" \
"buffer_load_dwordx4 v1, s[44:47], s48 offen lds\n" \
"s_add_u32 m0, s34, 20480\n" \
"s_mov_b32 s48, 0x2a0000\n" \
"buffer_load_dwordx4 v1, s[44:47], s48 offen lds\n" \
"s_add_u32 m0, s34, 24576\n" \
"s_mov_b32 s48, 0x2c0000\n" \
"buffer_load_dwordx4 v1, s[44:47], s48 offen lds\n" \
"s_add_u32 m0, s34, 28672\n" \
"s_mov_b32 s48, 0x2e0000\n" \
"buffer_load_dwordx4 v1, s[44:47], s48 offen lds\n" \
"s_waitcnt vmcnt(32)\n" \
"s_barrier\n" \
"v_add_u32 v20, 0x8000, v4\n" \
"ds_read_b64_tr_b16 a[32:33], v20 offset:0\n" \
"ds_read_b64_tr_b16 a[34:35], v20 offset:1024\n" \
"ds_read_b64_tr_b16 a[48:49], v20 offset:8192\n" \
"ds_read_b64_tr_b16 a[50:51], v20 offset:9216\n" \
"v_add_u32 v20, 0x8000, v5\n" \
"ds_read_b64_tr_b16 a[36:37], v20 offset:0\n" \
"ds_read_b64_tr_b16 a[38:39], v20 offset:1024\n" \
"ds_read_b64_tr_b16 a[52:53], v20 offset:8192\n" \
"ds_read_b64_tr_b16 a[54:55], v20 offset:9216\n" \
"v_add_u32 v20, 0x8000, v6\n" \
"ds_read_b64_tr_b16 a[40:41], v20 offset:0\n" \
"ds_read_b64_tr_b16 a[42:43], v20 offset:1024\n" \
"ds_read_b64_tr_b16 a[56:57], v20 offset:8192\n" \
"ds_read_b64_tr_b16 a[58:59], v20 offset:9216\n" \
"v_add_u32 v20, 0x8000, v7\n" \
"ds_read_b64_tr_b16 a[44:45], v20 offset:0\n" \
"ds_read_b64_tr_b16 a[46:47], v20 offset:1024\n" \
"ds_read_b64_tr_b16 a[60:61], v20 offset:8192\n" \
"ds_read_b64_tr_b16 a[62:63], v20 offset:9216\n" \
"s_waitcnt lgkmcnt(0)\n" \
"s_barrier\n" \
"s_add_u32 m0, s34, 32768\n" \
"s_mov_b32 s48, 0x300000\n" \
"buffer_load_dwordx4 v1, s[36:39], s48 offen lds\n" \
"s_add_u32 m0, s34, 36864\n" \
"s_mov_b32 s48, 0x320000\n" \
"buffer_load_dwordx4 v1, s[36:39], s48 offen lds\n" \
"s_add_u32 m0, s34, 40960\n" \
"s_mov_b32 s48, 0x340000\n" \
"buffer_load_dwordx4 v1, s[36:39], s48 offen lds\n" \
"s_add_u32 m0, s34, 45056\n" \
"s_mov_b32 s48, 0x360000\n" \
"buffer_load_dwordx4 v1, s[36:39], s48 offen lds\n" \
"s_add_u32 m0, s34, 49152\n" \
"s_mov_b32 s48, 0x300000\n" \
"buffer_load_dwordx4 v1, s[44:47], s48 offen lds\n" \
"s_add_u32 m0, s34, 53248\n" \
"s_mov_b32 s48, 0x320000\n" \
"buffer_load_dwordx4 v1, s[44:47], s48 offen lds\n" \
"s_add_u32 m0, s34, 57344\n" \
"s_mov_b32 s48, 0x340000\n" \
"buffer_load_dwordx4 v1, s[44:47], s48 offen lds\n" \
"s_add_u32 m0, s34, 61440\n" \
"s_mov_b32 s48, 0x360000\n" \
"buffer_load_dwordx4 v1, s[44:47], s48 offen lds\n" \
"s_waitcnt vmcnt(32)\n" \
"s_barrier\n" \
"v_add_u32 v20, 0x10000, v4\n" \
"ds_read_b64_tr_b16 a[64:65], v20 offset:0\n" \
"ds_read_b64_tr_b16 a[66:67], v20 offset:1024\n" \
"ds_read_b64_tr_b16 a[80:81], v20 offset:8192\n" \
"ds_read_b64_tr_b16 a[82:83], v20 offset:9216\n" \
"v_add_u32 v20, 0x10000, v5\n" \
"ds_read_b64_tr_b16 a[68:69], v20 offset:0\n" \
"ds_read_b64_tr_b16 a[70:71], v20 offset:1024\n" \
"ds_read_b64_tr_b16 a[84:85], v20 offset:8192\n" \
"ds_read_b64_tr_b16 a[86:87], v20 offset:9216\n" \
"v_add_u32 v20, 0x10000, v6\n" \
"ds_read_b64_tr_b16 a[72:73], v20 offset:0\n" \
"ds_read_b64_tr_b16 a[74:75], v20 offset:1024\n" \
"ds_read_b64_tr_b16 a[88:89], v20 offset:8192\n" \
"ds_read_b64_tr_b16 a[90:91], v20 offset:9216\n" \
"v_add_u32 v20, 0x10000, v7\n" \
"ds_read_b64_tr_b16 a[76:77], v20 offset:0\n" \
"ds_read_b64_tr_b16 a[78:79], v20 offset:1024\n" \
"ds_read_b64_tr_b16 a[92:93], v20 offset:8192\n" \
"ds_read_b64_tr_b16 a[94:95], v20 offset:9216\n" \
"s_waitcnt lgkmcnt(0)\n" \
"s_barrier\n" \
"s_add_u32 m0, s34, 65536\n" \
"s_mov_b32 s48, 0x380000\n" \
"buffer_load_dwordx4 v1, s[36:39], s48 offen lds\n" \
"s_add_u32 m0, s34, 69632\n" \
"s_mov_b32 s48, 0x3a0000\n" \
"buffer_load_dwordx4 v1, s[36:39], s48 offen lds\n" \
"s_add_u32 m0, s34, 73728\n" \
"s_mov_b32 s48, 0x3c0000\n" \
"buffer_load_dwordx4 v1, s[36:39], s48 offen lds\n" \
"s_add_u32 m0, s34, 77824\n" \
"s_mov_b32 s48, 0x3e0000\n" \
"buffer_load_dwordx4 v1, s[36:39], s48 offen lds\n" \
"s_add_u32 m0, s34, 81920\n" \
"s_mov_b32 s48, 0x380000\n" \
"buffer_load_dwordx4 v1, s[44:47], s48 offen lds\n" \
"s_add_u32 m0, s34, 86016\n" \
"s_mov_b32 s48, 0x3a0000\n" \
"buffer_load_dwordx4 v1, s[44:47], s48 offen lds\n" \
"s_add_u32 m0, s34, 90112\n" \
"s_mov_b32 s48, 0x3c0000\n" \
"buffer_load_dwordx4 v1, s[44:47], s48 offen lds\n" \
"s_add_u32 m0, s34, 94208\n" \
"s_mov_b32 s48, 0x3e0000\n" \
"buffer_load_dwordx4 v1, s[44:47], s48 offen lds\n" \
"s_waitcnt vmcnt(32)\n" \
"s_barrier\n" \
"v_add_u32 v20, 0x18000, v4\n" \
"ds_read_b64_tr_b16 a[96:97], v20 offset:0\n" \
"ds_read_b64_tr_b16 a[98:99], v20 offset:1024\n" \
"ds_read_b64_tr_b16 a[112:113], v20 offset:8192\n" \
"ds_read_b64_tr_b16 a[114:115], v20 offset:9216\n" \
"v_add_u32 v20, 0x18000, v5\n" \
"ds_read_b64_tr_b16 a[100:101], v20 offset:0\n" \
"ds_read_b64_tr_b16 a[102:103], v20 offset:1024\n" \
"ds_read_b64_tr_b16 a[116:117], v20 offset:8192\n" \
"ds_read_b64_tr_b16 a[118:119], v20 offset:9216\n" \
"v_add_u32 v20, 0x18000, v6\n" \
"ds_read_b64_tr_b16 a[104:105], v20 offset:0\n" \
"ds_read_b64_tr_b16 a[106:107], v20 offset:1024\n" \
"ds_read_b64_tr_b16 a[120:121], v20 offset:8192\n" \
"ds_read_b64_tr_b16 a[122:123], v20 offset:9216\n" \
"v_add_u32 v20, 0x18000, v7\n" \
"ds_read_b64_tr_b16 a[108:109], v20 offset:0\n" \
"ds_read_b64_tr_b16 a[110:111], v20 offset:1024\n" \
"ds_read_b64_tr_b16 a[124:125], v20 offset:8192\n" \
"ds_read_b64_tr_b16 a[126:127], v20 offset:9216\n" \
"s_waitcnt lgkmcnt(0)\n" \
"s_barrier\n" \
"s_add_u32 m0, s34, 98304\n" \
"s_mov_b32 s48, 0x400000\n" \
"buffer_load_dwordx4 v1, s[36:39], s48 offen lds\n" \
"s_add_u32 m0, s34, 102400\n" \
"s_mov_b32 s48, 0x420000\n" \
"buffer_load_dwordx4 v1, s[36:39], s48 offen lds\n" \
"s_add_u32 m0, s34, 106496\n" \
"s_mov_b32 s48, 0x440000\n" \
"buffer_load_dwordx4 v1, s[36:39], s48 offen lds\n" \
"s_add_u32 m0, s34, 110592\n" \
"s_mov_b32 s48, 0x460000\n" \
"buffer_load_dwordx4 v1, s[36:39], s48 offen lds\n" \
"s_add_u32 m0, s34, 114688\n" \
"s_mov_b32 s48, 0x400000\n" \
"buffer_load_dwordx4 v1, s[44:47], s48 offen lds\n" \
"s_add_u32 m0, s34, 118784\n" \
"s_mov_b32 s48, 0x420000\n" \
"buffer_load_dwordx4 v1, s[44:47], s48 offen lds\n" \
"s_add_u32 m0, s34, 122880\n" \
"s_mov_b32 s48, 0x440000\n" \
"buffer_load_dwordx4 v1, s[44:47], s48 offen lds\n" \
"s_add_u32 m0, s34, 126976\n" \
"s_mov_b32 s48, 0x460000\n" \
"buffer_load_dwordx4 v1, s[44:47], s48 offen lds\n" \
"s_waitcnt vmcnt(32)\n" \
"s_barrier\n" \
"v_add_u32 v20, 0x20000, v4\n" \
"ds_read_b64_tr_b16 a[128:129], v20 offset:0\n" \
"ds_read_b64_tr_b16 a[130:131], v20 offset:1024\n" \
"ds_read_b64_tr_b16 a[144:145], v20 offset:8192\n" \
"ds_read_b64_tr_b16 a[146:147], v20 offset:9216\n" \
"v_add_u32 v20, 0x20000, v5\n" \
"ds_read_b64_tr_b16 a[132:133], v20 offset:0\n" \
"ds_read_b64_tr_b16 a[134:135], v20 offset:1024\n" \
"ds_read_b64_tr_b16 a[148:149], v20 offset:8192\n" \
"ds_read_b64_tr_b16 a[150:151], v20 offset:9216\n" \
"v_add_u32 v20, 0x20000, v6\n" \
"ds_read_b64_tr_b16 a[136:137], v20 offset:0\n" \
"ds_read_b64_tr_b16 a[138:139], v20 offset:1024\n" \
"ds_read_b64_tr_b16 a[152:153], v20 offset:8192\n" \
"ds_read_b64_tr_b16 a[154:155], v20 offset:9216\n" \
"v_add_u32 v20, 0x20000, v7\n" \
"ds_read_b64_tr_b16 a[140:141], v20 offset:0\n" \
"ds_read_b64_tr_b16 a[142:143], v20 offset:1024\n" \
"ds_read_b64_tr_b16 a[156:157], v20 offset:8192\n" \
"ds_read_b64_tr_b16 a[158:159], v20 offset:9216\n" \
"s_waitcnt lgkmcnt(0)\n" \
"s_barrier\n" \
"s_add_u32 m0, s34, 131072\n" \
"s_mov_b32 s48, 0x480000\n" \
"buffer_load_dwordx4 v1, s[36:39], s48 offen lds\n" \
"s_add_u32 m0, s34, 135168\n" \
"s_mov_b32 s48, 0x4a0000\n" \
"buffer_load_dwordx4 v1, s[36:39], s48 offen lds\n" \
"s_add_u32 m0, s34, 139264\n" \
"s_mov_b32 s48, 0x4c0000\n" \
"buffer_load_dwordx4 v1, s[36:39], s48 offen lds\n" \
"s_add_u32 m0, s34, 143360\n" \
"s_mov_b32 s48, 0x4e0000\n" \
"buffer_load_dwordx4 v1, s[36:39], s48 offen lds\n" \
"s_add_u32 m0, s34, 147456\n" \
"s_mov_b32 s48, 0x480000\n" \
"buffer_load_dwordx4 v1, s[44:47], s48 offen lds\n" \
"s_add_u32 m0, s34, 151552\n" \
"s_mov_b32 s48, 0x4a0000\n" \
"buffer_load_dwordx4 v1, s[44:47], s48 offen lds\n" \
"s_add_u32 m0, s34, 155648\n" \
"s_mov_b32 s48, 0x4c0000\n" \
"buffer_load_dwordx4 v1, s[44:47], s48 offen lds\n" \
"s_add_u32 m0, s34, 159744\n" \
"s_mov_b32 s48, 0x4e0000\n" \
"buffer_load_dwordx4 v1, s[44:47], s48 offen lds\n" \
"s_waitcnt vmcnt(32)\n" \
"s_barrier\n" \
"v_add_u32 v20, 0x0, v4\n" \
"ds_read_b64_tr_b16 a[160:161], v20 offset:0\n" \
"ds_read_b64_tr_b16 a[162:163], v20 offset:1024\n" \
"ds_read_b64_tr_b16 a[176:177], v20 offset:8192\n" \
"ds_read_b64_tr_b16 a[178:179], v20 offset:9216\n" \
"v_add_u32 v20, 0x0, v5\n" \
"ds_read_b64_tr_b16 a[164:165], v20 offset:0\n" \
"ds_read_b64_tr_b16 a[166:167], v20 offset:1024\n" \
"ds_read_b64_tr_b16 a[180:181], v20 offset:8192\n" \
"ds_read_b64_tr_b16 a[182:183], v20 offset:9216\n" \
"v_add_u32 v20, 0x0, v6\n" \
"ds_read_b64_tr_b16 a[168:169], v20 offset:0\n" \
"ds_read_b64_tr_b16 a[170:171], v20 offset:1024\n" \
"ds_read_b64_tr_b16 a[184:185], v20 offset:8192\n" \
"ds_read_b64_tr_b16 a[186:187], v20 offset:9216\n" \
"v_add_u32 v20, 0x0, v7\n" \
"ds_read_b64_tr_b16 a[172:173], v20 offset:0\n" \
"ds_read_b64_tr_b16 a[174:175], v20 offset:1024\n" \
"ds_read_b64_tr_b16 a[188:189], v20 offset:8192\n" \
"ds_read_b64_tr_b16 a[190:191], v20 offset:9216\n" \
"s_waitcnt lgkmcnt(0)\n" \
"s_barrier\n" \
"s_waitcnt vmcnt(24)\n" \
"s_barrier\n" \
"v_add_u32 v20, 0x8000, v4\n" \
"ds_read_b64_tr_b16 a[192:193], v20 offset:0\n" \
"ds_read_b64_tr_b16 a[194:195], v20 offset:1024\n" \
"ds_read_b64_tr_b16 a[208:209], v20 offset:8192\n" \
"ds_read_b64_tr_b16 a[210:211], v20 offset:9216\n" \
"v_add_u32 v20, 0x8000, v5\n" \
"ds_read_b64_tr_b16 a[196:197], v20 offset:0\n" \
"ds_read_b64_tr_b16 a[198:199], v20 offset:1024\n" \
"ds_read_b64_tr_b16 a[212:213], v20 offset:8192\n" \
"ds_read_b64_tr_b16 a[214:215], v20 offset:9216\n" \
"v_add_u32 v20, 0x8000, v6\n" \
"ds_read_b64_tr_b16 a[200:201], v20 offset:0\n" \
"ds_read_b64_tr_b16 a[202:203], v20 offset:1024\n" \
"ds_read_b64_tr_b16 a[216:217], v20 offset:8192\n" \
"ds_read_b64_tr_b16 a[218:219], v20 offset:9216\n" \
"v_add_u32 v20, 0x8000, v7\n" \
"ds_read_b64_tr_b16 a[204:205], v20 offset:0\n" \
"ds_read_b64_tr_b16 a[206:207], v20 offset:1024\n" \
"ds_read_b64_tr_b16 a[220:221], v20 offset:8192\n" \
"ds_read_b64_tr_b16 a[222:223], v20 offset:9216\n" \
"s_waitcnt lgkmcnt(0)\n" \
"s_barrier\n" \
"s_waitcnt vmcnt(16)\n" \
"s_barrier\n" \
"v_add_u32 v20, 0x10000, v4\n" \
"ds_read_b64_tr_b16 a[224:225], v20 offset:0\n" \
"ds_read_b64_tr_b16 a[226:227], v20 offset:1024\n" \
"ds_read_b64_tr_b16 a[240:241], v20 offset:8192\n" \
"ds_read_b64_tr_b16 a[242:243], v20 offset:9216\n" \
"v_add_u32 v20, 0x10000, v5\n" \
"ds_read_b64_tr_b16 a[228:229], v20 offset:0\n" \
"ds_read_b64_tr_b16 a[230:231], v20 offset:1024\n" \
"ds_read_b64_tr_b16 a[244:245], v20 offset:8192\n" \
"ds_read_b64_tr_b16 a[246:247], v20 offset:9216\n" \
"v_add_u32 v20, 0x10000, v6\n" \
"ds_read_b64_tr_b16 a[232:233], v20 offset:0\n" \
"ds_read_b64_tr_b16 a[234:235], v20 offset:1024\n" \
"ds_read_b64_tr_b16 a[248:249], v20 offset:8192\n" \
"ds_read_b64_tr_b16 a[250:251], v20 offset:9216\n" \
"v_add_u32 v20, 0x10000, v7\n" \
"ds_read_b64_tr_b16 a[236:237], v20 offset:0\n" \
"ds_read_b64_tr_b16 a[238:239], v20 offset:1024\n" \
"ds_read_b64_tr_b16 a[252:253], v20 offset:8192\n" \
"ds_read_b64_tr_b16 a[254:255], v20 offset:9216\n" \
"s_waitcnt lgkmcnt(0)\n" \
"s_barrier\n" \
"s_waitcnt vmcnt(8)\n" \
"s_barrier\n" \
"v_add_u32 v20, 0x18000, v4\n" \
"ds_read_b64_tr_b16 v[192:193], v20 offset:0\n" \
"ds_read_b64_tr_b16 v[194:195], v20 offset:1024\n" \
"ds_read_b64_tr_b16 v[208:209], v20 offset:8192\n" \
"ds_read_b64_tr_b16 v[210:211], v20 offset:9216\n" \
"v_add_u32 v20, 0x18000, v5\n" \
"ds_read_b64_tr_b16 v[196:197], v20 offset:0\n" \
"ds_read_b64_tr_b16 v[198:199], v20 offset:1024\n" \
"ds_read_b64_tr_b16 v[212:213], v20 offset:8192\n" \
"ds_read_b64_tr_b16 v[214:215], v20 offset:9216\n" \
"v_add_u32 v20, 0x18000, v6\n" \
"ds_read_b64_tr_b16 v[200:201], v20 offset:0\n" \
"ds_read_b64_tr_b16 v[202:203], v20 offset:1024\n" \
"ds_read_b64_tr_b16 v[216:217], v20 offset:8192\n" \
"ds_read_b64_tr_b16 v[218:219], v20 offset:9216\n" \
"v_add_u32 v20, 0x18000, v7\n" \
"ds_read_b64_tr_b16 v[204:205], v20 offset:0\n" \
"ds_read_b64_tr_b16 v[206:207], v20 offset:1024\n" \
"ds_read_b64_tr_b16 v[220:221], v20 offset:8192\n" \
"ds_read_b64_tr_b16 v[222:223], v20 offset:9216\n" \
"s_waitcnt lgkmcnt(0)\n" \
"s_barrier\n" \
"s_waitcnt vmcnt(0)\n" \
"s_barrier\n" \
"v_add_u32 v20, 0x20000, v4\n" \
"ds_read_b64_tr_b16 v[224:225], v20 offset:0\n" \
"ds_read_b64_tr_b16 v[226:227], v20 offset:1024\n" \
"ds_read_b64_tr_b16 v[240:241], v20 offset:8192\n" \
"ds_read_b64_tr_b16 v[242:243], v20 offset:9216\n" \
"v_add_u32 v20, 0x20000, v5\n" \
"ds_read_b64_tr_b16 v[228:229], v20 offset:0\n" \
"ds_read_b64_tr_b16 v[230:231], v20 offset:1024\n" \
"ds_read_b64_tr_b16 v[244:245], v20 offset:8192\n" \
"ds_read_b64_tr_b16 v[246:247], v20 offset:9216\n" \
"v_add_u32 v20, 0x20000, v6\n" \
"ds_read_b64_tr_b16 v[232:233], v20 offset:0\n" \
"ds_read_b64_tr_b16 v[234:235], v20 offset:1024\n" \
"ds_read_b64_tr_b16 v[248:249], v20 offset:8192\n" \
"ds_read_b64_tr_b16 v[250:251], v20 offset:9216\n" \
"v_add_u32 v20, 0x20000, v7\n" \
"ds_read_b64_tr_b16 v[236:237], v20 offset:0\n" \
"ds_read_b64_tr_b16 v[238:239], v20 offset:1024\n" \
"ds_read_b64_tr_b16 v[252:253], v20 offset:8192\n" \
"ds_read_b64_tr_b16 v[254:255], v20 offset:9216\n" \
"s_waitcnt lgkmcnt(0)\n" \
"s_barrier\n" \
"s_mov_b32 s57, s34\n" \
"s_add_u32 m0, s57, 0\n" \
"s_add_u32 s48, s53, 0\n" \
"buffer_load_dwordx4 v0, s[40:43], s48 offen lds\n" \
"s_add_u32 m0, s57, 4096\n" \
"s_add_u32 s48, s53, 4096\n" \
"buffer_load_dwordx4 v0, s[40:43], s48 offen lds\n" \
"s_add_u32 m0, s57, 8192\n" \
"s_add_u32 s48, s53, 16384\n" \
"buffer_load_dwordx4 v0, s[40:43], s48 offen lds\n" \
"s_add_u32 m0, s57, 12288\n" \
"s_add_u32 s48, s53, 20480\n" \
"buffer_load_dwordx4 v0, s[40:43], s48 offen lds\n" \
"s_add_u32 m0, s57, 16384\n" \
"s_add_u32 s48, s53, 32768\n" \
"buffer_load_dwordx4 v0, s[40:43], s48 offen lds\n" \
"s_add_u32 m0, s57, 20480\n" \
"s_add_u32 s48, s53, 36864\n" \
"buffer_load_dwordx4 v0, s[40:43], s48 offen lds\n" \
"s_add_u32 m0, s57, 24576\n" \
"s_add_u32 s48, s53, 49152\n" \
"buffer_load_dwordx4 v0, s[40:43], s48 offen lds\n" \
"s_add_u32 m0, s57, 28672\n" \
"s_add_u32 s48, s53, 53248\n" \
"buffer_load_dwordx4 v0, s[40:43], s48 offen lds\n" \
"s_add_u32 m0, s57, 32768\n" \
"s_add_u32 s48, s53, 65536\n" \
"buffer_load_dwordx4 v0, s[40:43], s48 offen lds\n" \
"s_add_u32 m0, s57, 36864\n" \
"s_add_u32 s48, s53, 69632\n" \
"buffer_load_dwordx4 v0, s[40:43], s48 offen lds\n" \
"v_mov_b32 v8, 0xff81ff81\n" \
"v_mov_b32 v9, 0xff81ff81\n" \
"v_mov_b32 v10, 0xff81ff81\n" \
"v_mov_b32 v11, 0xff81ff81\n" \
"v_mov_b32 v3, v0\n" \
"s_mov_b32 s54, 0x7f000000\n" \
"s_waitcnt vmcnt(0)\n" \
"s_barrier\n" \
"ds_read_b128 v[48:51], v3 offset:0\n" \
"ds_read_b128 v[52:55], v3 offset:20480\n" \
"ds_read_b128 v[56:59], v3 offset:1024\n" \
"ds_read_b128 v[60:63], v3 offset:21504\n" \
"s_bitcmp1_b32 s33, 1\n" \
"s_cbranch_scc1 KVL_%=\n" \
"KKL_%=:\n" \
"s_waitcnt lgkmcnt(2)\n" \
"v_mfma_f32_16x16x32_bf16 v[16:19], a[0:3], v[48:51], 0\n" \
"ds_read_b128 v[64:67], v3 offset:2048\n" \
"v_cvt_pk_bf16_f32 v92, v36, v37\n" \
"v_mfma_f32_16x16x32_bf16 v[20:23], a[0:3], v[52:55], 0\n" \
"ds_read_b128 v[68:71], v3 offset:22528\n" \
"v_cvt_pk_bf16_f32 v93, v38, v39\n" \
"v_mfma_f32_16x16x32_bf16 v[24:27], a[4:7], v[48:51], 0\n" \
"v_cvt_pk_bf16_f32 v86, v40, v41\n" \
"v_cvt_pk_bf16_f32 v87, v42, v43\n" \
"v_mfma_f32_16x16x32_bf16 v[28:31], a[4:7], v[52:55], 0\n" \
"v_cvt_pk_bf16_f32 v94, v44, v45\n" \
"v_cvt_pk_bf16_f32 v95, v46, v47\n" \
"v_mfma_f32_16x16x32_bf16 v[32:35], a[8:11], v[48:51], 0\n" \
"s_bitcmp1_b64 %[dpar], s35\n" \
"s_cselect_b32 s69, 0x2000, 0\n" \
"v_mfma_f32_16x16x32_bf16 v[36:39], a[8:11], v[52:55], 0\n" \
"s_add_u32 s53, s53, 0x200000\n" \
"s_xor_b32 s53, s53, s69\n" \
"s_add_u32 s48, s54, 0\n" \
"buffer_store_dwordx4 v[80:83], v0, s[40:43], s48 offen offset:0 sc1\n" \
"v_mfma_f32_16x16x32_bf16 v[40:43], a[12:15], v[48:51], 0\n" \
"s_xor_b32 s57, s57, 40960\n" \
"v_mfma_f32_16x16x32_bf16 v[44:47], a[12:15], v[52:55], 0\n" \
"s_waitcnt lgkmcnt(2)\n" \
"v_mfma_f32_16x16x32_bf16 v[16:19], a[16:19], v[56:59], v[16:19]\n" \
"ds_read_b128 v[72:75], v3 offset:3072\n" \
"v_mfma_f32_16x16x32_bf16 v[20:23], a[16:19], v[60:63], v[20:23]\n" \
"ds_read_b128 v[76:79], v3 offset:23552\n" \
"s_add_u32 s48, s54, 0\n" \
"buffer_store_dwordx4 v[84:87], v0, s[40:43], s48 offen offset:1024 sc1\n" \
"v_mfma_f32_16x16x32_bf16 v[24:27], a[20:23], v[56:59], v[24:27]\n" \
"v_mfma_f32_16x16x32_bf16 v[28:31], a[20:23], v[60:63], v[28:31]\n" \
"v_mfma_f32_16x16x32_bf16 v[32:35], a[24:27], v[56:59], v[32:35]\n" \
"v_mfma_f32_16x16x32_bf16 v[36:39], a[24:27], v[60:63], v[36:39]\n" \
"s_add_u32 s48, s54, 4096\n" \
"buffer_store_dwordx4 v[88:91], v0, s[40:43], s48 offen offset:0 sc1\n" \
"v_mfma_f32_16x16x32_bf16 v[40:43], a[28:31], v[56:59], v[40:43]\n" \
"v_mfma_f32_16x16x32_bf16 v[44:47], a[28:31], v[60:63], v[44:47]\n" \
"s_waitcnt lgkmcnt(2)\n" \
"v_mfma_f32_16x16x32_bf16 v[16:19], a[32:35], v[64:67], v[16:19]\n" \
"ds_read_b128 v[48:51], v3 offset:4096\n" \
"v_mfma_f32_16x16x32_bf16 v[20:23], a[32:35], v[68:71], v[20:23]\n" \
"ds_read_b128 v[52:55], v3 offset:24576\n" \
"s_add_u32 s48, s54, 4096\n" \
"buffer_store_dwordx4 v[92:95], v0, s[40:43], s48 offen offset:1024 sc1\n" \
"v_mfma_f32_16x16x32_bf16 v[24:27], a[36:39], v[64:67], v[24:27]\n" \
"v_mfma_f32_16x16x32_bf16 v[28:31], a[36:39], v[68:71], v[28:31]\n" \
"v_mfma_f32_16x16x32_bf16 v[32:35], a[40:43], v[64:67], v[32:35]\n" \
"s_add_u32 m0, s57, 0\n" \
"s_add_u32 s48, s53, 0\n" \
"v_mfma_f32_16x16x32_bf16 v[36:39], a[40:43], v[68:71], v[36:39]\n" \
"buffer_load_dwordx4 v0, s[40:43], s48 offen lds\n" \
"v_mfma_f32_16x16x32_bf16 v[40:43], a[44:47], v[64:67], v[40:43]\n" \
"v_mfma_f32_16x16x32_bf16 v[44:47], a[44:47], v[68:71], v[44:47]\n" \
"s_waitcnt lgkmcnt(2)\n" \
"v_mfma_f32_16x16x32_bf16 v[16:19], a[48:51], v[72:75], v[16:19]\n" \
"ds_read_b128 v[56:59], v3 offset:5120\n" \
"v_mfma_f32_16x16x32_bf16 v[20:23], a[48:51], v[76:79], v[20:23]\n" \
"ds_read_b128 v[60:63], v3 offset:25600\n" \
"v_mfma_f32_16x16x32_bf16 v[24:27], a[52:55], v[72:75], v[24:27]\n" \
"v_mfma_f32_16x16x32_bf16 v[28:31], a[52:55], v[76:79], v[28:31]\n" \
"v_mfma_f32_16x16x32_bf16 v[32:35], a[56:59], v[72:75], v[32:35]\n" \
"s_add_u32 m0, s57, 4096\n" \
"s_add_u32 s48, s53, 4096\n" \
"v_mfma_f32_16x16x32_bf16 v[36:39], a[56:59], v[76:79], v[36:39]\n" \
"buffer_load_dwordx4 v0, s[40:43], s48 offen lds\n" \
"v_mfma_f32_16x16x32_bf16 v[40:43], a[60:63], v[72:75], v[40:43]\n" \
"v_mfma_f32_16x16x32_bf16 v[44:47], a[60:63], v[76:79], v[44:47]\n" \
"s_waitcnt lgkmcnt(2)\n" \
"v_mfma_f32_16x16x32_bf16 v[16:19], a[64:67], v[48:51], v[16:19]\n" \
"ds_read_b128 v[64:67], v3 offset:6144\n" \
"v_mfma_f32_16x16x32_bf16 v[20:23], a[64:67], v[52:55], v[20:23]\n" \
"ds_read_b128 v[68:71], v3 offset:26624\n" \
"v_mfma_f32_16x16x32_bf16 v[24:27], a[68:71], v[48:51], v[24:27]\n" \
"v_mfma_f32_16x16x32_bf16 v[28:31], a[68:71], v[52:55], v[28:31]\n" \
"v_mfma_f32_16x16x32_bf16 v[32:35], a[72:75], v[48:51], v[32:35]\n" \
"s_add_u32 m0, s57, 8192\n" \
"s_add_u32 s48, s53, 16384\n" \
"v_mfma_f32_16x16x32_bf16 v[36:39], a[72:75], v[52:55], v[36:39]\n" \
"buffer_load_dwordx4 v0, s[40:43], s48 offen lds\n" \
"v_mfma_f32_16x16x32_bf16 v[40:43], a[76:79], v[48:51], v[40:43]\n" \
"v_mfma_f32_16x16x32_bf16 v[44:47], a[76:79], v[52:55], v[44:47]\n" \
"s_waitcnt lgkmcnt(2)\n" \
"v_mfma_f32_16x16x32_bf16 v[16:19], a[80:83], v[56:59], v[16:19]\n" \
"ds_read_b128 v[72:75], v3 offset:7168\n" \
"v_mfma_f32_16x16x32_bf16 v[20:23], a[80:83], v[60:63], v[20:23]\n" \
"ds_read_b128 v[76:79], v3 offset:27648\n" \
"v_mfma_f32_16x16x32_bf16 v[24:27], a[84:87], v[56:59], v[24:27]\n" \
"v_mfma_f32_16x16x32_bf16 v[28:31], a[84:87], v[60:63], v[28:31]\n" \
"v_mfma_f32_16x16x32_bf16 v[32:35], a[88:91], v[56:59], v[32:35]\n" \
"s_add_u32 m0, s57, 12288\n" \
"s_add_u32 s48, s53, 20480\n" \
"v_mfma_f32_16x16x32_bf16 v[36:39], a[88:91], v[60:63], v[36:39]\n" \
"buffer_load_dwordx4 v0, s[40:43], s48 offen lds\n" \
"v_mfma_f32_16x16x32_bf16 v[40:43], a[92:95], v[56:59], v[40:43]\n" \
"v_mfma_f32_16x16x32_bf16 v[44:47], a[92:95], v[60:63], v[44:47]\n" \
"s_waitcnt lgkmcnt(2)\n" \
"v_mfma_f32_16x16x32_bf16 v[16:19], a[96:99], v[64:67], v[16:19]\n" \
"ds_read_b128 v[48:51], v3 offset:8192\n" \
"v_mfma_f32_16x16x32_bf16 v[20:23], a[96:99], v[68:71], v[20:23]\n" \
"ds_read_b128 v[52:55], v3 offset:28672\n" \
"v_mfma_f32_16x16x32_bf16 v[24:27], a[100:103], v[64:67], v[24:27]\n" \
"v_mfma_f32_16x16x32_bf16 v[28:31], a[100:103], v[68:71], v[28:31]\n" \
"v_mfma_f32_16x16x32_bf16 v[32:35], a[104:107], v[64:67], v[32:35]\n" \
"s_add_u32 m0, s57, 16384\n" \
"s_add_u32 s48, s53, 32768\n" \
"v_mfma_f32_16x16x32_bf16 v[36:39], a[104:107], v[68:71], v[36:39]\n" \
"buffer_load_dwordx4 v0, s[40:43], s48 offen lds\n" \
"v_mfma_f32_16x16x32_bf16 v[40:43], a[108:111], v[64:67], v[40:43]\n" \
"v_mfma_f32_16x16x32_bf16 v[44:47], a[108:111], v[68:71], v[44:47]\n" \
"s_waitcnt lgkmcnt(2)\n" \
"v_mfma_f32_16x16x32_bf16 v[16:19], a[112:115], v[72:75], v[16:19]\n" \
"ds_read_b128 v[56:59], v3 offset:9216\n" \
"v_mfma_f32_16x16x32_bf16 v[20:23], a[112:115], v[76:79], v[20:23]\n" \
"ds_read_b128 v[60:63], v3 offset:29696\n" \
"v_mfma_f32_16x16x32_bf16 v[24:27], a[116:119], v[72:75], v[24:27]\n" \
"v_mfma_f32_16x16x32_bf16 v[28:31], a[116:119], v[76:79], v[28:31]\n" \
"v_mfma_f32_16x16x32_bf16 v[32:35], a[120:123], v[72:75], v[32:35]\n" \
"s_add_u32 m0, s57, 20480\n" \
"s_add_u32 s48, s53, 36864\n" \
"v_mfma_f32_16x16x32_bf16 v[36:39], a[120:123], v[76:79], v[36:39]\n" \
"buffer_load_dwordx4 v0, s[40:43], s48 offen lds\n" \
"v_mfma_f32_16x16x32_bf16 v[40:43], a[124:127], v[72:75], v[40:43]\n" \
"v_mfma_f32_16x16x32_bf16 v[44:47], a[124:127], v[76:79], v[44:47]\n" \
"s_waitcnt lgkmcnt(2)\n" \
"v_mfma_f32_16x16x32_bf16 v[16:19], a[128:131], v[48:51], v[16:19]\n" \
"ds_read_b128 v[64:67], v3 offset:10240\n" \
"v_mfma_f32_16x16x32_bf16 v[20:23], a[128:131], v[52:55], v[20:23]\n" \
"ds_read_b128 v[68:71], v3 offset:30720\n" \
"v_mfma_f32_16x16x32_bf16 v[24:27], a[132:135], v[48:51], v[24:27]\n" \
"v_mfma_f32_16x16x32_bf16 v[28:31], a[132:135], v[52:55], v[28:31]\n" \
"v_mfma_f32_16x16x32_bf16 v[32:35], a[136:139], v[48:51], v[32:35]\n" \
"s_add_u32 m0, s57, 24576\n" \
"s_add_u32 s48, s53, 49152\n" \
"v_mfma_f32_16x16x32_bf16 v[36:39], a[136:139], v[52:55], v[36:39]\n" \
"buffer_load_dwordx4 v0, s[40:43], s48 offen lds\n" \
"v_mfma_f32_16x16x32_bf16 v[40:43], a[140:143], v[48:51], v[40:43]\n" \
"v_mfma_f32_16x16x32_bf16 v[44:47], a[140:143], v[52:55], v[44:47]\n" \
"s_waitcnt lgkmcnt(2)\n" \
"v_mfma_f32_16x16x32_bf16 v[16:19], a[144:147], v[56:59], v[16:19]\n" \
"ds_read_b128 v[72:75], v3 offset:11264\n" \
"v_mfma_f32_16x16x32_bf16 v[20:23], a[144:147], v[60:63], v[20:23]\n" \
"ds_read_b128 v[76:79], v3 offset:31744\n" \
"s_waitcnt vmcnt(7)\n" \
"s_barrier\n" \
"v_mfma_f32_16x16x32_bf16 v[24:27], a[148:151], v[56:59], v[24:27]\n" \
"v_mov_b32 v12, s35\n" \
"s_mov_b64 exec, 3\n" \
"buffer_store_dword v12, v2, s[40:43], s51 offen sc1\n" \
"s_mov_b64 exec, -1\n" \
"v_mfma_f32_16x16x32_bf16 v[28:31], a[148:151], v[60:63], v[28:31]\n" \
"v_mfma_f32_16x16x32_bf16 v[32:35], a[152:155], v[56:59], v[32:35]\n" \
"s_add_u32 m0, s57, 28672\n" \
"s_add_u32 s48, s53, 53248\n" \
"v_mfma_f32_16x16x32_bf16 v[36:39], a[152:155], v[60:63], v[36:39]\n" \
"buffer_load_dwordx4 v0, s[40:43], s48 offen lds\n" \
"v_mfma_f32_16x16x32_bf16 v[40:43], a[156:159], v[56:59], v[40:43]\n" \
"v_mfma_f32_16x16x32_bf16 v[44:47], a[156:159], v[60:63], v[44:47]\n" \
"s_waitcnt lgkmcnt(2)\n" \
"v_mfma_f32_16x16x32_bf16 v[16:19], a[160:163], v[64:67], v[16:19]\n" \
"ds_read_b128 v[48:51], v3 offset:12288\n" \
"v_mfma_f32_16x16x32_bf16 v[20:23], a[160:163], v[68:71], v[20:23]\n" \
"ds_read_b128 v[52:55], v3 offset:32768\n" \
"v_mfma_f32_16x16x32_bf16 v[24:27], a[164:167], v[64:67], v[24:27]\n" \
"v_mfma_f32_16x16x32_bf16 v[28:31], a[164:167], v[68:71], v[28:31]\n" \
"v_mfma_f32_16x16x32_bf16 v[32:35], a[168:171], v[64:67], v[32:35]\n" \
"s_add_u32 m0, s57, 32768\n" \
"s_add_u32 s48, s53, 65536\n" \
"v_mfma_f32_16x16x32_bf16 v[36:39], a[168:171], v[68:71], v[36:39]\n" \
"buffer_load_dwordx4 v0, s[40:43], s48 offen lds\n" \
"v_mfma_f32_16x16x32_bf16 v[40:43], a[172:175], v[64:67], v[40:43]\n" \
"v_mfma_f32_16x16x32_bf16 v[44:47], a[172:175], v[68:71], v[44:47]\n" \
"s_waitcnt lgkmcnt(2)\n" \
"v_mfma_f32_16x16x32_bf16 v[16:19], a[176:179], v[72:75], v[16:19]\n" \
"ds_read_b128 v[56:59], v3 offset:13312\n" \
"v_mfma_f32_16x16x32_bf16 v[20:23], a[176:179], v[76:79], v[20:23]\n" \
"ds_read_b128 v[60:63], v3 offset:33792\n" \
"v_mfma_f32_16x16x32_bf16 v[24:27], a[180:183], v[72:75], v[24:27]\n" \
"v_mfma_f32_16x16x32_bf16 v[28:31], a[180:183], v[76:79], v[28:31]\n" \
"v_mfma_f32_16x16x32_bf16 v[32:35], a[184:187], v[72:75], v[32:35]\n" \
"s_add_u32 m0, s57, 36864\n" \
"s_add_u32 s48, s53, 69632\n" \
"v_mfma_f32_16x16x32_bf16 v[36:39], a[184:187], v[76:79], v[36:39]\n" \
"buffer_load_dwordx4 v0, s[40:43], s48 offen lds\n" \
"v_mfma_f32_16x16x32_bf16 v[40:43], a[188:191], v[72:75], v[40:43]\n" \
"v_mfma_f32_16x16x32_bf16 v[44:47], a[188:191], v[76:79], v[44:47]\n" \
"s_waitcnt lgkmcnt(2)\n" \
"v_mfma_f32_16x16x32_bf16 v[16:19], a[192:195], v[48:51], v[16:19]\n" \
"ds_read_b128 v[64:67], v3 offset:14336\n" \
"v_mfma_f32_16x16x32_bf16 v[20:23], a[192:195], v[52:55], v[20:23]\n" \
"ds_read_b128 v[68:71], v3 offset:34816\n" \
"v_mfma_f32_16x16x32_bf16 v[24:27], a[196:199], v[48:51], v[24:27]\n" \
"v_mfma_f32_16x16x32_bf16 v[28:31], a[196:199], v[52:55], v[28:31]\n" \
"v_mfma_f32_16x16x32_bf16 v[32:35], a[200:203], v[48:51], v[32:35]\n" \
"v_mfma_f32_16x16x32_bf16 v[36:39], a[200:203], v[52:55], v[36:39]\n" \
"v_mfma_f32_16x16x32_bf16 v[40:43], a[204:207], v[48:51], v[40:43]\n" \
"v_mfma_f32_16x16x32_bf16 v[44:47], a[204:207], v[52:55], v[44:47]\n" \
"s_waitcnt lgkmcnt(2)\n" \
"v_mfma_f32_16x16x32_bf16 v[16:19], a[208:211], v[56:59], v[16:19]\n" \
"ds_read_b128 v[72:75], v3 offset:15360\n" \
"v_mfma_f32_16x16x32_bf16 v[20:23], a[208:211], v[60:63], v[20:23]\n" \
"ds_read_b128 v[76:79], v3 offset:35840\n" \
"v_mfma_f32_16x16x32_bf16 v[24:27], a[212:215], v[56:59], v[24:27]\n" \
"v_mfma_f32_16x16x32_bf16 v[28:31], a[212:215], v[60:63], v[28:31]\n" \
"v_mfma_f32_16x16x32_bf16 v[32:35], a[216:219], v[56:59], v[32:35]\n" \
"v_mfma_f32_16x16x32_bf16 v[36:39], a[216:219], v[60:63], v[36:39]\n" \
"v_mfma_f32_16x16x32_bf16 v[40:43], a[220:223], v[56:59], v[40:43]\n" \
"v_mfma_f32_16x16x32_bf16 v[44:47], a[220:223], v[60:63], v[44:47]\n" \
"s_waitcnt lgkmcnt(2)\n" \
"v_mfma_f32_16x16x32_bf16 v[16:19], a[224:227], v[64:67], v[16:19]\n" \
"ds_read_b128 v[48:51], v3 offset:16384\n" \
"v_mfma_f32_16x16x32_bf16 v[20:23], a[224:227], v[68:71], v[20:23]\n" \
"ds_read_b128 v[52:55], v3 offset:36864\n" \
"v_mfma_f32_16x16x32_bf16 v[24:27], a[228:231], v[64:67], v[24:27]\n" \
"v_mfma_f32_16x16x32_bf16 v[28:31], a[228:231], v[68:71], v[28:31]\n" \
"buffer_store_dwordx4 v[8:11], v0, s[40:43], s55 offen offset:0 sc1\n" \
"v_mfma_f32_16x16x32_bf16 v[32:35], a[232:235], v[64:67], v[32:35]\n" \
"v_mfma_f32_16x16x32_bf16 v[36:39], a[232:235], v[68:71], v[36:39]\n" \
"v_mfma_f32_16x16x32_bf16 v[40:43], a[236:239], v[64:67], v[40:43]\n" \
"v_mfma_f32_16x16x32_bf16 v[44:47], a[236:239], v[68:71], v[44:47]\n" \
"s_waitcnt lgkmcnt(2)\n" \
"v_mfma_f32_16x16x32_bf16 v[16:19], a[240:243], v[72:75], v[16:19]\n" \
"ds_read_b128 v[56:59], v3 offset:17408\n" \
"v_mfma_f32_16x16x32_bf16 v[20:23], a[240:243], v[76:79], v[20:23]\n" \
"ds_read_b128 v[60:63], v3 offset:37888\n" \
"v_mfma_f32_16x16x32_bf16 v[24:27], a[244:247], v[72:75], v[24:27]\n" \
"v_mfma_f32_16x16x32_bf16 v[28:31], a[244:247], v[76:79], v[28:31]\n" \
"v_mfma_f32_16x16x32_bf16 v[32:35], a[248:251], v[72:75], v[32:35]\n" \
"v_mfma_f32_16x16x32_bf16 v[36:39], a[248:251], v[76:79], v[36:39]\n" \
"v_mfma_f32_16x16x32_bf16 v[40:43], a[252:255], v[72:75], v[40:43]\n" \
"v_mfma_f32_16x16x32_bf16 v[44:47], a[252:255], v[76:79], v[44:47]\n" \
"s_waitcnt lgkmcnt(2)\n" \
"v_mfma_f32_16x16x32_bf16 v[16:19], v[192:195], v[48:51], v[16:19]\n" \
"ds_read_b128 v[64:67], v3 offset:18432\n" \
"v_mfma_f32_16x16x32_bf16 v[20:23], v[192:195], v[52:55], v[20:23]\n" \
"ds_read_b128 v[68:71], v3 offset:38912\n" \
"v_mfma_f32_16x16x32_bf16 v[24:27], v[196:199], v[48:51], v[24:27]\n" \
"v_mfma_f32_16x16x32_bf16 v[28:31], v[196:199], v[52:55], v[28:31]\n" \
"buffer_store_dwordx4 v[8:11], v0, s[40:43], s55 offen offset:1024 sc1\n" \
"v_mfma_f32_16x16x32_bf16 v[32:35], v[200:203], v[48:51], v[32:35]\n" \
"v_mfma_f32_16x16x32_bf16 v[36:39], v[200:203], v[52:55], v[36:39]\n" \
"v_mfma_f32_16x16x32_bf16 v[40:43], v[204:207], v[48:51], v[40:43]\n" \
"v_mfma_f32_16x16x32_bf16 v[44:47], v[204:207], v[52:55], v[44:47]\n" \
"s_waitcnt lgkmcnt(2)\n" \
"v_mfma_f32_16x16x32_bf16 v[16:19], v[208:211], v[56:59], v[16:19]\n" \
"ds_read_b128 v[72:75], v3 offset:19456\n" \
"v_mfma_f32_16x16x32_bf16 v[20:23], v[208:211], v[60:63], v[20:23]\n" \
"ds_read_b128 v[76:79], v3 offset:39936\n" \
"v_mfma_f32_16x16x32_bf16 v[24:27], v[212:215], v[56:59], v[24:27]\n" \
"s_mov_b32 s54, s56\n" \
"s_add_u32 s56, s56, 0x200000\n" \
"s_xor_b32 s56, s56, s69\n" \
"v_mfma_f32_16x16x32_bf16 v[28:31], v[212:215], v[60:63], v[28:31]\n" \
"v_mfma_f32_16x16x32_bf16 v[32:35], v[216:219], v[56:59], v[32:35]\n" \
"s_add_u32 s55, s55, 0x200000\n" \
"s_xor_b32 s55, s55, s69\n" \
"v_mfma_f32_16x16x32_bf16 v[36:39], v[216:219], v[60:63], v[36:39]\n" \
"v_xor_b32 v3, 40960, v3\n" \
"s_add_u32 s35, s35, 1\n" \
"v_mfma_f32_16x16x32_bf16 v[40:43], v[220:223], v[56:59], v[40:43]\n" \
"v_mfma_f32_16x16x32_bf16 v[44:47], v[220:223], v[60:63], v[44:47]\n" \
"s_waitcnt vmcnt(2)\n" \
"s_waitcnt lgkmcnt(0)\n" \
"s_barrier\n" \
"v_mfma_f32_16x16x32_bf16 v[16:19], v[224:227], v[64:67], v[16:19]\n" \
"ds_read_b128 v[48:51], v3 offset:0\n" \
"v_mfma_f32_16x16x32_bf16 v[20:23], v[224:227], v[68:71], v[20:23]\n" \
"ds_read_b128 v[52:55], v3 offset:20480\n" \
"v_mfma_f32_16x16x32_bf16 v[24:27], v[228:231], v[64:67], v[24:27]\n" \
"v_mfma_f32_16x16x32_bf16 v[28:31], v[228:231], v[68:71], v[28:31]\n" \
"v_mfma_f32_16x16x32_bf16 v[32:35], v[232:235], v[64:67], v[32:35]\n" \
"v_mfma_f32_16x16x32_bf16 v[36:39], v[232:235], v[68:71], v[36:39]\n" \
"v_mfma_f32_16x16x32_bf16 v[40:43], v[236:239], v[64:67], v[40:43]\n" \
"v_mfma_f32_16x16x32_bf16 v[44:47], v[236:239], v[68:71], v[44:47]\n" \
"v_mfma_f32_16x16x32_bf16 v[16:19], v[240:243], v[72:75], v[16:19]\n" \
"ds_read_b128 v[56:59], v3 offset:1024\n" \
"v_mfma_f32_16x16x32_bf16 v[20:23], v[240:243], v[76:79], v[20:23]\n" \
"ds_read_b128 v[60:63], v3 offset:21504\n" \
"v_mfma_f32_16x16x32_bf16 v[24:27], v[244:247], v[72:75], v[24:27]\n" \
"v_mfma_f32_16x16x32_bf16 v[28:31], v[244:247], v[76:79], v[28:31]\n" \
"v_cvt_pk_bf16_f32 v80, v16, v17\n" \
"v_cvt_pk_bf16_f32 v81, v18, v19\n" \
"v_mfma_f32_16x16x32_bf16 v[32:35], v[248:251], v[72:75], v[32:35]\n" \
"v_cvt_pk_bf16_f32 v88, v20, v21\n" \
"v_cvt_pk_bf16_f32 v89, v22, v23\n" \
"v_mfma_f32_16x16x32_bf16 v[36:39], v[248:251], v[76:79], v[36:39]\n" \
"v_cvt_pk_bf16_f32 v82, v24, v25\n" \
"v_cvt_pk_bf16_f32 v83, v26, v27\n" \
"v_mfma_f32_16x16x32_bf16 v[40:43], v[252:255], v[72:75], v[40:43]\n" \
"v_cvt_pk_bf16_f32 v90, v28, v29\n" \
"v_cvt_pk_bf16_f32 v91, v30, v31\n" \
"v_mfma_f32_16x16x32_bf16 v[44:47], v[252:255], v[76:79], v[44:47]\n" \
"v_cvt_pk_bf16_f32 v84, v32, v33\n" \
"v_cvt_pk_bf16_f32 v85, v34, v35\n" \
"s_cmp_lt_u32 s35, 64\n" \
"s_cbranch_scc1 KKL_%=\n" \
"s_nop 7\n" \
"s_nop 1\n" \
"v_cvt_pk_bf16_f32 v92, v36, v37\n" \
"v_cvt_pk_bf16_f32 v93, v38, v39\n" \
"v_cvt_pk_bf16_f32 v86, v40, v41\n" \
"v_cvt_pk_bf16_f32 v87, v42, v43\n" \
"v_cvt_pk_bf16_f32 v94, v44, v45\n" \
"v_cvt_pk_bf16_f32 v95, v46, v47\n" \
"s_add_u32 s48, s54, 0\n" \
"buffer_store_dwordx4 v[80:83], v0, s[40:43], s48 offen offset:0 sc1\n" \
"s_add_u32 s48, s54, 0\n" \
"buffer_store_dwordx4 v[84:87], v0, s[40:43], s48 offen offset:1024 sc1\n" \
"s_add_u32 s48, s54, 4096\n" \
"buffer_store_dwordx4 v[88:91], v0, s[40:43], s48 offen offset:0 sc1\n" \
"s_add_u32 s48, s54, 4096\n" \
"buffer_store_dwordx4 v[92:95], v0, s[40:43], s48 offen offset:1024 sc1\n" \
"s_branch KF_%=\n" \
"KVL_%=:\n" \
"s_waitcnt lgkmcnt(2)\n" \
"v_mfma_f32_16x16x32_bf16 v[16:19], v[48:51], a[0:3], 0\n" \
"ds_read_b128 v[64:67], v3 offset:2048\n" \
"v_cvt_pk_bf16_f32 v90, v36, v37\n" \
"v_mfma_f32_16x16x32_bf16 v[20:23], v[52:55], a[0:3], 0\n" \
"ds_read_b128 v[68:71], v3 offset:22528\n" \
"v_cvt_pk_bf16_f32 v91, v38, v39\n" \
"v_mfma_f32_16x16x32_bf16 v[24:27], v[48:51], a[4:7], 0\n" \
"v_cvt_pk_bf16_f32 v92, v40, v41\n" \
"v_cvt_pk_bf16_f32 v93, v42, v43\n" \
"v_mfma_f32_16x16x32_bf16 v[28:31], v[52:55], a[4:7], 0\n" \
"v_cvt_pk_bf16_f32 v94, v44, v45\n" \
"v_cvt_pk_bf16_f32 v95, v46, v47\n" \
"v_mfma_f32_16x16x32_bf16 v[32:35], v[48:51], a[8:11], 0\n" \
"s_bitcmp1_b64 %[dpar], s35\n" \
"s_cselect_b32 s69, 0x2000, 0\n" \
"v_mfma_f32_16x16x32_bf16 v[36:39], v[52:55], a[8:11], 0\n" \
"s_add_u32 s53, s53, 0x200000\n" \
"s_xor_b32 s53, s53, s69\n" \
"buffer_store_dwordx4 v[80:83], v0, s[40:43], s54 offen offset:0 sc1\n" \
"v_mfma_f32_16x16x32_bf16 v[40:43], v[48:51], a[12:15], 0\n" \
"s_xor_b32 s57, s57, 40960\n" \
"v_mfma_f32_16x16x32_bf16 v[44:47], v[52:55], a[12:15], 0\n" \
"s_waitcnt lgkmcnt(2)\n" \
"v_mfma_f32_16x16x32_bf16 v[16:19], v[56:59], a[16:19], v[16:19]\n" \
"ds_read_b128 v[72:75], v3 offset:3072\n" \
"v_mfma_f32_16x16x32_bf16 v[20:23], v[60:63], a[16:19], v[20:23]\n" \
"ds_read_b128 v[76:79], v3 offset:23552\n" \
"buffer_store_dwordx4 v[84:87], v0, s[40:43], s54 offen offset:1024 sc1\n" \
"v_mfma_f32_16x16x32_bf16 v[24:27], v[56:59], a[20:23], v[24:27]\n" \
"v_mfma_f32_16x16x32_bf16 v[28:31], v[60:63], a[20:23], v[28:31]\n" \
"v_mfma_f32_16x16x32_bf16 v[32:35], v[56:59], a[24:27], v[32:35]\n" \
"v_mfma_f32_16x16x32_bf16 v[36:39], v[60:63], a[24:27], v[36:39]\n" \
"buffer_store_dwordx4 v[88:91], v0, s[40:43], s54 offen offset:2048 sc1\n" \
"v_mfma_f32_16x16x32_bf16 v[40:43], v[56:59], a[28:31], v[40:43]\n" \
"v_mfma_f32_16x16x32_bf16 v[44:47], v[60:63], a[28:31], v[44:47]\n" \
"s_waitcnt lgkmcnt(2)\n" \
"v_mfma_f32_16x16x32_bf16 v[16:19], v[64:67], a[32:35], v[16:19]\n" \
"ds_read_b128 v[48:51], v3 offset:4096\n" \
"v_mfma_f32_16x16x32_bf16 v[20:23], v[68:71], a[32:35], v[20:23]\n" \
"ds_read_b128 v[52:55], v3 offset:24576\n" \
"buffer_store_dwordx4 v[92:95], v0, s[40:43], s54 offen offset:3072 sc1\n" \
"v_mfma_f32_16x16x32_bf16 v[24:27], v[64:67], a[36:39], v[24:27]\n" \
"v_mfma_f32_16x16x32_bf16 v[28:31], v[68:71], a[36:39], v[28:31]\n" \
"v_mfma_f32_16x16x32_bf16 v[32:35], v[64:67], a[40:43], v[32:35]\n" \
"s_add_u32 m0, s57, 0\n" \
"s_add_u32 s48, s53, 0\n" \
"v_mfma_f32_16x16x32_bf16 v[36:39], v[68:71], a[40:43], v[36:39]\n" \
"buffer_load_dwordx4 v0, s[40:43], s48 offen lds\n" \
"v_mfma_f32_16x16x32_bf16 v[40:43], v[64:67], a[44:47], v[40:43]\n" \
"v_mfma_f32_16x16x32_bf16 v[44:47], v[68:71], a[44:47], v[44:47]\n" \
"s_waitcnt lgkmcnt(2)\n" \
"v_mfma_f32_16x16x32_bf16 v[16:19], v[72:75], a[48:51], v[16:19]\n" \
"ds_read_b128 v[56:59], v3 offset:5120\n" \
"v_mfma_f32_16x16x32_bf16 v[20:23], v[76:79], a[48:51], v[20:23]\n" \
"ds_read_b128 v[60:63], v3 offset:25600\n" \
"v_mfma_f32_16x16x32_bf16 v[24:27], v[72:75], a[52:55], v[24:27]\n" \
"v_mfma_f32_16x16x32_bf16 v[28:31], v[76:79], a[52:55], v[28:31]\n" \
"v_mfma_f32_16x16x32_bf16 v[32:35], v[72:75], a[56:59], v[32:35]\n" \
"s_add_u32 m0, s57, 4096\n" \
"s_add_u32 s48, s53, 4096\n" \
"v_mfma_f32_16x16x32_bf16 v[36:39], v[76:79], a[56:59], v[36:39]\n" \
"buffer_load_dwordx4 v0, s[40:43], s48 offen lds\n" \
"v_mfma_f32_16x16x32_bf16 v[40:43], v[72:75], a[60:63], v[40:43]\n" \
"v_mfma_f32_16x16x32_bf16 v[44:47], v[76:79], a[60:63], v[44:47]\n" \
"s_waitcnt lgkmcnt(2)\n" \
"v_mfma_f32_16x16x32_bf16 v[16:19], v[48:51], a[64:67], v[16:19]\n" \
"ds_read_b128 v[64:67], v3 offset:6144\n" \
"v_mfma_f32_16x16x32_bf16 v[20:23], v[52:55], a[64:67], v[20:23]\n" \
"ds_read_b128 v[68:71], v3 offset:26624\n" \
"v_mfma_f32_16x16x32_bf16 v[24:27], v[48:51], a[68:71], v[24:27]\n" \
"v_mfma_f32_16x16x32_bf16 v[28:31], v[52:55], a[68:71], v[28:31]\n" \
"v_mfma_f32_16x16x32_bf16 v[32:35], v[48:51], a[72:75], v[32:35]\n" \
"s_add_u32 m0, s57, 8192\n" \
"s_add_u32 s48, s53, 16384\n" \
"v_mfma_f32_16x16x32_bf16 v[36:39], v[52:55], a[72:75], v[36:39]\n" \
"buffer_load_dwordx4 v0, s[40:43], s48 offen lds\n" \
"v_mfma_f32_16x16x32_bf16 v[40:43], v[48:51], a[76:79], v[40:43]\n" \
"v_mfma_f32_16x16x32_bf16 v[44:47], v[52:55], a[76:79], v[44:47]\n" \
"s_waitcnt lgkmcnt(2)\n" \
"v_mfma_f32_16x16x32_bf16 v[16:19], v[56:59], a[80:83], v[16:19]\n" \
"ds_read_b128 v[72:75], v3 offset:7168\n" \
"v_mfma_f32_16x16x32_bf16 v[20:23], v[60:63], a[80:83], v[20:23]\n" \
"ds_read_b128 v[76:79], v3 offset:27648\n" \
"v_mfma_f32_16x16x32_bf16 v[24:27], v[56:59], a[84:87], v[24:27]\n" \
"v_mfma_f32_16x16x32_bf16 v[28:31], v[60:63], a[84:87], v[28:31]\n" \
"v_mfma_f32_16x16x32_bf16 v[32:35], v[56:59], a[88:91], v[32:35]\n" \
"s_add_u32 m0, s57, 12288\n" \
"s_add_u32 s48, s53, 20480\n" \
"v_mfma_f32_16x16x32_bf16 v[36:39], v[60:63], a[88:91], v[36:39]\n" \
"buffer_load_dwordx4 v0, s[40:43], s48 offen lds\n" \
"v_mfma_f32_16x16x32_bf16 v[40:43], v[56:59], a[92:95], v[40:43]\n" \
"v_mfma_f32_16x16x32_bf16 v[44:47], v[60:63], a[92:95], v[44:47]\n" \
"s_waitcnt lgkmcnt(2)\n" \
"v_mfma_f32_16x16x32_bf16 v[16:19], v[64:67], a[96:99], v[16:19]\n" \
"ds_read_b128 v[48:51], v3 offset:8192\n" \
"v_mfma_f32_16x16x32_bf16 v[20:23], v[68:71], a[96:99], v[20:23]\n" \
"ds_read_b128 v[52:55], v3 offset:28672\n" \
"v_mfma_f32_16x16x32_bf16 v[24:27], v[64:67], a[100:103], v[24:27]\n" \
"v_mfma_f32_16x16x32_bf16 v[28:31], v[68:71], a[100:103], v[28:31]\n" \
"v_mfma_f32_16x16x32_bf16 v[32:35], v[64:67], a[104:107], v[32:35]\n" \
"s_add_u32 m0, s57, 16384\n" \
"s_add_u32 s48, s53, 32768\n" \
"v_mfma_f32_16x16x32_bf16 v[36:39], v[68:71], a[104:107], v[36:39]\n" \
"buffer_load_dwordx4 v0, s[40:43], s48 offen lds\n" \
"v_mfma_f32_16x16x32_bf16 v[40:43], v[64:67], a[108:111], v[40:43]\n" \
"v_mfma_f32_16x16x32_bf16 v[44:47], v[68:71], a[108:111], v[44:47]\n" \
"s_waitcnt lgkmcnt(2)\n" \
"v_mfma_f32_16x16x32_bf16 v[16:19], v[72:75], a[112:115], v[16:19]\n" \
"ds_read_b128 v[56:59], v3 offset:9216\n" \
"v_mfma_f32_16x16x32_bf16 v[20:23], v[76:79], a[112:115], v[20:23]\n" \
"ds_read_b128 v[60:63], v3 offset:29696\n" \
"v_mfma_f32_16x16x32_bf16 v[24:27], v[72:75], a[116:119], v[24:27]\n" \
"v_mfma_f32_16x16x32_bf16 v[28:31], v[76:79], a[116:119], v[28:31]\n" \
"v_mfma_f32_16x16x32_bf16 v[32:35], v[72:75], a[120:123], v[32:35]\n" \
"s_add_u32 m0, s57, 20480\n" \
"s_add_u32 s48, s53, 36864\n" \
"v_mfma_f32_16x16x32_bf16 v[36:39], v[76:79], a[120:123], v[36:39]\n" \
"buffer_load_dwordx4 v0, s[40:43], s48 offen lds\n" \
"v_mfma_f32_16x16x32_bf16 v[40:43], v[72:75], a[124:127], v[40:43]\n" \
"v_mfma_f32_16x16x32_bf16 v[44:47], v[76:79], a[124:127], v[44:47]\n" \
"s_waitcnt lgkmcnt(2)\n" \
"v_mfma_f32_16x16x32_bf16 v[16:19], v[48:51], a[128:131], v[16:19]\n" \
"ds_read_b128 v[64:67], v3 offset:10240\n" \
"v_mfma_f32_16x16x32_bf16 v[20:23], v[52:55], a[128:131], v[20:23]\n" \
"ds_read_b128 v[68:71], v3 offset:30720\n" \
"v_mfma_f32_16x16x32_bf16 v[24:27], v[48:51], a[132:135], v[24:27]\n" \
"v_mfma_f32_16x16x32_bf16 v[28:31], v[52:55], a[132:135], v[28:31]\n" \
"v_mfma_f32_16x16x32_bf16 v[32:35], v[48:51], a[136:139], v[32:35]\n" \
"s_add_u32 m0, s57, 24576\n" \
"s_add_u32 s48, s53, 49152\n" \
"v_mfma_f32_16x16x32_bf16 v[36:39], v[52:55], a[136:139], v[36:39]\n" \
"buffer_load_dwordx4 v0, s[40:43], s48 offen lds\n" \
"v_mfma_f32_16x16x32_bf16 v[40:43], v[48:51], a[140:143], v[40:43]\n" \
"v_mfma_f32_16x16x32_bf16 v[44:47], v[52:55], a[140:143], v[44:47]\n" \
"s_waitcnt lgkmcnt(2)\n" \
"v_mfma_f32_16x16x32_bf16 v[16:19], v[56:59], a[144:147], v[16:19]\n" \
"ds_read_b128 v[72:75], v3 offset:11264\n" \
"v_mfma_f32_16x16x32_bf16 v[20:23], v[60:63], a[144:147], v[20:23]\n" \
"ds_read_b128 v[76:79], v3 offset:31744\n" \
"s_waitcnt vmcnt(7)\n" \
"s_barrier\n" \
"v_mfma_f32_16x16x32_bf16 v[24:27], v[56:59], a[148:151], v[24:27]\n" \
"v_mov_b32 v12, s35\n" \
"s_mov_b64 exec, 3\n" \
"buffer_store_dword v12, v2, s[40:43], s51 offen sc1\n" \
"s_mov_b64 exec, -1\n" \
"v_mfma_f32_16x16x32_bf16 v[28:31], v[60:63], a[148:151], v[28:31]\n" \
"v_mfma_f32_16x16x32_bf16 v[32:35], v[56:59], a[152:155], v[32:35]\n" \
"s_add_u32 m0, s57, 28672\n" \
"s_add_u32 s48, s53, 53248\n" \
"v_mfma_f32_16x16x32_bf16 v[36:39], v[60:63], a[152:155], v[36:39]\n" \
"buffer_load_dwordx4 v0, s[40:43], s48 offen lds\n" \
"v_mfma_f32_16x16x32_bf16 v[40:43], v[56:59], a[156:159], v[40:43]\n" \
"v_mfma_f32_16x16x32_bf16 v[44:47], v[60:63], a[156:159], v[44:47]\n" \
"s_waitcnt lgkmcnt(2)\n" \
"v_mfma_f32_16x16x32_bf16 v[16:19], v[64:67], a[160:163], v[16:19]\n" \
"ds_read_b128 v[48:51], v3 offset:12288\n" \
"v_mfma_f32_16x16x32_bf16 v[20:23], v[68:71], a[160:163], v[20:23]\n" \
"ds_read_b128 v[52:55], v3 offset:32768\n" \
"v_mfma_f32_16x16x32_bf16 v[24:27], v[64:67], a[164:167], v[24:27]\n" \
"v_mfma_f32_16x16x32_bf16 v[28:31], v[68:71], a[164:167], v[28:31]\n" \
"v_mfma_f32_16x16x32_bf16 v[32:35], v[64:67], a[168:171], v[32:35]\n" \
"s_add_u32 m0, s57, 32768\n" \
"s_add_u32 s48, s53, 65536\n" \
"v_mfma_f32_16x16x32_bf16 v[36:39], v[68:71], a[168:171], v[36:39]\n" \
"buffer_load_dwordx4 v0, s[40:43], s48 offen lds\n" \
"v_mfma_f32_16x16x32_bf16 v[40:43], v[64:67], a[172:175], v[40:43]\n" \
"v_mfma_f32_16x16x32_bf16 v[44:47], v[68:71], a[172:175], v[44:47]\n" \
"s_waitcnt lgkmcnt(2)\n" \
"v_mfma_f32_16x16x32_bf16 v[16:19], v[72:75], a[176:179], v[16:19]\n" \
"ds_read_b128 v[56:59], v3 offset:13312\n" \
"v_mfma_f32_16x16x32_bf16 v[20:23], v[76:79], a[176:179], v[20:23]\n" \
"ds_read_b128 v[60:63], v3 offset:33792\n" \
"v_mfma_f32_16x16x32_bf16 v[24:27], v[72:75], a[180:183], v[24:27]\n" \
"v_mfma_f32_16x16x32_bf16 v[28:31], v[76:79], a[180:183], v[28:31]\n" \
"v_mfma_f32_16x16x32_bf16 v[32:35], v[72:75], a[184:187], v[32:35]\n" \
"s_add_u32 m0, s57, 36864\n" \
"s_add_u32 s48, s53, 69632\n" \
"v_mfma_f32_16x16x32_bf16 v[36:39], v[76:79], a[184:187], v[36:39]\n" \
"buffer_load_dwordx4 v0, s[40:43], s48 offen lds\n" \
"v_mfma_f32_16x16x32_bf16 v[40:43], v[72:75], a[188:191], v[40:43]\n" \
"v_mfma_f32_16x16x32_bf16 v[44:47], v[76:79], a[188:191], v[44:47]\n" \
"s_waitcnt lgkmcnt(2)\n" \
"v_mfma_f32_16x16x32_bf16 v[16:19], v[48:51], a[192:195], v[16:19]\n" \
"ds_read_b128 v[64:67], v3 offset:14336\n" \
"v_mfma_f32_16x16x32_bf16 v[20:23], v[52:55], a[192:195], v[20:23]\n" \
"ds_read_b128 v[68:71], v3 offset:34816\n" \
"v_mfma_f32_16x16x32_bf16 v[24:27], v[48:51], a[196:199], v[24:27]\n" \
"v_mfma_f32_16x16x32_bf16 v[28:31], v[52:55], a[196:199], v[28:31]\n" \
"v_mfma_f32_16x16x32_bf16 v[32:35], v[48:51], a[200:203], v[32:35]\n" \
"v_mfma_f32_16x16x32_bf16 v[36:39], v[52:55], a[200:203], v[36:39]\n" \
"v_mfma_f32_16x16x32_bf16 v[40:43], v[48:51], a[204:207], v[40:43]\n" \
"v_mfma_f32_16x16x32_bf16 v[44:47], v[52:55], a[204:207], v[44:47]\n" \
"s_waitcnt lgkmcnt(2)\n" \
"v_mfma_f32_16x16x32_bf16 v[16:19], v[56:59], a[208:211], v[16:19]\n" \
"ds_read_b128 v[72:75], v3 offset:15360\n" \
"v_mfma_f32_16x16x32_bf16 v[20:23], v[60:63], a[208:211], v[20:23]\n" \
"ds_read_b128 v[76:79], v3 offset:35840\n" \
"v_mfma_f32_16x16x32_bf16 v[24:27], v[56:59], a[212:215], v[24:27]\n" \
"v_mfma_f32_16x16x32_bf16 v[28:31], v[60:63], a[212:215], v[28:31]\n" \
"v_mfma_f32_16x16x32_bf16 v[32:35], v[56:59], a[216:219], v[32:35]\n" \
"v_mfma_f32_16x16x32_bf16 v[36:39], v[60:63], a[216:219], v[36:39]\n" \
"v_mfma_f32_16x16x32_bf16 v[40:43], v[56:59], a[220:223], v[40:43]\n" \
"v_mfma_f32_16x16x32_bf16 v[44:47], v[60:63], a[220:223], v[44:47]\n" \
"s_waitcnt lgkmcnt(2)\n" \
"v_mfma_f32_16x16x32_bf16 v[16:19], v[64:67], a[224:227], v[16:19]\n" \
"ds_read_b128 v[48:51], v3 offset:16384\n" \
"v_mfma_f32_16x16x32_bf16 v[20:23], v[68:71], a[224:227], v[20:23]\n" \
"ds_read_b128 v[52:55], v3 offset:36864\n" \
"v_mfma_f32_16x16x32_bf16 v[24:27], v[64:67], a[228:231], v[24:27]\n" \
"v_mfma_f32_16x16x32_bf16 v[28:31], v[68:71], a[228:231], v[28:31]\n" \
"buffer_store_dwordx4 v[8:11], v0, s[40:43], s55 offen offset:0 sc1\n" \
"v_mfma_f32_16x16x32_bf16 v[32:35], v[64:67], a[232:235], v[32:35]\n" \
"v_mfma_f32_16x16x32_bf16 v[36:39], v[68:71], a[232:235], v[36:39]\n" \
"v_mfma_f32_16x16x32_bf16 v[40:43], v[64:67], a[236:239], v[40:43]\n" \
"v_mfma_f32_16x16x32_bf16 v[44:47], v[68:71], a[236:239], v[44:47]\n" \
"s_waitcnt lgkmcnt(2)\n" \
"v_mfma_f32_16x16x32_bf16 v[16:19], v[72:75], a[240:243], v[16:19]\n" \
"ds_read_b128 v[56:59], v3 offset:17408\n" \
"v_mfma_f32_16x16x32_bf16 v[20:23], v[76:79], a[240:243], v[20:23]\n" \
"ds_read_b128 v[60:63], v3 offset:37888\n" \
"v_mfma_f32_16x16x32_bf16 v[24:27], v[72:75], a[244:247], v[24:27]\n" \
"v_mfma_f32_16x16x32_bf16 v[28:31], v[76:79], a[244:247], v[28:31]\n" \
"v_mfma_f32_16x16x32_bf16 v[32:35], v[72:75], a[248:251], v[32:35]\n" \
"v_mfma_f32_16x16x32_bf16 v[36:39], v[76:79], a[248:251], v[36:39]\n" \
"v_mfma_f32_16x16x32_bf16 v[40:43], v[72:75], a[252:255], v[40:43]\n" \
"v_mfma_f32_16x16x32_bf16 v[44:47], v[76:79], a[252:255], v[44:47]\n" \
"s_waitcnt lgkmcnt(2)\n" \
"v_mfma_f32_16x16x32_bf16 v[16:19], v[48:51], v[192:195], v[16:19]\n" \
"ds_read_b128 v[64:67], v3 offset:18432\n" \
"v_mfma_f32_16x16x32_bf16 v[20:23], v[52:55], v[192:195], v[20:23]\n" \
"ds_read_b128 v[68:71], v3 offset:38912\n" \
"v_mfma_f32_16x16x32_bf16 v[24:27], v[48:51], v[196:199], v[24:27]\n" \
"v_mfma_f32_16x16x32_bf16 v[28:31], v[52:55], v[196:199], v[28:31]\n" \
"buffer_store_dwordx4 v[8:11], v0, s[40:43], s55 offen offset:1024 sc1\n" \
"v_mfma_f32_16x16x32_bf16 v[32:35], v[48:51], v[200:203], v[32:35]\n" \
"v_mfma_f32_16x16x32_bf16 v[36:39], v[52:55], v[200:203], v[36:39]\n" \
"v_mfma_f32_16x16x32_bf16 v[40:43], v[48:51], v[204:207], v[40:43]\n" \
"v_mfma_f32_16x16x32_bf16 v[44:47], v[52:55], v[204:207], v[44:47]\n" \
"s_waitcnt lgkmcnt(2)\n" \
"v_mfma_f32_16x16x32_bf16 v[16:19], v[56:59], v[208:211], v[16:19]\n" \
"ds_read_b128 v[72:75], v3 offset:19456\n" \
"v_mfma_f32_16x16x32_bf16 v[20:23], v[60:63], v[208:211], v[20:23]\n" \
"ds_read_b128 v[76:79], v3 offset:39936\n" \
"v_mfma_f32_16x16x32_bf16 v[24:27], v[56:59], v[212:215], v[24:27]\n" \
"s_mov_b32 s54, s56\n" \
"s_add_u32 s56, s56, 0x200000\n" \
"s_xor_b32 s56, s56, s69\n" \
"v_mfma_f32_16x16x32_bf16 v[28:31], v[60:63], v[212:215], v[28:31]\n" \
"v_mfma_f32_16x16x32_bf16 v[32:35], v[56:59], v[216:219], v[32:35]\n" \
"s_add_u32 s55, s55, 0x200000\n" \
"s_xor_b32 s55, s55, s69\n" \
"v_mfma_f32_16x16x32_bf16 v[36:39], v[60:63], v[216:219], v[36:39]\n" \
"v_xor_b32 v3, 40960, v3\n" \
"s_add_u32 s35, s35, 1\n" \
"v_mfma_f32_16x16x32_bf16 v[40:43], v[56:59], v[220:223], v[40:43]\n" \
"v_mfma_f32_16x16x32_bf16 v[44:47], v[60:63], v[220:223], v[44:47]\n" \
"s_waitcnt vmcnt(2)\n" \
"s_waitcnt lgkmcnt(0)\n" \
"s_barrier\n" \
"v_mfma_f32_16x16x32_bf16 v[16:19], v[64:67], v[224:227], v[16:19]\n" \
"ds_read_b128 v[48:51], v3 offset:0\n" \
"v_mfma_f32_16x16x32_bf16 v[20:23], v[68:71], v[224:227], v[20:23]\n" \
"ds_read_b128 v[52:55], v3 offset:20480\n" \
"v_mfma_f32_16x16x32_bf16 v[24:27], v[64:67], v[228:231], v[24:27]\n" \
"v_mfma_f32_16x16x32_bf16 v[28:31], v[68:71], v[228:231], v[28:31]\n" \
"v_mfma_f32_16x16x32_bf16 v[32:35], v[64:67], v[232:235], v[32:35]\n" \
"v_mfma_f32_16x16x32_bf16 v[36:39], v[68:71], v[232:235], v[36:39]\n" \
"v_mfma_f32_16x16x32_bf16 v[40:43], v[64:67], v[236:239], v[40:43]\n" \
"v_mfma_f32_16x16x32_bf16 v[44:47], v[68:71], v[236:239], v[44:47]\n" \
"v_mfma_f32_16x16x32_bf16 v[16:19], v[72:75], v[240:243], v[16:19]\n" \
"ds_read_b128 v[56:59], v3 offset:1024\n" \
"v_mfma_f32_16x16x32_bf16 v[20:23], v[76:79], v[240:243], v[20:23]\n" \
"ds_read_b128 v[60:63], v3 offset:21504\n" \
"v_mfma_f32_16x16x32_bf16 v[24:27], v[72:75], v[244:247], v[24:27]\n" \
"v_mfma_f32_16x16x32_bf16 v[28:31], v[76:79], v[244:247], v[28:31]\n" \
"v_cvt_pk_bf16_f32 v80, v16, v17\n" \
"v_cvt_pk_bf16_f32 v81, v18, v19\n" \
"v_mfma_f32_16x16x32_bf16 v[32:35], v[72:75], v[248:251], v[32:35]\n" \
"v_cvt_pk_bf16_f32 v82, v20, v21\n" \
"v_cvt_pk_bf16_f32 v83, v22, v23\n" \
"v_mfma_f32_16x16x32_bf16 v[36:39], v[76:79], v[248:251], v[36:39]\n" \
"v_cvt_pk_bf16_f32 v84, v24, v25\n" \
"v_cvt_pk_bf16_f32 v85, v26, v27\n" \
"v_mfma_f32_16x16x32_bf16 v[40:43], v[72:75], v[252:255], v[40:43]\n" \
"v_cvt_pk_bf16_f32 v86, v28, v29\n" \
"v_cvt_pk_bf16_f32 v87, v30, v31\n" \
"v_mfma_f32_16x16x32_bf16 v[44:47], v[76:79], v[252:255], v[44:47]\n" \
"v_cvt_pk_bf16_f32 v88, v32, v33\n" \
"v_cvt_pk_bf16_f32 v89, v34, v35\n" \
"s_cmp_lt_u32 s35, 64\n" \
"s_cbranch_scc1 KVL_%=\n" \
"s_nop 7\n" \
"s_nop 1\n" \
"v_cvt_pk_bf16_f32 v90, v36, v37\n" \
"v_cvt_pk_bf16_f32 v91, v38, v39\n" \
"v_cvt_pk_bf16_f32 v92, v40, v41\n" \
"v_cvt_pk_bf16_f32 v93, v42, v43\n" \
"v_cvt_pk_bf16_f32 v94, v44, v45\n" \
"v_cvt_pk_bf16_f32 v95, v46, v47\n" \
"buffer_store_dwordx4 v[80:83], v0, s[40:43], s54 offen offset:0 sc1\n" \
"buffer_store_dwordx4 v[84:87], v0, s[40:43], s54 offen offset:1024 sc1\n" \
"buffer_store_dwordx4 v[88:91], v0, s[40:43], s54 offen offset:2048 sc1\n" \
"buffer_store_dwordx4 v[92:95], v0, s[40:43], s54 offen offset:3072 sc1\n" \
"s_branch KF_%=\n" \
"KF_%=:\n" \
"s_waitcnt vmcnt(0)\n" \
"s_barrier\n" \
"v_mov_b32 v12, 64\n" \
"s_mov_b64 exec, 3\n" \
"buffer_store_dword v12, v2, s[40:43], s51 offen sc1\n" \
"s_mov_b64 exec, -1\n" \
"s_cmp_lg_u32 s50, 0\n" \
"s_cbranch_scc1 KE_%=\n" \
"v_mov_b32 v0, 1\n" \
"buffer_store_dword v0, off, s[40:43], 0 offset:256 sc1\n" \
"KE_%=:\n" \
"KCZ_%=:\n" \
"s_endpgm\n"
#define QA_ASM_TEXT \
"s_getpc_b64 s[46:47]\n" \
"QCW_%=:\n" \
"v_mbcnt_lo_u32_b32 v60, -1, 0\n" \
"v_mbcnt_hi_u32_b32 v60, -1, v60\n" \
"v_lshlrev_b32 v60, 7, v60\n" \
"v_min_u32 v61, QCZ_%=-QCW_%=, v60\n" \
"global_load_dword v62, v61, s[46:47]\n" \
"v_add_u32 v60, 0x2000, v60\n" \
"v_min_u32 v61, QCZ_%=-QCW_%=, v60\n" \
"global_load_dword v62, v61, s[46:47]\n" \
"v_add_u32 v60, 0x2000, v60\n" \
"s_mov_b64 s[40:41], %[ws]\n" \
"s_add_u32 s40, s40, 0x800000\n" \
"s_addc_u32 s41, s41, 0\n" \
"s_and_b32 s41, s41, 0xffff\n" \
"s_mov_b32 s42, 0x9000000\n" \
"s_mov_b32 s43, 0x00020000\n" \
"s_mov_b32 s33, %[wave]\n" \
"v_mbcnt_lo_u32_b32 v48, -1, 0\n" \
"v_mbcnt_hi_u32_b32 v48, -1, v48\n" \
"s_lshl_b32 s48, s33, 6\n" \
"v_add_lshl_u32 v48, v48, s48, 4\n" \
"ds_read_b128 v[0:3], v48 offset:0\n" \
"ds_read_b128 v[4:7], v48 offset:4096\n" \
"ds_read_b128 v[8:11], v48 offset:8192\n" \
"ds_read_b128 v[32:35], v48 offset:12288\n" \
"ds_read_b128 v[36:39], v48 offset:16384\n" \
"ds_read_b128 v[40:43], v48 offset:20480\n" \
"s_waitcnt lgkmcnt(0)\n" \
"s_barrier\n" \
"v_readfirstlane_b32 s70, v32\n" \
"v_readfirstlane_b32 s71, v33\n" \
"v_readfirstlane_b32 s72, v34\n" \
"v_readfirstlane_b32 s73, v35\n" \
"v_readfirstlane_b32 s51, v36\n" \
"v_readfirstlane_b32 s55, v37\n" \
"v_readfirstlane_b32 s56, v38\n" \
"v_readfirstlane_b32 s57, v39\n" \
"v_readfirstlane_b32 s53, v40\n" \
"v_readfirstlane_b32 s62, v41\n" \
"s_nop 4\n" \
"s_lshl_b32 s34, s33, 10\n" \
"s_cmp_eq_u32 s33, 0\n" \
"s_cselect_b32 s51, s51, 0x7ff00000\n" \
"s_mov_b32 s50, 0x80000\n" \
"s_mov_b32 s35, 0\n" \
"s_mov_b64 s[44:45], s[70:71]\n" \
"s_and_b32 s45, s45, 0xffff\n" \
"s_mov_b32 s46, 0x500000\n" \
"s_mov_b32 s47, 0x00020000\n" \
"s_sleep 24\n" \
"s_add_u32 m0, s34, 0\n" \
"s_mov_b32 s48, 0x0\n" \
"buffer_load_dwordx4 v1, s[44:47], s48 offen lds\n" \
"s_add_u32 m0, s34, 4096\n" \
"s_mov_b32 s48, 0x20000\n" \
"buffer_load_dwordx4 v1, s[44:47], s48 offen lds\n" \
"s_add_u32 m0, s34, 8192\n" \
"s_mov_b32 s48, 0x40000\n" \
"buffer_load_dwordx4 v1, s[44:47], s48 offen lds\n" \
"s_add_u32 m0, s34, 12288\n" \
"s_mov_b32 s48, 0x60000\n" \
"buffer_load_dwordx4 v1, s[44:47], s48 offen lds\n" \
"s_add_u32 m0, s34, 16384\n" \
"s_mov_b32 s48, 0x80000\n" \
"buffer_load_dwordx4 v1, s[44:47], s48 offen lds\n" \
"s_add_u32 m0, s34, 20480\n" \
"s_mov_b32 s48, 0xa0000\n" \
"buffer_load_dwordx4 v1, s[44:47], s48 offen lds\n" \
"s_add_u32 m0, s34, 24576\n" \
"s_mov_b32 s48, 0xc0000\n" \
"buffer_load_dwordx4 v1, s[44:47], s48 offen lds\n" \
"s_add_u32 m0, s34, 28672\n" \
"s_mov_b32 s48, 0xe0000\n" \
"buffer_load_dwordx4 v1, s[44:47], s48 offen lds\n" \
"s_add_u32 m0, s34, 32768\n" \
"s_mov_b32 s48, 0x100000\n" \
"buffer_load_dwordx4 v1, s[44:47], s48 offen lds\n" \
"s_add_u32 m0, s34, 36864\n" \
"s_mov_b32 s48, 0x120000\n" \
"buffer_load_dwordx4 v1, s[44:47], s48 offen lds\n" \
"s_add_u32 m0, s34, 40960\n" \
"s_mov_b32 s48, 0x140000\n" \
"buffer_load_dwordx4 v1, s[44:47], s48 offen lds\n" \
"s_add_u32 m0, s34, 45056\n" \
"s_mov_b32 s48, 0x160000\n" \
"buffer_load_dwordx4 v1, s[44:47], s48 offen lds\n" \
"s_add_u32 m0, s34, 49152\n" \
"s_mov_b32 s48, 0x180000\n" \
"buffer_load_dwordx4 v1, s[44:47], s48 offen lds\n" \
"s_add_u32 m0, s34, 53248\n" \
"s_mov_b32 s48, 0x1a0000\n" \
"buffer_load_dwordx4 v1, s[44:47], s48 offen lds\n" \
"s_add_u32 m0, s34, 57344\n" \
"s_mov_b32 s48, 0x1c0000\n" \
"buffer_load_dwordx4 v1, s[44:47], s48 offen lds\n" \
"s_add_u32 m0, s34, 61440\n" \
"s_mov_b32 s48, 0x1e0000\n" \
"buffer_load_dwordx4 v1, s[44:47], s48 offen lds\n" \
"s_add_u32 m0, s34, 65536\n" \
"s_mov_b32 s48, 0x200000\n" \
"buffer_load_dwordx4 v1, s[44:47], s48 offen lds\n" \
"s_add_u32 m0, s34, 69632\n" \
"s_mov_b32 s48, 0x220000\n" \
"buffer_load_dwordx4 v1, s[44:47], s48 offen lds\n" \
"s_add_u32 m0, s34, 73728\n" \
"s_mov_b32 s48, 0x240000\n" \
"buffer_load_dwordx4 v1, s[44:47], s48 offen lds\n" \
"s_add_u32 m0, s34, 77824\n" \
"s_mov_b32 s48, 0x260000\n" \
"buffer_load_dwordx4 v1, s[44:47], s48 offen lds\n" \
"s_add_u32 m0, s34, 81920\n" \
"s_mov_b32 s48, 0x280000\n" \
"buffer_load_dwordx4 v1, s[44:47], s48 offen lds\n" \
"s_add_u32 m0, s34, 86016\n" \
"s_mov_b32 s48, 0x2a0000\n" \
"buffer_load_dwordx4 v1, s[44:47], s48 offen lds\n" \
"s_add_u32 m0, s34, 90112\n" \
"s_mov_b32 s48, 0x2c0000\n" \
"buffer_load_dwordx4 v1, s[44:47], s48 offen lds\n" \
"s_add_u32 m0, s34, 94208\n" \
"s_mov_b32 s48, 0x2e0000\n" \
"buffer_load_dwordx4 v1, s[44:47], s48 offen lds\n" \
"s_add_u32 m0, s34, 98304\n" \
"s_mov_b32 s48, 0x300000\n" \
"buffer_load_dwordx4 v1, s[44:47], s48 offen lds\n" \
"s_add_u32 m0, s34, 102400\n" \
"s_mov_b32 s48, 0x320000\n" \
"buffer_load_dwordx4 v1, s[44:47], s48 offen lds\n" \
"s_add_u32 m0, s34, 106496\n" \
"s_mov_b32 s48, 0x340000\n" \
"buffer_load_dwordx4 v1, s[44:47], s48 offen lds\n" \
"s_add_u32 m0, s34, 110592\n" \
"s_mov_b32 s48, 0x360000\n" \
"buffer_load_dwordx4 v1, s[44:47], s48 offen lds\n" \
"s_add_u32 m0, s34, 114688\n" \
"s_mov_b32 s48, 0x380000\n" \
"buffer_load_dwordx4 v1, s[44:47], s48 offen lds\n" \
"s_add_u32 m0, s34, 118784\n" \
"s_mov_b32 s48, 0x3a0000\n" \
"buffer_load_dwordx4 v1, s[44:47], s48 offen lds\n" \
"s_add_u32 m0, s34, 122880\n" \
"s_mov_b32 s48, 0x3c0000\n" \
"buffer_load_dwordx4 v1, s[44:47], s48 offen lds\n" \
"s_add_u32 m0, s34, 126976\n" \
"s_mov_b32 s48, 0x3e0000\n" \
"buffer_load_dwordx4 v1, s[44:47], s48 offen lds\n" \
"s_add_u32 m0, s34, 131072\n" \
"s_mov_b32 s48, 0x400000\n" \
"buffer_load_dwordx4 v1, s[44:47], s48 offen lds\n" \
"s_add_u32 m0, s34, 135168\n" \
"s_mov_b32 s48, 0x420000\n" \
"buffer_load_dwordx4 v1, s[44:47], s48 offen lds\n" \
"s_add_u32 m0, s34, 139264\n" \
"s_mov_b32 s48, 0x440000\n" \
"buffer_load_dwordx4 v1, s[44:47], s48 offen lds\n" \
"s_add_u32 m0, s34, 143360\n" \
"s_mov_b32 s48, 0x460000\n" \
"buffer_load_dwordx4 v1, s[44:47], s48 offen lds\n" \
"s_add_u32 m0, s34, 147456\n" \
"s_mov_b32 s48, 0x480000\n" \
"buffer_load_dwordx4 v1, s[44:47], s48 offen lds\n" \
"s_add_u32 m0, s34, 151552\n" \
"s_mov_b32 s48, 0x4a0000\n" \
"buffer_load_dwordx4 v1, s[44:47], s48 offen lds\n" \
"s_add_u32 m0, s34, 155648\n" \
"s_mov_b32 s48, 0x4c0000\n" \
"buffer_load_dwordx4 v1, s[44:47], s48 offen lds\n" \
"s_add_u32 m0, s34, 159744\n" \
"s_mov_b32 s48, 0x4e0000\n" \
"buffer_load_dwordx4 v1, s[44:47], s48 offen lds\n" \
"s_waitcnt vmcnt(32)\n" \
"s_barrier\n" \
"v_add_u32 v20, 0x0, v2\n" \
"ds_read_b64_tr_b16 a[0:1], v20 offset:0\n" \
"ds_read_b64_tr_b16 a[2:3], v20 offset:1024\n" \
"ds_read_b64_tr_b16 a[8:9], v20 offset:8192\n" \
"ds_read_b64_tr_b16 a[10:11], v20 offset:9216\n" \
"ds_read_b64_tr_b16 a[16:17], v20 offset:16384\n" \
"ds_read_b64_tr_b16 a[18:19], v20 offset:17408\n" \
"ds_read_b64_tr_b16 a[24:25], v20 offset:24576\n" \
"ds_read_b64_tr_b16 a[26:27], v20 offset:25600\n" \
"v_add_u32 v20, 0x0, v3\n" \
"ds_read_b64_tr_b16 a[4:5], v20 offset:0\n" \
"ds_read_b64_tr_b16 a[6:7], v20 offset:1024\n" \
"ds_read_b64_tr_b16 a[12:13], v20 offset:8192\n" \
"ds_read_b64_tr_b16 a[14:15], v20 offset:9216\n" \
"ds_read_b64_tr_b16 a[20:21], v20 offset:16384\n" \
"ds_read_b64_tr_b16 a[22:23], v20 offset:17408\n" \
"ds_read_b64_tr_b16 a[28:29], v20 offset:24576\n" \
"ds_read_b64_tr_b16 a[30:31], v20 offset:25600\n" \
"s_waitcnt lgkmcnt(0)\n" \
"s_barrier\n" \
"s_waitcnt vmcnt(24)\n" \
"s_barrier\n" \
"v_add_u32 v20, 0x8000, v2\n" \
"ds_read_b64_tr_b16 a[32:33], v20 offset:0\n" \
"ds_read_b64_tr_b16 a[34:35], v20 offset:1024\n" \
"ds_read_b64_tr_b16 a[40:41], v20 offset:8192\n" \
"ds_read_b64_tr_b16 a[42:43], v20 offset:9216\n" \
"ds_read_b64_tr_b16 a[48:49], v20 offset:16384\n" \
"ds_read_b64_tr_b16 a[50:51], v20 offset:17408\n" \
"ds_read_b64_tr_b16 a[56:57], v20 offset:24576\n" \
"ds_read_b64_tr_b16 a[58:59], v20 offset:25600\n" \
"v_add_u32 v20, 0x8000, v3\n" \
"ds_read_b64_tr_b16 a[36:37], v20 offset:0\n" \
"ds_read_b64_tr_b16 a[38:39], v20 offset:1024\n" \
"ds_read_b64_tr_b16 a[44:45], v20 offset:8192\n" \
"ds_read_b64_tr_b16 a[46:47], v20 offset:9216\n" \
"ds_read_b64_tr_b16 a[52:53], v20 offset:16384\n" \
"ds_read_b64_tr_b16 a[54:55], v20 offset:17408\n" \
"ds_read_b64_tr_b16 a[60:61], v20 offset:24576\n" \
"ds_read_b64_tr_b16 a[62:63], v20 offset:25600\n" \
"s_waitcnt lgkmcnt(0)\n" \
"s_barrier\n" \
"s_waitcnt vmcnt(16)\n" \
"s_barrier\n" \
"v_add_u32 v20, 0x10000, v2\n" \
"ds_read_b64_tr_b16 a[64:65], v20 offset:0\n" \
"ds_read_b64_tr_b16 a[66:67], v20 offset:1024\n" \
"ds_read_b64_tr_b16 a[72:73], v20 offset:8192\n" \
"ds_read_b64_tr_b16 a[74:75], v20 offset:9216\n" \
"ds_read_b64_tr_b16 a[80:81], v20 offset:16384\n" \
"ds_read_b64_tr_b16 a[82:83], v20 offset:17408\n" \
"ds_read_b64_tr_b16 a[88:89], v20 offset:24576\n" \
"ds_read_b64_tr_b16 a[90:91], v20 offset:25600\n" \
"v_add_u32 v20, 0x10000, v3\n" \
"ds_read_b64_tr_b16 a[68:69], v20 offset:0\n" \
"ds_read_b64_tr_b16 a[70:71], v20 offset:1024\n" \
"ds_read_b64_tr_b16 a[76:77], v20 offset:8192\n" \
"ds_read_b64_tr_b16 a[78:79], v20 offset:9216\n" \
"ds_read_b64_tr_b16 a[84:85], v20 offset:16384\n" \
"ds_read_b64_tr_b16 a[86:87], v20 offset:17408\n" \
"ds_read_b64_tr_b16 a[92:93], v20 offset:24576\n" \
"ds_read_b64_tr_b16 a[94:95], v20 offset:25600\n" \
"s_waitcnt lgkmcnt(0)\n" \
"s_barrier\n" \
"s_waitcnt vmcnt(8)\n" \
"s_barrier\n" \
"v_add_u32 v20, 0x18000, v2\n" \
"ds_read_b64_tr_b16 a[96:97], v20 offset:0\n" \
"ds_read_b64_tr_b16 a[98:99], v20 offset:1024\n" \
"ds_read_b64_tr_b16 a[104:105], v20 offset:8192\n" \
"ds_read_b64_tr_b16 a[106:107], v20 offset:9216\n" \
"ds_read_b64_tr_b16 a[112:113], v20 offset:16384\n" \
"ds_read_b64_tr_b16 a[114:115], v20 offset:17408\n" \
"ds_read_b64_tr_b16 a[120:121], v20 offset:24576\n" \
"ds_read_b64_tr_b16 a[122:123], v20 offset:25600\n" \
"v_add_u32 v20, 0x18000, v3\n" \
"ds_read_b64_tr_b16 a[100:101], v20 offset:0\n" \
"ds_read_b64_tr_b16 a[102:103], v20 offset:1024\n" \
"ds_read_b64_tr_b16 a[108:109], v20 offset:8192\n" \
"ds_read_b64_tr_b16 a[110:111], v20 offset:9216\n" \
"ds_read_b64_tr_b16 a[116:117], v20 offset:16384\n" \
"ds_read_b64_tr_b16 a[118:119], v20 offset:17408\n" \
"ds_read_b64_tr_b16 a[124:125], v20 offset:24576\n" \
"ds_read_b64_tr_b16 a[126:127], v20 offset:25600\n" \
"s_waitcnt lgkmcnt(0)\n" \
"s_barrier\n" \
"s_waitcnt vmcnt(0)\n" \
"s_barrier\n" \
"v_add_u32 v20, 0x20000, v2\n" \
"ds_read_b64_tr_b16 a[128:129], v20 offset:0\n" \
"ds_read_b64_tr_b16 a[130:131], v20 offset:1024\n" \
"ds_read_b64_tr_b16 a[136:137], v20 offset:8192\n" \
"ds_read_b64_tr_b16 a[138:139], v20 offset:9216\n" \
"ds_read_b64_tr_b16 a[144:145], v20 offset:16384\n" \
"ds_read_b64_tr_b16 a[146:147], v20 offset:17408\n" \
"ds_read_b64_tr_b16 a[152:153], v20 offset:24576\n" \
"ds_read_b64_tr_b16 a[154:155], v20 offset:25600\n" \
"v_add_u32 v20, 0x20000, v3\n" \
"ds_read_b64_tr_b16 a[132:133], v20 offset:0\n" \
"ds_read_b64_tr_b16 a[134:135], v20 offset:1024\n" \
"ds_read_b64_tr_b16 a[140:141], v20 offset:8192\n" \
"ds_read_b64_tr_b16 a[142:143], v20 offset:9216\n" \
"ds_read_b64_tr_b16 a[148:149], v20 offset:16384\n" \
"ds_read_b64_tr_b16 a[150:151], v20 offset:17408\n" \
"ds_read_b64_tr_b16 a[156:157], v20 offset:24576\n" \
"ds_read_b64_tr_b16 a[158:159], v20 offset:25600\n" \
"s_waitcnt lgkmcnt(0)\n" \
"s_barrier\n" \
"s_mov_b32 s54, s34\n" \
"s_add_u32 m0, s54, 0\n" \
"s_add_u32 s48, s53, 0\n" \
"buffer_load_dwordx4 v0, s[40:43], s48 offen lds\n" \
"s_add_u32 m0, s54, 4096\n" \
"s_add_u32 s48, s53, 4096\n" \
"buffer_load_dwordx4 v0, s[40:43], s48 offen lds\n" \
"s_add_u32 m0, s54, 8192\n" \
"s_add_u32 s48, s53, 16384\n" \
"buffer_load_dwordx4 v0, s[40:43], s48 offen lds\n" \
"s_add_u32 m0, s54, 12288\n" \
"s_add_u32 s48, s53, 20480\n" \
"buffer_load_dwordx4 v0, s[40:43], s48 offen lds\n" \
"s_add_u32 m0, s54, 16384\n" \
"s_add_u32 s48, s53, 32768\n" \
"buffer_load_dwordx4 v0, s[40:43], s48 offen lds\n" \
"s_add_u32 m0, s54, 20480\n" \
"s_add_u32 s48, s53, 36864\n" \
"buffer_load_dwordx4 v0, s[40:43], s48 offen lds\n" \
"s_add_u32 m0, s54, 24576\n" \
"s_add_u32 s48, s53, 49152\n" \
"buffer_load_dwordx4 v0, s[40:43], s48 offen lds\n" \
"s_add_u32 m0, s54, 28672\n" \
"s_add_u32 s48, s53, 53248\n" \
"buffer_load_dwordx4 v0, s[40:43], s48 offen lds\n" \
"s_add_u32 m0, s54, 32768\n" \
"s_add_u32 s48, s53, 65536\n" \
"buffer_load_dwordx4 v0, s[40:43], s48 offen lds\n" \
"s_add_u32 m0, s54, 36864\n" \
"s_add_u32 s48, s53, 69632\n" \
"buffer_load_dwordx4 v0, s[40:43], s48 offen lds\n" \
"s_mov_b32 s60, 0x3e0293ee\n" \
"v_mov_b32 v13, 0xceabfb8f\n" \
"v_cmp_le_i32 s[70:71], 0, v11\n" \
"v_cmp_le_i32 s[78:79], 16, v11\n" \
"v_cmp_le_i32 s[72:73], 1, v11\n" \
"v_cmp_le_i32 s[80:81], 17, v11\n" \
"v_cmp_le_i32 s[74:75], 2, v11\n" \
"v_cmp_le_i32 s[82:83], 18, v11\n" \
"v_cmp_le_i32 s[76:77], 3, v11\n" \
"v_cmp_le_i32 s[84:85], 19, v11\n" \
"v_mov_b32 v4, v0\n" \
"v_add_u32 v14, 0x18000, v0\n" \
"v_mov_b64 v[176:177], 0\n" \
"v_mov_b64 v[178:179], 0\n" \
"s_mov_b32 s61, s60\n" \
"s_add_u32 s59, s34, 98304\n" \
"s_mov_b32 s52, 1\n" \
"QO_%=:\n" \
"buffer_load_dword v12, v10, s[40:43], 0 offen sc1\n" \
"s_waitcnt vmcnt(0)\n" \
"QOC_%=:\n" \
"v_cmp_gt_u32 vcc, s52, v12\n" \
"s_cbranch_vccz QOD_%=\n" \
"s_sleep 2\n" \
"s_sub_u32 s50, s50, 1\n" \
"s_cbranch_scc0 QO_%=\n" \
"s_mov_b32 s50, 0\n" \
"QOD_%=:\n" \
"v_readfirstlane_b32 s58, v12\n" \
"s_add_u32 m0, s59, 0\n" \
"s_add_u32 s48, s55, 0x0\n" \
"buffer_load_dwordx4 v0, s[40:43], s48 offen sc1 lds\n" \
"s_add_u32 m0, s59, 4096\n" \
"s_add_u32 s48, s55, 0x1000\n" \
"buffer_load_dwordx4 v0, s[40:43], s48 offen sc1 lds\n" \
"s_add_u32 m0, s59, 8192\n" \
"s_add_u32 s48, s55, 0x80000\n" \
"buffer_load_dwordx4 v0, s[40:43], s48 offen sc1 lds\n" \
"s_add_u32 m0, s59, 12288\n" \
"s_add_u32 s48, s55, 0x81000\n" \
"buffer_load_dwordx4 v0, s[40:43], s48 offen sc1 lds\n" \
"v_mov_b32 v64, 0xff81ff81\n" \
"v_mov_b32 v65, 0xff81ff81\n" \
"v_mov_b32 v66, 0xff81ff81\n" \
"v_mov_b32 v67, 0xff81ff81\n" \
"v_mov_b32 v68, 0xff81ff81\n" \
"v_mov_b32 v69, 0xff81ff81\n" \
"v_mov_b32 v70, 0xff81ff81\n" \
"v_mov_b32 v71, 0xff81ff81\n" \
"v_mov_b32 v72, 0xff81ff81\n" \
"v_mov_b32 v73, 0xff81ff81\n" \
"v_mov_b32 v74, 0xff81ff81\n" \
"v_mov_b32 v75, 0xff81ff81\n" \
"v_mov_b32 v76, 0xff81ff81\n" \
"v_mov_b32 v77, 0xff81ff81\n" \
"v_mov_b32 v78, 0xff81ff81\n" \
"v_mov_b32 v79, 0xff81ff81\n" \
"s_mov_b32 s64, 0\n" \
"s_mov_b32 s65, 0\n" \
"s_waitcnt vmcnt(0)\n" \
"s_barrier\n" \
"ds_read_b128 v[32:35], v4 offset:0\n" \
"ds_read_b128 v[36:39], v4 offset:20480\n" \
"ds_read_b128 v[40:43], v4 offset:1024\n" \
"ds_read_b128 v[44:47], v4 offset:21504\n" \
"ds_read_b128 v[48:51], v4 offset:2048\n" \
"ds_read_b128 v[52:55], v4 offset:22528\n" \
"s_waitcnt lgkmcnt(0)\n" \
"QL_%=:\n" \
"s_add_u32 s52, s35, 2\n" \
"s_min_u32 s52, s52, 64\n" \
"s_cmp_ge_u32 s58, s52\n" \
"s_cbranch_scc1 QG_%=\n" \
"QP_%=:\n" \
"buffer_load_dword v12, v10, s[40:43], 0 offen sc1\n" \
"s_waitcnt vmcnt(0)\n" \
"QPC_%=:\n" \
"v_cmp_gt_u32 vcc, s52, v12\n" \
"s_cbranch_vccz QPD_%=\n" \
"s_sleep 2\n" \
"s_sub_u32 s50, s50, 1\n" \
"s_cbranch_scc0 QP_%=\n" \
"s_mov_b32 s50, 0\n" \
"QPD_%=:\n" \
"v_readfirstlane_b32 s58, v12\n" \
"QG_%=:\n" \
"s_bitcmp1_b64 %[dpar], s35\n" \
"s_cselect_b32 s69, 0x2000, 0\n" \
"s_add_u32 s53, s53, 0x200000\n" \
"s_xor_b32 s53, s53, s69\n" \
"s_add_u32 s55, s55, 0x200000\n" \
"s_xor_b32 s55, s55, s69\n" \
"s_xor_b32 s54, s54, 40960\n" \
"s_xor_b32 s59, s59, 0x4000\n" \
"s_cmp_lt_u32 s35, 63\n" \
"s_cselect_b32 s55, s55, 0x7f000000\n" \
"s_cselect_b32 s53, s53, 0x7f000000\n" \
"v_mfma_f32_16x16x32_bf16 v[16:19], a[0:3], v[32:35], 0\n" \
"ds_read_b128 v[56:59], v4 offset:3072\n" \
"v_mfma_f32_16x16x32_bf16 v[20:23], a[4:7], v[32:35], 0\n" \
"ds_read_b128 v[60:63], v4 offset:23552\n" \
"s_add_u32 m0, s54, 0\n" \
"s_add_u32 s48, s53, 0\n" \
"v_mfma_f32_16x16x32_bf16 v[24:27], a[0:3], v[36:39], 0\n" \
"v_mov_b64 v[80:81], v[212:213]\n" \
"v_mov_b64 v[82:83], v[214:215]\n" \
"v_mfma_f32_16x16x32_bf16 v[28:31], a[4:7], v[36:39], 0\n" \
"buffer_load_dwordx4 v0, s[40:43], s48 offen lds\n" \
"v_mfma_f32_16x16x32_bf16 v[16:19], a[8:11], v[40:43], v[16:19]\n" \
"ds_read_b128 v[32:35], v4 offset:4096\n" \
"v_mfma_f32_16x16x32_bf16 v[20:23], a[12:15], v[40:43], v[20:23]\n" \
"ds_read_b128 v[36:39], v4 offset:24576\n" \
"s_add_u32 m0, s54, 4096\n" \
"s_add_u32 s48, s53, 4096\n" \
"v_mfma_f32_16x16x32_bf16 v[24:27], a[8:11], v[44:47], v[24:27]\n" \
"v_mov_b64 v[84:85], v[216:217]\n" \
"v_mov_b64 v[86:87], v[218:219]\n" \
"v_mfma_f32_16x16x32_bf16 v[28:31], a[12:15], v[44:47], v[28:31]\n" \
"buffer_load_dwordx4 v0, s[40:43], s48 offen lds\n" \
"v_mfma_f32_16x16x32_bf16 v[16:19], a[16:19], v[48:51], v[16:19]\n" \
"ds_read_b128 v[40:43], v4 offset:5120\n" \
"v_mfma_f32_16x16x32_bf16 v[20:23], a[20:23], v[48:51], v[20:23]\n" \
"ds_read_b128 v[44:47], v4 offset:25600\n" \
"s_add_u32 m0, s54, 8192\n" \
"s_add_u32 s48, s53, 16384\n" \
"v_mfma_f32_16x16x32_bf16 v[24:27], a[16:19], v[52:55], v[24:27]\n" \
"v_mov_b64 v[88:89], v[220:221]\n" \
"v_mov_b64 v[90:91], v[222:223]\n" \
"v_mfma_f32_16x16x32_bf16 v[28:31], a[20:23], v[52:55], v[28:31]\n" \
"buffer_load_dwordx4 v0, s[40:43], s48 offen lds\n" \
"s_waitcnt lgkmcnt(4)\n" \
"v_mfma_f32_16x16x32_bf16 v[16:19], a[24:27], v[56:59], v[16:19]\n" \
"ds_read_b128 v[48:51], v4 offset:6144\n" \
"v_mfma_f32_16x16x32_bf16 v[20:23], a[28:31], v[56:59], v[20:23]\n" \
"ds_read_b128 v[52:55], v4 offset:26624\n" \
"s_add_u32 m0, s54, 12288\n" \
"s_add_u32 s48, s53, 20480\n" \
"v_mfma_f32_16x16x32_bf16 v[24:27], a[24:27], v[60:63], v[24:27]\n" \
"v_mov_b64 v[92:93], v[224:225]\n" \
"v_mov_b64 v[94:95], v[226:227]\n" \
"v_mfma_f32_16x16x32_bf16 v[28:31], a[28:31], v[60:63], v[28:31]\n" \
"buffer_load_dwordx4 v0, s[40:43], s48 offen lds\n" \
"s_waitcnt lgkmcnt(4)\n" \
"v_mfma_f32_16x16x32_bf16 v[16:19], a[32:35], v[32:35], v[16:19]\n" \
"ds_read_b128 v[56:59], v4 offset:7168\n" \
"v_mfma_f32_16x16x32_bf16 v[20:23], a[36:39], v[32:35], v[20:23]\n" \
"ds_read_b128 v[60:63], v4 offset:27648\n" \
"s_add_u32 m0, s54, 16384\n" \
"s_add_u32 s48, s53, 32768\n" \
"v_mfma_f32_16x16x32_bf16 v[24:27], a[32:35], v[36:39], v[24:27]\n" \
"v_mov_b64 v[128:129], v[228:229]\n" \
"v_mov_b64 v[130:131], v[230:231]\n" \
"v_mfma_f32_16x16x32_bf16 v[28:31], a[36:39], v[36:39], v[28:31]\n" \
"buffer_load_dwordx4 v0, s[40:43], s48 offen lds\n" \
"s_waitcnt lgkmcnt(4)\n" \
"v_mfma_f32_16x16x32_bf16 v[16:19], a[40:43], v[40:43], v[16:19]\n" \
"ds_read_b128 v[32:35], v4 offset:8192\n" \
"v_mfma_f32_16x16x32_bf16 v[20:23], a[44:47], v[40:43], v[20:23]\n" \
"ds_read_b128 v[36:39], v4 offset:28672\n" \
"s_add_u32 m0, s54, 20480\n" \
"s_add_u32 s48, s53, 36864\n" \
"v_mfma_f32_16x16x32_bf16 v[24:27], a[40:43], v[44:47], v[24:27]\n" \
"v_mov_b64 v[132:133], v[232:233]\n" \
"v_mov_b64 v[134:135], v[234:235]\n" \
"v_mfma_f32_16x16x32_bf16 v[28:31], a[44:47], v[44:47], v[28:31]\n" \
"buffer_load_dwordx4 v0, s[40:43], s48 offen lds\n" \
"s_waitcnt lgkmcnt(4)\n" \
"v_mfma_f32_16x16x32_bf16 v[16:19], a[48:51], v[48:51], v[16:19]\n" \
"ds_read_b128 v[40:43], v4 offset:9216\n" \
"v_mfma_f32_16x16x32_bf16 v[20:23], a[52:55], v[48:51], v[20:23]\n" \
"ds_read_b128 v[44:47], v4 offset:29696\n" \
"s_add_u32 m0, s54, 24576\n" \
"s_add_u32 s48, s53, 49152\n" \
"v_mfma_f32_16x16x32_bf16 v[24:27], a[48:51], v[52:55], v[24:27]\n" \
"v_mov_b64 v[136:137], v[236:237]\n" \
"v_mov_b64 v[138:139], v[238:239]\n" \
"v_mfma_f32_16x16x32_bf16 v[28:31], a[52:55], v[52:55], v[28:31]\n" \
"buffer_load_dwordx4 v0, s[40:43], s48 offen lds\n" \
"s_waitcnt lgkmcnt(4)\n" \
"v_mfma_f32_16x16x32_bf16 v[16:19], a[56:59], v[56:59], v[16:19]\n" \
"ds_read_b128 v[48:51], v4 offset:10240\n" \
"v_mfma_f32_16x16x32_bf16 v[20:23], a[60:63], v[56:59], v[20:23]\n" \
"ds_read_b128 v[52:55], v4 offset:30720\n" \
"s_add_u32 m0, s54, 28672\n" \
"s_add_u32 s48, s53, 53248\n" \
"v_mfma_f32_16x16x32_bf16 v[24:27], a[56:59], v[60:63], v[24:27]\n" \
"v_mov_b64 v[140:141], v[240:241]\n" \
"v_mov_b64 v[142:143], v[242:243]\n" \
"v_mfma_f32_16x16x32_bf16 v[28:31], a[60:63], v[60:63], v[28:31]\n" \
"buffer_load_dwordx4 v0, s[40:43], s48 offen lds\n" \
"s_waitcnt lgkmcnt(4)\n" \
"v_mfma_f32_16x16x32_bf16 v[16:19], a[64:67], v[32:35], v[16:19]\n" \
"ds_read_b128 v[56:59], v4 offset:11264\n" \
"v_mfma_f32_16x16x32_bf16 v[20:23], a[68:71], v[32:35], v[20:23]\n" \
"ds_read_b128 v[60:63], v4 offset:31744\n" \
"s_add_u32 m0, s54, 32768\n" \
"s_add_u32 s48, s53, 65536\n" \
"v_mfma_f32_16x16x32_bf16 v[24:27], a[64:67], v[36:39], v[24:27]\n" \
"v_mov_b64 v[160:161], v[244:245]\n" \
"v_mov_b64 v[162:163], v[246:247]\n" \
"v_mfma_f32_16x16x32_bf16 v[28:31], a[68:71], v[36:39], v[28:31]\n" \
"buffer_load_dwordx4 v0, s[40:43], s48 offen lds\n" \
"s_waitcnt lgkmcnt(4)\n" \
"v_mfma_f32_16x16x32_bf16 v[16:19], a[72:75], v[40:43], v[16:19]\n" \
"ds_read_b128 v[32:35], v4 offset:12288\n" \
"v_mfma_f32_16x16x32_bf16 v[20:23], a[76:79], v[40:43], v[20:23]\n" \
"ds_read_b128 v[36:39], v4 offset:32768\n" \
"s_add_u32 m0, s54, 36864\n" \
"s_add_u32 s48, s53, 69632\n" \
"v_mfma_f32_16x16x32_bf16 v[24:27], a[72:75], v[44:47], v[24:27]\n" \
"v_mov_b64 v[164:165], v[248:249]\n" \
"v_mov_b64 v[166:167], v[250:251]\n" \
"v_mfma_f32_16x16x32_bf16 v[28:31], a[76:79], v[44:47], v[28:31]\n" \
"buffer_load_dwordx4 v0, s[40:43], s48 offen lds\n" \
"s_waitcnt lgkmcnt(4)\n" \
"v_mfma_f32_16x16x32_bf16 v[16:19], a[80:83], v[48:51], v[16:19]\n" \
"ds_read_b128 v[40:43], v4 offset:13312\n" \
"v_mfma_f32_16x16x32_bf16 v[20:23], a[84:87], v[48:51], v[20:23]\n" \
"ds_read_b128 v[44:47], v4 offset:33792\n" \
"buffer_load_dword v15, off, s[40:43], s62 sc1\n" \
"v_mfma_f32_16x16x32_bf16 v[24:27], a[80:83], v[52:55], v[24:27]\n" \
"v_mfma_f32_16x16x32_bf16 v[28:31], a[84:87], v[52:55], v[28:31]\n" \
"s_waitcnt vmcnt(11)\n" \
"s_barrier\n" \
"s_waitcnt lgkmcnt(4)\n" \
"v_mfma_f32_16x16x32_bf16 v[16:19], a[88:91], v[56:59], v[16:19]\n" \
"ds_read_b128 v[48:51], v4 offset:14336\n" \
"v_mfma_f32_16x16x32_bf16 v[20:23], a[92:95], v[56:59], v[20:23]\n" \
"ds_read_b128 v[52:55], v4 offset:34816\n" \
"v_mfma_f32_16x16x32_bf16 v[24:27], a[88:91], v[60:63], v[24:27]\n" \
"ds_read_b128 v[96:99], v14 offset:0\n" \
"v_mfma_f32_16x16x32_bf16 v[28:31], a[92:95], v[60:63], v[28:31]\n" \
"v_mov_b32 v12, s65\n" \
"s_mov_b64 exec, 1\n" \
"buffer_store_dword v12, off, s[40:43], s51 sc1\n" \
"s_mov_b64 exec, -1\n" \
"s_waitcnt lgkmcnt(5)\n" \
"v_mfma_f32_16x16x32_bf16 v[16:19], a[96:99], v[32:35], v[16:19]\n" \
"ds_read_b128 v[56:59], v4 offset:15360\n" \
"v_mfma_f32_16x16x32_bf16 v[20:23], a[100:103], v[32:35], v[20:23]\n" \
"ds_read_b128 v[60:63], v4 offset:35840\n" \
"v_mfma_f32_16x16x32_bf16 v[24:27], a[96:99], v[36:39], v[24:27]\n" \
"ds_read_b128 v[100:103], v14 offset:1024\n" \
"v_max3_u32 v180, v64, v65, v66\n" \
"v_mfma_f32_16x16x32_bf16 v[28:31], a[100:103], v[36:39], v[28:31]\n" \
"ds_read_b128 v[104:107], v14 offset:2048\n" \
"v_max3_u32 v180, v180, v68, v69\n" \
"s_waitcnt lgkmcnt(7)\n" \
"v_mfma_f32_16x16x32_bf16 v[16:19], a[104:107], v[40:43], v[16:19]\n" \
"ds_read_b128 v[32:35], v4 offset:16384\n" \
"v_mfma_f32_16x16x32_bf16 v[20:23], a[108:111], v[40:43], v[20:23]\n" \
"ds_read_b128 v[36:39], v4 offset:36864\n" \
"v_mfma_f32_16x16x32_bf16 v[24:27], a[104:107], v[44:47], v[24:27]\n" \
"ds_read_b128 v[108:111], v14 offset:3072\n" \
"v_max3_u32 v180, v180, v70, v72\n" \
"v_mfma_f32_16x16x32_bf16 v[28:31], a[108:111], v[44:47], v[28:31]\n" \
"ds_read_b128 v[112:115], v14 offset:4096\n" \
"v_max3_u32 v180, v180, v73, v74\n" \
"s_waitcnt lgkmcnt(9)\n" \
"v_mfma_f32_16x16x32_bf16 v[16:19], a[112:115], v[48:51], v[16:19]\n" \
"ds_read_b128 v[40:43], v4 offset:17408\n" \
"v_mfma_f32_16x16x32_bf16 v[20:23], a[116:119], v[48:51], v[20:23]\n" \
"ds_read_b128 v[44:47], v4 offset:37888\n" \
"v_mfma_f32_16x16x32_bf16 v[24:27], a[112:115], v[52:55], v[24:27]\n" \
"ds_read_b128 v[116:119], v14 offset:5120\n" \
"v_max3_u32 v180, v180, v76, v77\n" \
"v_mfma_f32_16x16x32_bf16 v[28:31], a[116:119], v[52:55], v[28:31]\n" \
"ds_read_b128 v[120:123], v14 offset:6144\n" \
"v_max_u32 v180, v180, v78\n" \
"s_waitcnt lgkmcnt(10)\n" \
"v_mfma_f32_16x16x32_bf16 v[16:19], a[120:123], v[56:59], v[16:19]\n" \
"ds_read_b128 v[48:51], v4 offset:18432\n" \
"v_mfma_f32_16x16x32_bf16 v[20:23], a[124:127], v[56:59], v[20:23]\n" \
"ds_read_b128 v[52:55], v4 offset:38912\n" \
"v_mfma_f32_16x16x32_bf16 v[24:27], a[120:123], v[60:63], v[24:27]\n" \
"ds_read_b128 v[124:127], v14 offset:7168\n" \
"v_cmp_eq_u32 vcc, 0xff81ff81, v180\n" \
"v_mfma_f32_16x16x32_bf16 v[28:31], a[124:127], v[60:63], v[28:31]\n" \
"s_waitcnt vmcnt(1)\n" \
"ds_read_b128 v[228:231], v7 offset:0\n" \
"s_waitcnt lgkmcnt(10)\n" \
"v_mfma_f32_16x16x32_bf16 v[16:19], a[128:131], v[32:35], v[16:19]\n" \
"ds_read_b128 v[56:59], v4 offset:19456\n" \
"v_readfirstlane_b32 s63, v15\n" \
"v_mfma_f32_16x16x32_bf16 v[20:23], a[132:135], v[32:35], v[20:23]\n" \
"ds_read_b128 v[60:63], v4 offset:39936\n" \
"v_mfma_f32_16x16x32_bf16 v[24:27], a[128:131], v[36:39], v[24:27]\n" \
"ds_read_b128 v[232:235], v7 offset:1024\n" \
"v_mfma_f32_16x16x32_bf16 v[28:31], a[132:135], v[36:39], v[28:31]\n" \
"ds_read_b128 v[236:239], v7 offset:2048\n" \
"s_waitcnt lgkmcnt(10)\n" \
"v_mfma_f32_16x16x32_bf16 v[16:19], a[136:139], v[40:43], v[16:19]\n" \
"ds_write2_b32 v8, v64, v65 offset0:0 offset1:32\n" \
"v_mfma_f32_16x16x32_bf16 v[20:23], a[140:143], v[40:43], v[20:23]\n" \
"ds_read_b128 v[240:243], v7 offset:3072\n" \
"v_mfma_f32_16x16x32_bf16 v[24:27], a[136:139], v[44:47], v[24:27]\n" \
"ds_write2_b32 v8, v68, v69 offset0:64 offset1:96\n" \
"v_mfma_f32_16x16x32_bf16 v[28:31], a[140:143], v[44:47], v[28:31]\n" \
"v_xor_b32 v14, 0x4000, v14\n" \
"s_waitcnt lgkmcnt(9)\n" \
"v_mfma_f32_16x16x32_bf16 v[16:19], a[144:147], v[48:51], v[16:19]\n" \
"ds_write2_b32 v8, v72, v73 offset0:128 offset1:160\n" \
"v_mfma_f32_16x16x32_bf16 v[20:23], a[148:151], v[48:51], v[20:23]\n" \
"v_xor_b32 v7, 0x4000, v7\n" \
"v_mfma_f32_16x16x32_bf16 v[24:27], a[144:147], v[52:55], v[24:27]\n" \
"ds_write2_b32 v8, v76, v77 offset0:192 offset1:224\n" \
"v_mfma_f32_16x16x32_bf16 v[28:31], a[148:151], v[52:55], v[28:31]\n" \
"v_xor_b32 v4, 40960, v4\n" \
"s_waitcnt lgkmcnt(7)\n" \
"v_mfma_f32_16x16x32_bf16 v[16:19], a[152:155], v[56:59], v[16:19]\n" \
"ds_read_b64_tr_b16 v[144:145], v9 offset:0\n" \
"ds_read_b64_tr_b16 v[146:147], v9 offset:128\n" \
"v_mfma_f32_16x16x32_bf16 v[20:23], a[156:159], v[56:59], v[20:23]\n" \
"ds_read_b64_tr_b16 v[148:149], v9 offset:256\n" \
"ds_read_b64_tr_b16 v[150:151], v9 offset:384\n" \
"v_mfma_f32_16x16x32_bf16 v[24:27], a[152:155], v[60:63], v[24:27]\n" \
"ds_read_b64_tr_b16 v[152:153], v9 offset:512\n" \
"ds_read_b64_tr_b16 v[154:155], v9 offset:640\n" \
"v_mfma_f32_16x16x32_bf16 v[28:31], a[156:159], v[60:63], v[28:31]\n" \
"ds_read_b64_tr_b16 v[156:157], v9 offset:768\n" \
"ds_read_b64_tr_b16 v[158:159], v9 offset:896\n" \
"s_cmp_ge_u32 s63, s35\n" \
"s_cselect_b32 s64, s64, 0\n" \
"s_cmp_eq_u32 s64, 0\n" \
"s_cbranch_scc1 QU_%=\n" \
"buffer_load_dwordx4 v[64:67], v0, s[40:43], s56 offen offset:0 sc1\n" \
"buffer_load_dwordx4 v[68:71], v0, s[40:43], s56 offen offset:1024 sc1\n" \
"buffer_load_dwordx4 v[72:75], v0, s[40:43], s56 offen offset:2048 sc1\n" \
"buffer_load_dwordx4 v[76:79], v0, s[40:43], s56 offen offset:3072 sc1\n" \
"QU_%=:\n" \
"v_pk_mul_f32 v[16:17], v[16:17], s[60:61]\n" \
"v_pk_mul_f32 v[18:19], v[18:19], s[60:61]\n" \
"v_pk_mul_f32 v[20:21], v[20:21], s[60:61]\n" \
"v_pk_mul_f32 v[22:23], v[22:23], s[60:61]\n" \
"v_cvt_pk_bf16_f32 v16, v16, v17\n" \
"v_cvt_pk_bf16_f32 v17, v18, v19\n" \
"v_cvt_pk_bf16_f32 v18, v20, v21\n" \
"v_cvt_pk_bf16_f32 v19, v22, v23\n" \
"ds_write_b128 v5, v[16:19] offset:0\n" \
"v_pk_mul_f32 v[24:25], v[24:25], s[60:61]\n" \
"v_pk_mul_f32 v[26:27], v[26:27], s[60:61]\n" \
"v_pk_mul_f32 v[28:29], v[28:29], s[60:61]\n" \
"v_pk_mul_f32 v[30:31], v[30:31], s[60:61]\n" \
"v_cvt_pk_bf16_f32 v24, v24, v25\n" \
"v_cvt_pk_bf16_f32 v25, v26, v27\n" \
"v_cvt_pk_bf16_f32 v26, v28, v29\n" \
"v_cvt_pk_bf16_f32 v27, v30, v31\n" \
"ds_write_b128 v5, v[24:27] offset:4096\n" \
"s_waitcnt lgkmcnt(0)\n" \
"s_barrier\n" \
"ds_read_b128 v[212:215], v6 offset:0\n" \
"ds_read_b128 v[216:219], v6 offset:1024\n" \
"ds_read_b128 v[220:223], v6 offset:2048\n" \
"ds_read_b128 v[224:227], v6 offset:3072\n" \
"ds_read_b128 v[32:35], v4 offset:0\n" \
"ds_read_b128 v[36:39], v4 offset:20480\n" \
"ds_read_b128 v[40:43], v4 offset:1024\n" \
"ds_read_b128 v[44:47], v4 offset:21504\n" \
"ds_read_b128 v[48:51], v4 offset:2048\n" \
"ds_read_b128 v[52:55], v4 offset:22528\n" \
"s_waitcnt lgkmcnt(9)\n" \
"v_mfma_f32_16x16x32_bf16 v[244:247], v[96:99], v[212:215], 0\n" \
"v_mfma_f32_16x16x32_bf16 v[248:251], v[112:115], v[212:215], 0\n" \
"s_waitcnt lgkmcnt(8)\n" \
"v_mfma_f32_16x16x32_bf16 v[244:247], v[100:103], v[216:219], v[244:247]\n" \
"v_mfma_f32_16x16x32_bf16 v[248:251], v[116:119], v[216:219], v[248:251]\n" \
"s_waitcnt lgkmcnt(7)\n" \
"v_mfma_f32_16x16x32_bf16 v[244:247], v[104:107], v[220:223], v[244:247]\n" \
"v_mfma_f32_16x16x32_bf16 v[248:251], v[120:123], v[220:223], v[248:251]\n" \
"s_waitcnt lgkmcnt(6)\n" \
"v_mfma_f32_16x16x32_bf16 v[244:247], v[108:111], v[224:227], v[244:247]\n" \
"v_mfma_f32_16x16x32_bf16 v[248:251], v[124:127], v[224:227], v[248:251]\n" \
"s_cmp_eq_u32 s35, 0\n" \
"s_cbranch_scc1 QF0_%=\n" \
"s_cbranch_vccz QX_%=\n" \
"s_cmp_lg_u32 s64, 0\n" \
"s_cbranch_scc1 QV_%=\n" \
"QH_%=:\n" \
"buffer_load_dword v15, off, s[40:43], s62 sc1\n" \
"s_waitcnt vmcnt(0)\n" \
"v_readfirstlane_b32 s63, v15\n" \
"s_cmp_ge_u32 s63, s35\n" \
"s_cbranch_scc1 QW_%=\n" \
"s_sleep 1\n" \
"s_sub_u32 s50, s50, 1\n" \
"s_cbranch_scc0 QH_%=\n" \
"s_mov_b32 s50, 0\n" \
"QW_%=:\n" \
"buffer_load_dwordx4 v[64:67], v0, s[40:43], s56 offen offset:0 sc1\n" \
"buffer_load_dwordx4 v[68:71], v0, s[40:43], s56 offen offset:1024 sc1\n" \
"buffer_load_dwordx4 v[72:75], v0, s[40:43], s56 offen offset:2048 sc1\n" \
"buffer_load_dwordx4 v[76:79], v0, s[40:43], s56 offen offset:3072 sc1\n" \
"QV_%=:\n" \
"s_waitcnt vmcnt(0)\n" \
"v_max3_u32 v180, v64, v65, v66\n" \
"v_max3_u32 v180, v180, v68, v69\n" \
"v_max3_u32 v180, v180, v70, v72\n" \
"v_max3_u32 v180, v180, v73, v74\n" \
"v_max3_u32 v180, v180, v76, v77\n" \
"v_max_u32 v180, v180, v78\n" \
"v_cmp_eq_u32 vcc, 0xff81ff81, v180\n" \
"ds_write2_b32 v8, v64, v65 offset0:0 offset1:32\n" \
"ds_write2_b32 v8, v68, v69 offset0:64 offset1:96\n" \
"ds_write2_b32 v8, v72, v73 offset0:128 offset1:160\n" \
"ds_write2_b32 v8, v76, v77 offset0:192 offset1:224\n" \
"ds_read_b64_tr_b16 v[144:145], v9 offset:0\n" \
"ds_read_b64_tr_b16 v[146:147], v9 offset:128\n" \
"ds_read_b64_tr_b16 v[148:149], v9 offset:256\n" \
"ds_read_b64_tr_b16 v[150:151], v9 offset:384\n" \
"ds_read_b64_tr_b16 v[152:153], v9 offset:512\n" \
"ds_read_b64_tr_b16 v[154:155], v9 offset:640\n" \
"ds_read_b64_tr_b16 v[156:157], v9 offset:768\n" \
"ds_read_b64_tr_b16 v[158:159], v9 offset:896\n" \
"s_cbranch_vccz QX_%=\n" \
"s_sleep 6\n" \
"s_sub_u32 s50, s50, 1\n" \
"s_cbranch_scc0 QW_%=\n" \
"s_mov_b32 s50, 0\n" \
"QX_%=:\n" \
"buffer_load_dword v15, off, s[40:43], s62 sc1\n" \
"s_waitcnt lgkmcnt(6)\n" \
"v_mfma_f32_16x16x32_bf16 v[168:171], v[144:147], v[80:83], 0\n" \
"v_mov_b32 v208, v66\n" \
"v_mov_b32 v209, v70\n" \
"s_waitcnt lgkmcnt(4)\n" \
"v_mfma_f32_16x16x32_bf16 v[168:171], v[148:151], v[84:87], v[168:171]\n" \
"v_mov_b32 v210, v74\n" \
"v_mov_b32 v211, v78\n" \
"s_waitcnt lgkmcnt(2)\n" \
"v_mfma_f32_16x16x32_bf16 v[168:171], v[152:155], v[88:91], v[168:171]\n" \
"v_cndmask_b32 v160, v13, v160, s[70:71]\n" \
"v_cndmask_b32 v161, v13, v161, s[72:73]\n" \
"s_waitcnt lgkmcnt(0)\n" \
"v_mfma_f32_16x16x32_bf16 v[168:171], v[156:159], v[92:95], v[168:171]\n" \
"v_cndmask_b32 v162, v13, v162, s[74:75]\n" \
"v_cndmask_b32 v163, v13, v163, s[76:77]\n" \
"s_add_u32 m0, s59, 0\n" \
"s_add_u32 s48, s55, 0x0\n" \
"buffer_load_dwordx4 v0, s[40:43], s48 offen sc1 lds\n" \
"v_cndmask_b32 v164, v13, v164, s[78:79]\n" \
"v_cndmask_b32 v165, v13, v165, s[80:81]\n" \
"v_cndmask_b32 v166, v13, v166, s[82:83]\n" \
"v_cndmask_b32 v167, v13, v167, s[84:85]\n" \
"v_max3_f32 v182, v160, v161, v162\n" \
"v_max3_f32 v182, v182, v163, v164\n" \
"v_max3_f32 v182, v182, v165, v166\n" \
"v_max_f32 v182, v182, v167\n" \
"v_max3_f32 v180, v182, v168, v169\n" \
"v_max3_f32 v182, v182, v168, v169\n" \
"s_nop 1\n" \
"v_permlane32_swap_b32 v182, v180\n" \
"v_max_f32 v181, v182, v180\n" \
"v_max_f32 v180, v182, v180\n" \
"s_nop 1\n" \
"v_permlane16_swap_b32 v181, v180\n" \
"v_max_f32 v180, v180, v181\n" \
"s_add_u32 m0, s59, 4096\n" \
"s_add_u32 s48, s55, 0x1000\n" \
"buffer_load_dwordx4 v0, s[40:43], s48 offen sc1 lds\n" \
"v_pk_add_f32 v[160:161], v[160:161], v[180:181] op_sel_hi:[1,0] neg_lo:[0,1] neg_hi:[0,1]\n" \
"v_pk_add_f32 v[162:163], v[162:163], v[180:181] op_sel_hi:[1,0] neg_lo:[0,1] neg_hi:[0,1]\n" \
"v_pk_add_f32 v[164:165], v[164:165], v[180:181] op_sel_hi:[1,0] neg_lo:[0,1] neg_hi:[0,1]\n" \
"v_pk_add_f32 v[166:167], v[166:167], v[180:181] op_sel_hi:[1,0] neg_lo:[0,1] neg_hi:[0,1]\n" \
"v_pk_add_f32 v[168:169], v[168:169], v[180:181] op_sel_hi:[1,0] neg_lo:[0,1] neg_hi:[0,1]\n" \
"v_exp_f32 v160, v160\n" \
"v_exp_f32 v161, v161\n" \
"v_exp_f32 v162, v162\n" \
"v_exp_f32 v163, v163\n" \
"v_exp_f32 v164, v164\n" \
"v_cvt_pk_bf16_f32 v172, v160, v161\n" \
"v_exp_f32 v165, v165\n" \
"v_cvt_pk_bf16_f32 v173, v162, v163\n" \
"v_exp_f32 v166, v166\n" \
"v_pk_add_f32 v[160:161], v[160:161], v[162:163]\n" \
"v_exp_f32 v167, v167\n" \
"v_cvt_pk_bf16_f32 v174, v164, v165\n" \
"v_cvt_pk_bf16_f32 v175, v166, v167\n" \
"v_pk_add_f32 v[164:165], v[164:165], v[166:167]\n" \
"s_nop 0\n" \
"v_pk_add_f32 v[160:161], v[160:161], v[164:165]\n" \
"s_add_u32 s56, s56, 0x200000\n" \
"s_xor_b32 s56, s56, s69\n" \
"s_add_u32 m0, s59, 8192\n" \
"s_add_u32 s48, s55, 0x80000\n" \
"buffer_load_dwordx4 v0, s[40:43], s48 offen sc1 lds\n" \
"v_mfma_f32_16x16x32_bf16 v[184:187], v[128:131], v[172:175], 0\n" \
"v_exp_f32 v168, v168\n" \
"v_mfma_f32_16x16x32_bf16 v[188:191], v[132:135], v[172:175], 0\n" \
"v_exp_f32 v169, v169\n" \
"v_mfma_f32_16x16x32_bf16 v[192:195], v[136:139], v[172:175], 0\n" \
"v_cvt_pk_bf16_f32 v176, v168, v169\n" \
"v_cvt_pk_bf16_f32 v179, v168, v169\n" \
"v_mfma_f32_16x16x32_bf16 v[196:199], v[140:143], v[172:175], 0\n" \
"v_add_f32 v160, v160, v161\n" \
"v_add_f32 v168, v168, v169\n" \
"v_mfma_f32_16x16x16_bf16 v[184:187], v[208:209], v[176:177], v[184:187]\n" \
"v_add_f32 v182, v160, v168\n" \
"v_add_f32 v180, v160, v168\n" \
"v_mfma_f32_16x16x16_bf16 v[188:191], v[208:209], v[178:179], v[188:191]\n" \
"s_add_u32 m0, s59, 12288\n" \
"s_add_u32 s48, s55, 0x81000\n" \
"buffer_load_dwordx4 v0, s[40:43], s48 offen sc1 lds\n" \
"v_mfma_f32_16x16x16_bf16 v[192:195], v[210:211], v[176:177], v[192:195]\n" \
"v_permlane32_swap_b32 v182, v180\n" \
"v_mfma_f32_16x16x16_bf16 v[196:199], v[210:211], v[178:179], v[196:199]\n" \
"v_add_f32 v181, v182, v180\n" \
"v_add_f32 v180, v182, v180\n" \
"s_nop 1\n" \
"v_permlane16_swap_b32 v181, v180\n" \
"v_add_f32 v180, v180, v181\n" \
"v_rcp_f32 v180, v180\n" \
"s_nop 0\n" \
"v_pk_mul_f32 v[184:185], v[184:185], v[180:181] op_sel_hi:[1,0]\n" \
"v_pk_mul_f32 v[186:187], v[186:187], v[180:181] op_sel_hi:[1,0]\n" \
"v_pk_mul_f32 v[188:189], v[188:189], v[180:181] op_sel_hi:[1,0]\n" \
"v_pk_mul_f32 v[190:191], v[190:191], v[180:181] op_sel_hi:[1,0]\n" \
"v_cvt_pk_bf16_f32 v200, v184, v185\n" \
"v_cvt_pk_bf16_f32 v201, v186, v187\n" \
"v_cvt_pk_bf16_f32 v202, v188, v189\n" \
"v_cvt_pk_bf16_f32 v203, v190, v191\n" \
"buffer_store_dwordx4 v[200:203], v0, s[40:43], s57 offen offset:0 sc1\n" \
"v_pk_mul_f32 v[192:193], v[192:193], v[180:181] op_sel_hi:[1,0]\n" \
"v_pk_mul_f32 v[194:195], v[194:195], v[180:181] op_sel_hi:[1,0]\n" \
"v_pk_mul_f32 v[196:197], v[196:197], v[180:181] op_sel_hi:[1,0]\n" \
"v_pk_mul_f32 v[198:199], v[198:199], v[180:181] op_sel_hi:[1,0]\n" \
"v_cvt_pk_bf16_f32 v204, v192, v193\n" \
"v_cvt_pk_bf16_f32 v205, v194, v195\n" \
"v_cvt_pk_bf16_f32 v206, v196, v197\n" \
"v_cvt_pk_bf16_f32 v207, v198, v199\n" \
"buffer_store_dwordx4 v[204:207], v0, s[40:43], s57 offen offset:1024 sc1\n" \
"s_waitcnt vmcnt(6)\n" \
"v_readfirstlane_b32 s63, v15\n" \
"v_mov_b32 v64, 0xff81ff81\n" \
"s_cmp_gt_u32 s63, s35\n" \
"s_cselect_b32 s64, 0, 1\n" \
"s_cbranch_scc0 QZ_%=\n" \
"buffer_load_dwordx4 v[64:67], v0, s[40:43], s56 offen offset:0 sc1\n" \
"buffer_load_dwordx4 v[68:71], v0, s[40:43], s56 offen offset:1024 sc1\n" \
"buffer_load_dwordx4 v[72:75], v0, s[40:43], s56 offen offset:2048 sc1\n" \
"buffer_load_dwordx4 v[76:79], v0, s[40:43], s56 offen offset:3072 sc1\n" \
"QZ_%=:\n" \
"s_add_u32 s57, s57, 0x200000\n" \
"s_xor_b32 s57, s57, s69\n" \
"s_mov_b32 s65, s35\n" \
"QN_%=:\n" \
"s_waitcnt lgkmcnt(0)\n" \
"s_add_u32 s35, s35, 1\n" \
"s_cmp_lt_u32 s35, 64\n" \
"s_cbranch_scc1 QL_%=\n" \
"s_cmp_gt_u32 s35, 64\n" \
"s_cbranch_scc1 QD_%=\n" \
"v_mov_b64 v[80:81], v[212:213]\n" \
"v_mov_b64 v[82:83], v[214:215]\n" \
"v_mov_b64 v[84:85], v[216:217]\n" \
"v_mov_b64 v[86:87], v[218:219]\n" \
"v_mov_b64 v[88:89], v[220:221]\n" \
"v_mov_b64 v[90:91], v[222:223]\n" \
"v_mov_b64 v[92:93], v[224:225]\n" \
"v_mov_b64 v[94:95], v[226:227]\n" \
"v_mov_b64 v[128:129], v[228:229]\n" \
"v_mov_b64 v[130:131], v[230:231]\n" \
"v_mov_b64 v[132:133], v[232:233]\n" \
"v_mov_b64 v[134:135], v[234:235]\n" \
"v_mov_b64 v[136:137], v[236:237]\n" \
"v_mov_b64 v[138:139], v[238:239]\n" \
"v_mov_b64 v[140:141], v[240:241]\n" \
"v_mov_b64 v[142:143], v[242:243]\n" \
"v_mov_b64 v[160:161], v[244:245]\n" \
"v_mov_b64 v[162:163], v[246:247]\n" \
"v_mov_b64 v[164:165], v[248:249]\n" \
"v_mov_b64 v[166:167], v[250:251]\n" \
"s_waitcnt vmcnt(0)\n" \
"s_barrier\n" \
"v_mov_b32 v12, s65\n" \
"s_mov_b64 exec, 1\n" \
"buffer_store_dword v12, off, s[40:43], s51 sc1\n" \
"s_mov_b64 exec, -1\n" \
"s_cmp_eq_u32 s64, 0\n" \
"s_cbranch_scc1 QV_%=\n" \
"s_branch QH_%=\n" \
"QF0_%=:\n" \
"s_add_u32 m0, s59, 0\n" \
"s_add_u32 s48, s55, 0x0\n" \
"buffer_load_dwordx4 v0, s[40:43], s48 offen sc1 lds\n" \
"s_add_u32 m0, s59, 4096\n" \
"s_add_u32 s48, s55, 0x1000\n" \
"buffer_load_dwordx4 v0, s[40:43], s48 offen sc1 lds\n" \
"s_add_u32 m0, s59, 8192\n" \
"s_add_u32 s48, s55, 0x80000\n" \
"buffer_load_dwordx4 v0, s[40:43], s48 offen sc1 lds\n" \
"s_add_u32 m0, s59, 12288\n" \
"s_add_u32 s48, s55, 0x81000\n" \
"buffer_load_dwordx4 v0, s[40:43], s48 offen sc1 lds\n" \
"s_mov_b32 s64, 1\n" \
"s_nop 7\n" \
"s_branch QN_%=\n" \
"QD_%=:\n" \
"s_waitcnt vmcnt(0)\n" \
"s_barrier\n" \
"v_mov_b32 v12, 64\n" \
"s_mov_b64 exec, 1\n" \
"buffer_store_dword v12, off, s[40:43], s51 sc1\n" \
"s_mov_b64 exec, -1\n" \
"s_cmp_lg_u32 s50, 0\n" \
"s_cbranch_scc1 QE_%=\n" \
"v_mov_b32 v0, 1\n" \
"buffer_store_dword v0, off, s[40:43], 0 offset:256 sc1\n" \
"QE_%=:\n" \
"QCZ_%=:\n" \
"s_endpgm\n"
#define WO_ASM_TEXT \
"s_getpc_b64 s[46:47]\n" \
"WCW_%=:\n" \
"v_mbcnt_lo_u32_b32 v60, -1, 0\n" \
"v_mbcnt_hi_u32_b32 v60, -1, v60\n" \
"v_lshlrev_b32 v60, 7, v60\n" \
"v_min_u32 v61, WCZ_%=-WCW_%=, v60\n" \
"global_load_dword v62, v61, s[46:47]\n" \
"v_add_u32 v60, 0x2000, v60\n" \
"v_min_u32 v61, WCZ_%=-WCW_%=, v60\n" \
"global_load_dword v62, v61, s[46:47]\n" \
"v_add_u32 v60, 0x2000, v60\n" \
"s_mov_b64 s[40:41], %[ws]\n" \
"s_add_u32 s40, s40, 0x800000\n" \
"s_addc_u32 s41, s41, 0\n" \
"s_and_b32 s41, s41, 0xffff\n" \
"s_mov_b32 s42, 0x9000000\n" \
"s_mov_b32 s43, 0x00020000\n" \
"s_mov_b32 s33, %[wave]\n" \
"v_mbcnt_lo_u32_b32 v48, -1, 0\n" \
"v_mbcnt_hi_u32_b32 v48, -1, v48\n" \
"s_lshl_b32 s48, s33, 6\n" \
"v_add_lshl_u32 v48, v48, s48, 4\n" \
"ds_read_b128 v[0:3], v48 offset:0\n" \
"ds_read_b128 v[4:7], v48 offset:4096\n" \
"ds_read_b128 v[32:35], v48 offset:8192\n" \
"ds_read_b128 v[36:39], v48 offset:12288\n" \
"ds_read_b128 v[40:43], v48 offset:16384\n" \
"s_waitcnt lgkmcnt(0)\n" \
"s_barrier\n" \
"v_readfirstlane_b32 s70, v32\n" \
"v_readfirstlane_b32 s71, v33\n" \
"v_readfirstlane_b32 s72, v34\n" \
"v_readfirstlane_b32 s73, v35\n" \
"v_readfirstlane_b32 s51, v36\n" \
"v_readfirstlane_b32 s53, v37\n" \
"v_readfirstlane_b32 s55, v38\n" \
"s_nop 4\n" \
"s_lshl_b32 s34, s33, 10\n" \
"s_cmp_eq_u32 s33, 0\n" \
"s_cselect_b32 s51, s51, 0x7ff00000\n" \
"s_mov_b32 s50, 0x80000\n" \
"s_mov_b32 s35, 0\n" \
"s_mov_b64 s[44:45], s[70:71]\n" \
"s_and_b32 s45, s45, 0xffff\n" \
"s_mov_b32 s46, 0x140000\n" \
"s_mov_b32 s47, 0x00020000\n" \
"s_sleep 45\n" \
"s_add_u32 m0, s34, 0\n" \
"s_mov_b32 s48, 0x0\n" \
"buffer_load_dwordx4 v1, s[44:47], s48 offen lds\n" \
"s_add_u32 m0, s34, 4096\n" \
"s_mov_b32 s48, 0x5000\n" \
"buffer_load_dwordx4 v1, s[44:47], s48 offen lds\n" \
"s_add_u32 m0, s34, 8192\n" \
"s_mov_b32 s48, 0xa000\n" \
"buffer_load_dwordx4 v1, s[44:47], s48 offen lds\n" \
"s_add_u32 m0, s34, 12288\n" \
"s_mov_b32 s48, 0xf000\n" \
"buffer_load_dwordx4 v1, s[44:47], s48 offen lds\n" \
"s_add_u32 m0, s34, 16384\n" \
"s_mov_b32 s48, 0x14000\n" \
"buffer_load_dwordx4 v1, s[44:47], s48 offen lds\n" \
"s_add_u32 m0, s34, 20480\n" \
"s_mov_b32 s48, 0x19000\n" \
"buffer_load_dwordx4 v1, s[44:47], s48 offen lds\n" \
"s_add_u32 m0, s34, 24576\n" \
"s_mov_b32 s48, 0x1e000\n" \
"buffer_load_dwordx4 v1, s[44:47], s48 offen lds\n" \
"s_add_u32 m0, s34, 28672\n" \
"s_mov_b32 s48, 0x23000\n" \
"buffer_load_dwordx4 v1, s[44:47], s48 offen lds\n" \
"s_add_u32 m0, s34, 32768\n" \
"s_mov_b32 s48, 0x28000\n" \
"buffer_load_dwordx4 v1, s[44:47], s48 offen lds\n" \
"s_add_u32 m0, s34, 36864\n" \
"s_mov_b32 s48, 0x2d000\n" \
"buffer_load_dwordx4 v1, s[44:47], s48 offen lds\n" \
"s_add_u32 m0, s34, 40960\n" \
"s_mov_b32 s48, 0x32000\n" \
"buffer_load_dwordx4 v1, s[44:47], s48 offen lds\n" \
"s_add_u32 m0, s34, 45056\n" \
"s_mov_b32 s48, 0x37000\n" \
"buffer_load_dwordx4 v1, s[44:47], s48 offen lds\n" \
"s_add_u32 m0, s34, 49152\n" \
"s_mov_b32 s48, 0x3c000\n" \
"buffer_load_dwordx4 v1, s[44:47], s48 offen lds\n" \
"s_add_u32 m0, s34, 53248\n" \
"s_mov_b32 s48, 0x41000\n" \
"buffer_load_dwordx4 v1, s[44:47], s48 offen lds\n" \
"s_add_u32 m0, s34, 57344\n" \
"s_mov_b32 s48, 0x46000\n" \
"buffer_load_dwordx4 v1, s[44:47], s48 offen lds\n" \
"s_add_u32 m0, s34, 61440\n" \
"s_mov_b32 s48, 0x4b000\n" \
"buffer_load_dwordx4 v1, s[44:47], s48 offen lds\n" \
"s_add_u32 m0, s34, 65536\n" \
"s_mov_b32 s48, 0x50000\n" \
"buffer_load_dwordx4 v1, s[44:47], s48 offen lds\n" \
"s_add_u32 m0, s34, 69632\n" \
"s_mov_b32 s48, 0x55000\n" \
"buffer_load_dwordx4 v1, s[44:47], s48 offen lds\n" \
"s_add_u32 m0, s34, 73728\n" \
"s_mov_b32 s48, 0x5a000\n" \
"buffer_load_dwordx4 v1, s[44:47], s48 offen lds\n" \
"s_add_u32 m0, s34, 77824\n" \
"s_mov_b32 s48, 0x5f000\n" \
"buffer_load_dwordx4 v1, s[44:47], s48 offen lds\n" \
"s_add_u32 m0, s34, 81920\n" \
"s_mov_b32 s48, 0x64000\n" \
"buffer_load_dwordx4 v1, s[44:47], s48 offen lds\n" \
"s_add_u32 m0, s34, 86016\n" \
"s_mov_b32 s48, 0x69000\n" \
"buffer_load_dwordx4 v1, s[44:47], s48 offen lds\n" \
"s_add_u32 m0, s34, 90112\n" \
"s_mov_b32 s48, 0x6e000\n" \
"buffer_load_dwordx4 v1, s[44:47], s48 offen lds\n" \
"s_add_u32 m0, s34, 94208\n" \
"s_mov_b32 s48, 0x73000\n" \
"buffer_load_dwordx4 v1, s[44:47], s48 offen lds\n" \
"s_add_u32 m0, s34, 98304\n" \
"s_mov_b32 s48, 0x78000\n" \
"buffer_load_dwordx4 v1, s[44:47], s48 offen lds\n" \
"s_add_u32 m0, s34, 102400\n" \
"s_mov_b32 s48, 0x7d000\n" \
"buffer_load_dwordx4 v1, s[44:47], s48 offen lds\n" \
"s_add_u32 m0, s34, 106496\n" \
"s_mov_b32 s48, 0x82000\n" \
"buffer_load_dwordx4 v1, s[44:47], s48 offen lds\n" \
"s_add_u32 m0, s34, 110592\n" \
"s_mov_b32 s48, 0x87000\n" \
"buffer_load_dwordx4 v1, s[44:47], s48 offen lds\n" \
"s_add_u32 m0, s34, 114688\n" \
"s_mov_b32 s48, 0x8c000\n" \
"buffer_load_dwordx4 v1, s[44:47], s48 offen lds\n" \
"s_add_u32 m0, s34, 118784\n" \
"s_mov_b32 s48, 0x91000\n" \
"buffer_load_dwordx4 v1, s[44:47], s48 offen lds\n" \
"s_add_u32 m0, s34, 122880\n" \
"s_mov_b32 s48, 0x96000\n" \
"buffer_load_dwordx4 v1, s[44:47], s48 offen lds\n" \
"s_add_u32 m0, s34, 126976\n" \
"s_mov_b32 s48, 0x9b000\n" \
"buffer_load_dwordx4 v1, s[44:47], s48 offen lds\n" \
"s_add_u32 m0, s34, 131072\n" \
"s_mov_b32 s48, 0xa0000\n" \
"buffer_load_dwordx4 v1, s[44:47], s48 offen lds\n" \
"s_add_u32 m0, s34, 135168\n" \
"s_mov_b32 s48, 0xa5000\n" \
"buffer_load_dwordx4 v1, s[44:47], s48 offen lds\n" \
"s_add_u32 m0, s34, 139264\n" \
"s_mov_b32 s48, 0xaa000\n" \
"buffer_load_dwordx4 v1, s[44:47], s48 offen lds\n" \
"s_add_u32 m0, s34, 143360\n" \
"s_mov_b32 s48, 0xaf000\n" \
"buffer_load_dwordx4 v1, s[44:47], s48 offen lds\n" \
"s_add_u32 m0, s34, 147456\n" \
"s_mov_b32 s48, 0xb4000\n" \
"buffer_load_dwordx4 v1, s[44:47], s48 offen lds\n" \
"s_add_u32 m0, s34, 151552\n" \
"s_mov_b32 s48, 0xb9000\n" \
"buffer_load_dwordx4 v1, s[44:47], s48 offen lds\n" \
"s_add_u32 m0, s34, 155648\n" \
"s_mov_b32 s48, 0xbe000\n" \
"buffer_load_dwordx4 v1, s[44:47], s48 offen lds\n" \
"s_add_u32 m0, s34, 159744\n" \
"s_mov_b32 s48, 0xc3000\n" \
"buffer_load_dwordx4 v1, s[44:47], s48 offen lds\n" \
"s_waitcnt vmcnt(32)\n" \
"s_barrier\n" \
"v_add_u32 v20, 0x0, v2\n" \
"ds_read_b64_tr_b16 a[0:1], v20 offset:0\n" \
"ds_read_b64_tr_b16 a[2:3], v20 offset:4096\n" \
"ds_read_b64_tr_b16 a[8:9], v20 offset:8192\n" \
"ds_read_b64_tr_b16 a[10:11], v20 offset:12288\n" \
"ds_read_b64_tr_b16 a[16:17], v20 offset:16384\n" \
"ds_read_b64_tr_b16 a[18:19], v20 offset:20480\n" \
"ds_read_b64_tr_b16 a[24:25], v20 offset:24576\n" \
"ds_read_b64_tr_b16 a[26:27], v20 offset:28672\n" \
"v_add_u32 v20, 0x0, v3\n" \
"ds_read_b64_tr_b16 a[4:5], v20 offset:0\n" \
"ds_read_b64_tr_b16 a[6:7], v20 offset:4096\n" \
"ds_read_b64_tr_b16 a[12:13], v20 offset:8192\n" \
"ds_read_b64_tr_b16 a[14:15], v20 offset:12288\n" \
"ds_read_b64_tr_b16 a[20:21], v20 offset:16384\n" \
"ds_read_b64_tr_b16 a[22:23], v20 offset:20480\n" \
"ds_read_b64_tr_b16 a[28:29], v20 offset:24576\n" \
"ds_read_b64_tr_b16 a[30:31], v20 offset:28672\n" \
"s_waitcnt lgkmcnt(0)\n" \
"s_barrier\n" \
"s_add_u32 m0, s34, 0\n" \
"s_mov_b32 s48, 0xc8000\n" \
"buffer_load_dwordx4 v1, s[44:47], s48 offen lds\n" \
"s_add_u32 m0, s34, 4096\n" \
"s_mov_b32 s48, 0xcd000\n" \
"buffer_load_dwordx4 v1, s[44:47], s48 offen lds\n" \
"s_add_u32 m0, s34, 8192\n" \
"s_mov_b32 s48, 0xd2000\n" \
"buffer_load_dwordx4 v1, s[44:47], s48 offen lds\n" \
"s_add_u32 m0, s34, 12288\n" \
"s_mov_b32 s48, 0xd7000\n" \
"buffer_load_dwordx4 v1, s[44:47], s48 offen lds\n" \
"s_add_u32 m0, s34, 16384\n" \
"s_mov_b32 s48, 0xdc000\n" \
"buffer_load_dwordx4 v1, s[44:47], s48 offen lds\n" \
"s_add_u32 m0, s34, 20480\n" \
"s_mov_b32 s48, 0xe1000\n" \
"buffer_load_dwordx4 v1, s[44:47], s48 offen lds\n" \
"s_add_u32 m0, s34, 24576\n" \
"s_mov_b32 s48, 0xe6000\n" \
"buffer_load_dwordx4 v1, s[44:47], s48 offen lds\n" \
"s_add_u32 m0, s34, 28672\n" \
"s_mov_b32 s48, 0xeb000\n" \
"buffer_load_dwordx4 v1, s[44:47], s48 offen lds\n" \
"s_waitcnt vmcnt(32)\n" \
"s_barrier\n" \
"v_add_u32 v20, 0x8000, v2\n" \
"ds_read_b64_tr_b16 a[32:33], v20 offset:0\n" \
"ds_read_b64_tr_b16 a[34:35], v20 offset:4096\n" \
"ds_read_b64_tr_b16 a[40:41], v20 offset:8192\n" \
"ds_read_b64_tr_b16 a[42:43], v20 offset:12288\n" \
"ds_read_b64_tr_b16 a[48:49], v20 offset:16384\n" \
"ds_read_b64_tr_b16 a[50:51], v20 offset:20480\n" \
"ds_read_b64_tr_b16 a[56:57], v20 offset:24576\n" \
"ds_read_b64_tr_b16 a[58:59], v20 offset:28672\n" \
"v_add_u32 v20, 0x8000, v3\n" \
"ds_read_b64_tr_b16 a[36:37], v20 offset:0\n" \
"ds_read_b64_tr_b16 a[38:39], v20 offset:4096\n" \
"ds_read_b64_tr_b16 a[44:45], v20 offset:8192\n" \
"ds_read_b64_tr_b16 a[46:47], v20 offset:12288\n" \
"ds_read_b64_tr_b16 a[52:53], v20 offset:16384\n" \
"ds_read_b64_tr_b16 a[54:55], v20 offset:20480\n" \
"ds_read_b64_tr_b16 a[60:61], v20 offset:24576\n" \
"ds_read_b64_tr_b16 a[62:63], v20 offset:28672\n" \
"s_waitcnt lgkmcnt(0)\n" \
"s_barrier\n" \
"s_add_u32 m0, s34, 32768\n" \
"s_mov_b32 s48, 0xf0000\n" \
"buffer_load_dwordx4 v1, s[44:47], s48 offen lds\n" \
"s_add_u32 m0, s34, 36864\n" \
"s_mov_b32 s48, 0xf5000\n" \
"buffer_load_dwordx4 v1, s[44:47], s48 offen lds\n" \
"s_add_u32 m0, s34, 40960\n" \
"s_mov_b32 s48, 0xfa000\n" \
"buffer_load_dwordx4 v1, s[44:47], s48 offen lds\n" \
"s_add_u32 m0, s34, 45056\n" \
"s_mov_b32 s48, 0xff000\n" \
"buffer_load_dwordx4 v1, s[44:47], s48 offen lds\n" \
"s_add_u32 m0, s34, 49152\n" \
"s_mov_b32 s48, 0x104000\n" \
"buffer_load_dwordx4 v1, s[44:47], s48 offen lds\n" \
"s_add_u32 m0, s34, 53248\n" \
"s_mov_b32 s48, 0x109000\n" \
"buffer_load_dwordx4 v1, s[44:47], s48 offen lds\n" \
"s_add_u32 m0, s34, 57344\n" \
"s_mov_b32 s48, 0x10e000\n" \
"buffer_load_dwordx4 v1, s[44:47], s48 offen lds\n" \
"s_add_u32 m0, s34, 61440\n" \
"s_mov_b32 s48, 0x113000\n" \
"buffer_load_dwordx4 v1, s[44:47], s48 offen lds\n" \
"s_waitcnt vmcnt(32)\n" \
"s_barrier\n" \
"v_add_u32 v20, 0x10000, v2\n" \
"ds_read_b64_tr_b16 a[64:65], v20 offset:0\n" \
"ds_read_b64_tr_b16 a[66:67], v20 offset:4096\n" \
"ds_read_b64_tr_b16 a[72:73], v20 offset:8192\n" \
"ds_read_b64_tr_b16 a[74:75], v20 offset:12288\n" \
"ds_read_b64_tr_b16 a[80:81], v20 offset:16384\n" \
"ds_read_b64_tr_b16 a[82:83], v20 offset:20480\n" \
"ds_read_b64_tr_b16 a[88:89], v20 offset:24576\n" \
"ds_read_b64_tr_b16 a[90:91], v20 offset:28672\n" \
"v_add_u32 v20, 0x10000, v3\n" \
"ds_read_b64_tr_b16 a[68:69], v20 offset:0\n" \
"ds_read_b64_tr_b16 a[70:71], v20 offset:4096\n" \
"ds_read_b64_tr_b16 a[76:77], v20 offset:8192\n" \
"ds_read_b64_tr_b16 a[78:79], v20 offset:12288\n" \
"ds_read_b64_tr_b16 a[84:85], v20 offset:16384\n" \
"ds_read_b64_tr_b16 a[86:87], v20 offset:20480\n" \
"ds_read_b64_tr_b16 a[92:93], v20 offset:24576\n" \
"ds_read_b64_tr_b16 a[94:95], v20 offset:28672\n" \
"s_waitcnt lgkmcnt(0)\n" \
"s_barrier\n" \
"s_add_u32 m0, s34, 65536\n" \
"s_mov_b32 s48, 0x118000\n" \
"buffer_load_dwordx4 v1, s[44:47], s48 offen lds\n" \
"s_add_u32 m0, s34, 69632\n" \
"s_mov_b32 s48, 0x11d000\n" \
"buffer_load_dwordx4 v1, s[44:47], s48 offen lds\n" \
"s_add_u32 m0, s34, 73728\n" \
"s_mov_b32 s48, 0x122000\n" \
"buffer_load_dwordx4 v1, s[44:47], s48 offen lds\n" \
"s_add_u32 m0, s34, 77824\n" \
"s_mov_b32 s48, 0x127000\n" \
"buffer_load_dwordx4 v1, s[44:47], s48 offen lds\n" \
"s_add_u32 m0, s34, 81920\n" \
"s_mov_b32 s48, 0x12c000\n" \
"buffer_load_dwordx4 v1, s[44:47], s48 offen lds\n" \
"s_add_u32 m0, s34, 86016\n" \
"s_mov_b32 s48, 0x131000\n" \
"buffer_load_dwordx4 v1, s[44:47], s48 offen lds\n" \
"s_add_u32 m0, s34, 90112\n" \
"s_mov_b32 s48, 0x136000\n" \
"buffer_load_dwordx4 v1, s[44:47], s48 offen lds\n" \
"s_add_u32 m0, s34, 94208\n" \
"s_mov_b32 s48, 0x13b000\n" \
"buffer_load_dwordx4 v1, s[44:47], s48 offen lds\n" \
"s_waitcnt vmcnt(32)\n" \
"s_barrier\n" \
"v_add_u32 v20, 0x18000, v2\n" \
"ds_read_b64_tr_b16 a[96:97], v20 offset:0\n" \
"ds_read_b64_tr_b16 a[98:99], v20 offset:4096\n" \
"ds_read_b64_tr_b16 a[104:105], v20 offset:8192\n" \
"ds_read_b64_tr_b16 a[106:107], v20 offset:12288\n" \
"ds_read_b64_tr_b16 a[112:113], v20 offset:16384\n" \
"ds_read_b64_tr_b16 a[114:115], v20 offset:20480\n" \
"ds_read_b64_tr_b16 a[120:121], v20 offset:24576\n" \
"ds_read_b64_tr_b16 a[122:123], v20 offset:28672\n" \
"v_add_u32 v20, 0x18000, v3\n" \
"ds_read_b64_tr_b16 a[100:101], v20 offset:0\n" \
"ds_read_b64_tr_b16 a[102:103], v20 offset:4096\n" \
"ds_read_b64_tr_b16 a[108:109], v20 offset:8192\n" \
"ds_read_b64_tr_b16 a[110:111], v20 offset:12288\n" \
"ds_read_b64_tr_b16 a[116:117], v20 offset:16384\n" \
"ds_read_b64_tr_b16 a[118:119], v20 offset:20480\n" \
"ds_read_b64_tr_b16 a[124:125], v20 offset:24576\n" \
"ds_read_b64_tr_b16 a[126:127], v20 offset:28672\n" \
"s_waitcnt lgkmcnt(0)\n" \
"s_barrier\n" \
"s_waitcnt vmcnt(24)\n" \
"s_barrier\n" \
"v_add_u32 v20, 0x20000, v2\n" \
"ds_read_b64_tr_b16 a[128:129], v20 offset:0\n" \
"ds_read_b64_tr_b16 a[130:131], v20 offset:4096\n" \
"ds_read_b64_tr_b16 a[136:137], v20 offset:8192\n" \
"ds_read_b64_tr_b16 a[138:139], v20 offset:12288\n" \
"ds_read_b64_tr_b16 a[144:145], v20 offset:16384\n" \
"ds_read_b64_tr_b16 a[146:147], v20 offset:20480\n" \
"ds_read_b64_tr_b16 a[152:153], v20 offset:24576\n" \
"ds_read_b64_tr_b16 a[154:155], v20 offset:28672\n" \
"v_add_u32 v20, 0x20000, v3\n" \
"ds_read_b64_tr_b16 a[132:133], v20 offset:0\n" \
"ds_read_b64_tr_b16 a[134:135], v20 offset:4096\n" \
"ds_read_b64_tr_b16 a[140:141], v20 offset:8192\n" \
"ds_read_b64_tr_b16 a[142:143], v20 offset:12288\n" \
"ds_read_b64_tr_b16 a[148:149], v20 offset:16384\n" \
"ds_read_b64_tr_b16 a[150:151], v20 offset:20480\n" \
"ds_read_b64_tr_b16 a[156:157], v20 offset:24576\n" \
"ds_read_b64_tr_b16 a[158:159], v20 offset:28672\n" \
"s_waitcnt lgkmcnt(0)\n" \
"s_barrier\n" \
"s_waitcnt vmcnt(16)\n" \
"s_barrier\n" \
"v_add_u32 v20, 0x0, v2\n" \
"ds_read_b64_tr_b16 a[160:161], v20 offset:0\n" \
"ds_read_b64_tr_b16 a[162:163], v20 offset:4096\n" \
"ds_read_b64_tr_b16 a[168:169], v20 offset:8192\n" \
"ds_read_b64_tr_b16 a[170:171], v20 offset:12288\n" \
"ds_read_b64_tr_b16 a[176:177], v20 offset:16384\n" \
"ds_read_b64_tr_b16 a[178:179], v20 offset:20480\n" \
"ds_read_b64_tr_b16 a[184:185], v20 offset:24576\n" \
"ds_read_b64_tr_b16 a[186:187], v20 offset:28672\n" \
"v_add_u32 v20, 0x0, v3\n" \
"ds_read_b64_tr_b16 a[164:165], v20 offset:0\n" \
"ds_read_b64_tr_b16 a[166:167], v20 offset:4096\n" \
"ds_read_b64_tr_b16 a[172:173], v20 offset:8192\n" \
"ds_read_b64_tr_b16 a[174:175], v20 offset:12288\n" \
"ds_read_b64_tr_b16 a[180:181], v20 offset:16384\n" \
"ds_read_b64_tr_b16 a[182:183], v20 offset:20480\n" \
"ds_read_b64_tr_b16 a[188:189], v20 offset:24576\n" \
"ds_read_b64_tr_b16 a[190:191], v20 offset:28672\n" \
"s_waitcnt lgkmcnt(0)\n" \
"s_barrier\n" \
"s_waitcnt vmcnt(8)\n" \
"s_barrier\n" \
"v_add_u32 v20, 0x8000, v2\n" \
"ds_read_b64_tr_b16 a[192:193], v20 offset:0\n" \
"ds_read_b64_tr_b16 a[194:195], v20 offset:4096\n" \
"ds_read_b64_tr_b16 a[200:201], v20 offset:8192\n" \
"ds_read_b64_tr_b16 a[202:203], v20 offset:12288\n" \
"ds_read_b64_tr_b16 a[208:209], v20 offset:16384\n" \
"ds_read_b64_tr_b16 a[210:211], v20 offset:20480\n" \
"ds_read_b64_tr_b16 a[216:217], v20 offset:24576\n" \
"ds_read_b64_tr_b16 a[218:219], v20 offset:28672\n" \
"v_add_u32 v20, 0x8000, v3\n" \
"ds_read_b64_tr_b16 a[196:197], v20 offset:0\n" \
"ds_read_b64_tr_b16 a[198:199], v20 offset:4096\n" \
"ds_read_b64_tr_b16 a[204:205], v20 offset:8192\n" \
"ds_read_b64_tr_b16 a[206:207], v20 offset:12288\n" \
"ds_read_b64_tr_b16 a[212:213], v20 offset:16384\n" \
"ds_read_b64_tr_b16 a[214:215], v20 offset:20480\n" \
"ds_read_b64_tr_b16 a[220:221], v20 offset:24576\n" \
"ds_read_b64_tr_b16 a[222:223], v20 offset:28672\n" \
"s_waitcnt lgkmcnt(0)\n" \
"s_barrier\n" \
"s_waitcnt vmcnt(0)\n" \
"s_barrier\n" \
"v_add_u32 v20, 0x10000, v2\n" \
"ds_read_b64_tr_b16 a[224:225], v20 offset:0\n" \
"ds_read_b64_tr_b16 a[226:227], v20 offset:4096\n" \
"ds_read_b64_tr_b16 a[232:233], v20 offset:8192\n" \
"ds_read_b64_tr_b16 a[234:235], v20 offset:12288\n" \
"ds_read_b64_tr_b16 a[240:241], v20 offset:16384\n" \
"ds_read_b64_tr_b16 a[242:243], v20 offset:20480\n" \
"ds_read_b64_tr_b16 a[248:249], v20 offset:24576\n" \
"ds_read_b64_tr_b16 a[250:251], v20 offset:28672\n" \
"v_add_u32 v20, 0x10000, v3\n" \
"ds_read_b64_tr_b16 a[228:229], v20 offset:0\n" \
"ds_read_b64_tr_b16 a[230:231], v20 offset:4096\n" \
"ds_read_b64_tr_b16 a[236:237], v20 offset:8192\n" \
"ds_read_b64_tr_b16 a[238:239], v20 offset:12288\n" \
"ds_read_b64_tr_b16 a[244:245], v20 offset:16384\n" \
"ds_read_b64_tr_b16 a[246:247], v20 offset:20480\n" \
"ds_read_b64_tr_b16 a[252:253], v20 offset:24576\n" \
"ds_read_b64_tr_b16 a[254:255], v20 offset:28672\n" \
"s_waitcnt lgkmcnt(0)\n" \
"s_barrier\n" \
"s_mov_b32 s54, s34\n" \
"s_mov_b32 s56, 0\n" \
"v_mov_b32 v4, v0\n" \
"v_lshlrev_b32 v11, 10, v0\n" \
"v_mov_b32 v15, 0x22000\n" \
"s_lshl_b32 s48, s33, 2\n" \
"v_add_u32 v14, s48, v15\n" \
"WL_%=:\n" \
"s_add_u32 s52, s35, 1\n" \
"s_bitcmp1_b64 %[dpar], s35\n" \
"s_cselect_b32 s69, 0x2000, 0\n" \
"s_add_u32 s57, s53, 0x200000\n" \
"s_xor_b32 s57, s57, s69\n" \
"s_xor_b32 s58, s54, 0x10000\n" \
"s_cmp_lg_u32 s56, 0\n" \
"s_cbranch_scc1 WB_%=\n" \
"buffer_load_dword v13, v5, s[40:43], 0 offen sc1\n" \
"s_waitcnt vmcnt(0)\n" \
"s_barrier\n" \
"v_mov_b32 v12, s35\n" \
"s_mov_b64 exec, 15\n" \
"buffer_store_dword v12, v11, s[40:43], s51 offen sc1\n" \
"s_mov_b64 exec, -1\n" \
"s_cmp_lg_u32 s33, 0\n" \
"s_cbranch_scc1 WPD_%=\n" \
"s_branch WPC_%=\n" \
"WP_%=:\n" \
"buffer_load_dword v13, v5, s[40:43], 0 offen sc1\n" \
"s_waitcnt vmcnt(0)\n" \
"WPC_%=:\n" \
"v_cmp_gt_u32 vcc, s52, v13\n" \
"s_cbranch_vccz WPD_%=\n" \
"s_sleep 2\n" \
"s_sub_u32 s50, s50, 1\n" \
"s_cbranch_scc0 WP_%=\n" \
"s_mov_b32 s50, 0\n" \
"WPD_%=:\n" \
"s_barrier\n" \
"s_add_u32 m0, s54, 0\n" \
"s_add_u32 s48, s53, 0\n" \
"buffer_load_dwordx4 v0, s[40:43], s48 offen sc1 lds\n" \
"s_add_u32 m0, s54, 4096\n" \
"s_add_u32 s48, s53, 4096\n" \
"buffer_load_dwordx4 v0, s[40:43], s48 offen sc1 lds\n" \
"s_add_u32 m0, s54, 8192\n" \
"s_add_u32 s48, s53, 16384\n" \
"buffer_load_dwordx4 v0, s[40:43], s48 offen sc1 lds\n" \
"s_add_u32 m0, s54, 12288\n" \
"s_add_u32 s48, s53, 20480\n" \
"buffer_load_dwordx4 v0, s[40:43], s48 offen sc1 lds\n" \
"s_add_u32 m0, s54, 16384\n" \
"s_add_u32 s48, s53, 32768\n" \
"buffer_load_dwordx4 v0, s[40:43], s48 offen sc1 lds\n" \
"s_add_u32 m0, s54, 20480\n" \
"s_add_u32 s48, s53, 36864\n" \
"buffer_load_dwordx4 v0, s[40:43], s48 offen sc1 lds\n" \
"s_add_u32 s52, s52, 1\n" \
"s_waitcnt vmcnt(4)\n" \
"s_barrier\n" \
"ds_read_b128 v[32:35], v4 offset:0\n" \
"ds_read_b128 v[36:39], v4 offset:4096\n" \
"ds_read_b128 v[40:43], v4 offset:1024\n" \
"ds_read_b128 v[44:47], v4 offset:5120\n" \
"ds_read_b128 v[48:51], v4 offset:2048\n" \
"ds_read_b128 v[52:55], v4 offset:6144\n" \
"s_waitcnt lgkmcnt(4)\n" \
"v_mfma_f32_16x16x32_bf16 v[16:19], a[0:3], v[32:35], 0\n" \
"ds_read_b128 v[56:59], v4 offset:3072\n" \
"v_mfma_f32_16x16x32_bf16 v[20:23], a[4:7], v[32:35], 0\n" \
"ds_read_b128 v[60:63], v4 offset:7168\n" \
"s_add_u32 m0, s54, 24576\n" \
"s_add_u32 s48, s53, 49152\n" \
"v_mfma_f32_16x16x32_bf16 v[24:27], a[0:3], v[36:39], 0\n" \
"buffer_load_dwordx4 v0, s[40:43], s48 offen sc1 lds\n" \
"v_mfma_f32_16x16x32_bf16 v[28:31], a[4:7], v[36:39], 0\n" \
"s_waitcnt vmcnt(3)\n" \
"s_barrier\n" \
"s_waitcnt lgkmcnt(4)\n" \
"v_mfma_f32_16x16x32_bf16 v[16:19], a[8:11], v[40:43], v[16:19]\n" \
"ds_read_b128 v[32:35], v4 offset:8192\n" \
"v_mfma_f32_16x16x32_bf16 v[20:23], a[12:15], v[40:43], v[20:23]\n" \
"ds_read_b128 v[36:39], v4 offset:12288\n" \
"s_add_u32 m0, s54, 28672\n" \
"s_add_u32 s48, s53, 53248\n" \
"v_mfma_f32_16x16x32_bf16 v[24:27], a[8:11], v[44:47], v[24:27]\n" \
"buffer_load_dwordx4 v0, s[40:43], s48 offen sc1 lds\n" \
"v_mfma_f32_16x16x32_bf16 v[28:31], a[12:15], v[44:47], v[28:31]\n" \
"s_waitcnt lgkmcnt(4)\n" \
"v_mfma_f32_16x16x32_bf16 v[16:19], a[16:19], v[48:51], v[16:19]\n" \
"ds_read_b128 v[40:43], v4 offset:9216\n" \
"v_mfma_f32_16x16x32_bf16 v[20:23], a[20:23], v[48:51], v[20:23]\n" \
"ds_read_b128 v[44:47], v4 offset:13312\n" \
"s_add_u32 m0, s54, 32768\n" \
"s_add_u32 s48, s53, 65536\n" \
"v_mfma_f32_16x16x32_bf16 v[24:27], a[16:19], v[52:55], v[24:27]\n" \
"buffer_load_dwordx4 v0, s[40:43], s48 offen sc1 lds\n" \
"v_mfma_f32_16x16x32_bf16 v[28:31], a[20:23], v[52:55], v[28:31]\n" \
"s_waitcnt lgkmcnt(4)\n" \
"v_mfma_f32_16x16x32_bf16 v[16:19], a[24:27], v[56:59], v[16:19]\n" \
"ds_read_b128 v[48:51], v4 offset:10240\n" \
"v_mfma_f32_16x16x32_bf16 v[20:23], a[28:31], v[56:59], v[20:23]\n" \
"ds_read_b128 v[52:55], v4 offset:14336\n" \
"s_add_u32 m0, s54, 36864\n" \
"s_add_u32 s48, s53, 69632\n" \
"v_mfma_f32_16x16x32_bf16 v[24:27], a[24:27], v[60:63], v[24:27]\n" \
"buffer_load_dwordx4 v0, s[40:43], s48 offen sc1 lds\n" \
"v_mfma_f32_16x16x32_bf16 v[28:31], a[28:31], v[60:63], v[28:31]\n" \
"s_waitcnt lgkmcnt(4)\n" \
"v_mfma_f32_16x16x32_bf16 v[16:19], a[32:35], v[32:35], v[16:19]\n" \
"ds_read_b128 v[56:59], v4 offset:11264\n" \
"v_mfma_f32_16x16x32_bf16 v[20:23], a[36:39], v[32:35], v[20:23]\n" \
"ds_read_b128 v[60:63], v4 offset:15360\n" \
"s_add_u32 m0, s54, 40960\n" \
"s_add_u32 s48, s53, 81920\n" \
"v_mfma_f32_16x16x32_bf16 v[24:27], a[32:35], v[36:39], v[24:27]\n" \
"buffer_load_dwordx4 v0, s[40:43], s48 offen sc1 lds\n" \
"v_mfma_f32_16x16x32_bf16 v[28:31], a[36:39], v[36:39], v[28:31]\n" \
"s_waitcnt vmcnt(5)\n" \
"s_barrier\n" \
"s_waitcnt lgkmcnt(4)\n" \
"v_mfma_f32_16x16x32_bf16 v[16:19], a[40:43], v[40:43], v[16:19]\n" \
"ds_read_b128 v[32:35], v4 offset:16384\n" \
"v_mfma_f32_16x16x32_bf16 v[20:23], a[44:47], v[40:43], v[20:23]\n" \
"ds_read_b128 v[36:39], v4 offset:20480\n" \
"s_add_u32 m0, s54, 45056\n" \
"s_add_u32 s48, s53, 86016\n" \
"v_mfma_f32_16x16x32_bf16 v[24:27], a[40:43], v[44:47], v[24:27]\n" \
"buffer_load_dwordx4 v0, s[40:43], s48 offen sc1 lds\n" \
"v_mfma_f32_16x16x32_bf16 v[28:31], a[44:47], v[44:47], v[28:31]\n" \
"s_waitcnt lgkmcnt(4)\n" \
"v_mfma_f32_16x16x32_bf16 v[16:19], a[48:51], v[48:51], v[16:19]\n" \
"ds_read_b128 v[40:43], v4 offset:17408\n" \
"v_mfma_f32_16x16x32_bf16 v[20:23], a[52:55], v[48:51], v[20:23]\n" \
"ds_read_b128 v[44:47], v4 offset:21504\n" \
"s_add_u32 m0, s54, 49152\n" \
"s_add_u32 s48, s53, 98304\n" \
"v_mfma_f32_16x16x32_bf16 v[24:27], a[48:51], v[52:55], v[24:27]\n" \
"buffer_load_dwordx4 v0, s[40:43], s48 offen sc1 lds\n" \
"v_mfma_f32_16x16x32_bf16 v[28:31], a[52:55], v[52:55], v[28:31]\n" \
"s_waitcnt lgkmcnt(4)\n" \
"v_mfma_f32_16x16x32_bf16 v[16:19], a[56:59], v[56:59], v[16:19]\n" \
"ds_read_b128 v[48:51], v4 offset:18432\n" \
"v_mfma_f32_16x16x32_bf16 v[20:23], a[60:63], v[56:59], v[20:23]\n" \
"ds_read_b128 v[52:55], v4 offset:22528\n" \
"s_add_u32 m0, s54, 53248\n" \
"s_add_u32 s48, s53, 102400\n" \
"v_mfma_f32_16x16x32_bf16 v[24:27], a[56:59], v[60:63], v[24:27]\n" \
"buffer_load_dwordx4 v0, s[40:43], s48 offen sc1 lds\n" \
"v_mfma_f32_16x16x32_bf16 v[28:31], a[60:63], v[60:63], v[28:31]\n" \
"s_waitcnt lgkmcnt(4)\n" \
"v_mfma_f32_16x16x32_bf16 v[16:19], a[64:67], v[32:35], v[16:19]\n" \
"ds_read_b128 v[56:59], v4 offset:19456\n" \
"v_mfma_f32_16x16x32_bf16 v[20:23], a[68:71], v[32:35], v[20:23]\n" \
"ds_read_b128 v[60:63], v4 offset:23552\n" \
"s_add_u32 m0, s54, 57344\n" \
"s_add_u32 s48, s53, 114688\n" \
"v_mfma_f32_16x16x32_bf16 v[24:27], a[64:67], v[36:39], v[24:27]\n" \
"buffer_load_dwordx4 v0, s[40:43], s48 offen sc1 lds\n" \
"v_mfma_f32_16x16x32_bf16 v[28:31], a[68:71], v[36:39], v[28:31]\n" \
"s_waitcnt vmcnt(7)\n" \
"s_barrier\n" \
"s_waitcnt lgkmcnt(4)\n" \
"v_mfma_f32_16x16x32_bf16 v[16:19], a[72:75], v[40:43], v[16:19]\n" \
"ds_read_b128 v[32:35], v4 offset:24576\n" \
"v_mfma_f32_16x16x32_bf16 v[20:23], a[76:79], v[40:43], v[20:23]\n" \
"ds_read_b128 v[36:39], v4 offset:28672\n" \
"s_add_u32 m0, s54, 61440\n" \
"s_add_u32 s48, s53, 118784\n" \
"v_mfma_f32_16x16x32_bf16 v[24:27], a[72:75], v[44:47], v[24:27]\n" \
"buffer_load_dwordx4 v0, s[40:43], s48 offen sc1 lds\n" \
"v_mfma_f32_16x16x32_bf16 v[28:31], a[76:79], v[44:47], v[28:31]\n" \
"s_waitcnt lgkmcnt(4)\n" \
"v_mfma_f32_16x16x32_bf16 v[16:19], a[80:83], v[48:51], v[16:19]\n" \
"ds_read_b128 v[40:43], v4 offset:25600\n" \
"v_mfma_f32_16x16x32_bf16 v[20:23], a[84:87], v[48:51], v[20:23]\n" \
"ds_read_b128 v[44:47], v4 offset:29696\n" \
"v_mfma_f32_16x16x32_bf16 v[24:27], a[80:83], v[52:55], v[24:27]\n" \
"v_mfma_f32_16x16x32_bf16 v[28:31], a[84:87], v[52:55], v[28:31]\n" \
"s_waitcnt lgkmcnt(4)\n" \
"v_mfma_f32_16x16x32_bf16 v[16:19], a[88:91], v[56:59], v[16:19]\n" \
"ds_read_b128 v[48:51], v4 offset:26624\n" \
"v_mfma_f32_16x16x32_bf16 v[20:23], a[92:95], v[56:59], v[20:23]\n" \
"ds_read_b128 v[52:55], v4 offset:30720\n" \
"v_mfma_f32_16x16x32_bf16 v[24:27], a[88:91], v[60:63], v[24:27]\n" \
"v_mfma_f32_16x16x32_bf16 v[28:31], a[92:95], v[60:63], v[28:31]\n" \
"s_waitcnt lgkmcnt(4)\n" \
"v_mfma_f32_16x16x32_bf16 v[16:19], a[96:99], v[32:35], v[16:19]\n" \
"ds_read_b128 v[56:59], v4 offset:27648\n" \
"v_mfma_f32_16x16x32_bf16 v[20:23], a[100:103], v[32:35], v[20:23]\n" \
"ds_read_b128 v[60:63], v4 offset:31744\n" \
"v_mfma_f32_16x16x32_bf16 v[24:27], a[96:99], v[36:39], v[24:27]\n" \
"v_mfma_f32_16x16x32_bf16 v[28:31], a[100:103], v[36:39], v[28:31]\n" \
"s_waitcnt vmcnt(6)\n" \
"s_barrier\n" \
"s_waitcnt lgkmcnt(4)\n" \
"v_mfma_f32_16x16x32_bf16 v[16:19], a[104:107], v[40:43], v[16:19]\n" \
"ds_read_b128 v[32:35], v4 offset:32768\n" \
"v_mfma_f32_16x16x32_bf16 v[20:23], a[108:111], v[40:43], v[20:23]\n" \
"ds_read_b128 v[36:39], v4 offset:36864\n" \
"v_mfma_f32_16x16x32_bf16 v[24:27], a[104:107], v[44:47], v[24:27]\n" \
"v_mfma_f32_16x16x32_bf16 v[28:31], a[108:111], v[44:47], v[28:31]\n" \
"s_waitcnt lgkmcnt(4)\n" \
"v_mfma_f32_16x16x32_bf16 v[16:19], a[112:115], v[48:51], v[16:19]\n" \
"ds_read_b128 v[40:43], v4 offset:33792\n" \
"v_mfma_f32_16x16x32_bf16 v[20:23], a[116:119], v[48:51], v[20:23]\n" \
"ds_read_b128 v[44:47], v4 offset:37888\n" \
"v_mfma_f32_16x16x32_bf16 v[24:27], a[112:115], v[52:55], v[24:27]\n" \
"v_mfma_f32_16x16x32_bf16 v[28:31], a[116:119], v[52:55], v[28:31]\n" \
"s_waitcnt lgkmcnt(4)\n" \
"v_mfma_f32_16x16x32_bf16 v[16:19], a[120:123], v[56:59], v[16:19]\n" \
"ds_read_b128 v[48:51], v4 offset:34816\n" \
"v_mfma_f32_16x16x32_bf16 v[20:23], a[124:127], v[56:59], v[20:23]\n" \
"ds_read_b128 v[52:55], v4 offset:38912\n" \
"v_mfma_f32_16x16x32_bf16 v[24:27], a[120:123], v[60:63], v[24:27]\n" \
"buffer_load_dword v13, v5, s[40:43], 0 offen sc1\n" \
"v_mfma_f32_16x16x32_bf16 v[28:31], a[124:127], v[60:63], v[28:31]\n" \
"s_waitcnt lgkmcnt(4)\n" \
"v_mfma_f32_16x16x32_bf16 v[16:19], a[128:131], v[32:35], v[16:19]\n" \
"ds_read_b128 v[56:59], v4 offset:35840\n" \
"v_mfma_f32_16x16x32_bf16 v[20:23], a[132:135], v[32:35], v[20:23]\n" \
"ds_read_b128 v[60:63], v4 offset:39936\n" \
"v_mfma_f32_16x16x32_bf16 v[24:27], a[128:131], v[36:39], v[24:27]\n" \
"v_mfma_f32_16x16x32_bf16 v[28:31], a[132:135], v[36:39], v[28:31]\n" \
"s_waitcnt vmcnt(1)\n" \
"s_barrier\n" \
"s_waitcnt lgkmcnt(4)\n" \
"v_mfma_f32_16x16x32_bf16 v[16:19], a[136:139], v[40:43], v[16:19]\n" \
"ds_read_b128 v[32:35], v4 offset:40960\n" \
"v_mfma_f32_16x16x32_bf16 v[20:23], a[140:143], v[40:43], v[20:23]\n" \
"ds_read_b128 v[36:39], v4 offset:45056\n" \
"v_mfma_f32_16x16x32_bf16 v[24:27], a[136:139], v[44:47], v[24:27]\n" \
"v_mfma_f32_16x16x32_bf16 v[28:31], a[140:143], v[44:47], v[28:31]\n" \
"s_waitcnt lgkmcnt(4)\n" \
"v_mfma_f32_16x16x32_bf16 v[16:19], a[144:147], v[48:51], v[16:19]\n" \
"ds_read_b128 v[40:43], v4 offset:41984\n" \
"v_mfma_f32_16x16x32_bf16 v[20:23], a[148:151], v[48:51], v[20:23]\n" \
"ds_read_b128 v[44:47], v4 offset:46080\n" \
"v_mfma_f32_16x16x32_bf16 v[24:27], a[144:147], v[52:55], v[24:27]\n" \
"v_mfma_f32_16x16x32_bf16 v[28:31], a[148:151], v[52:55], v[28:31]\n" \
"s_waitcnt lgkmcnt(4)\n" \
"v_mfma_f32_16x16x32_bf16 v[16:19], a[152:155], v[56:59], v[16:19]\n" \
"ds_read_b128 v[48:51], v4 offset:43008\n" \
"v_mfma_f32_16x16x32_bf16 v[20:23], a[156:159], v[56:59], v[20:23]\n" \
"ds_read_b128 v[52:55], v4 offset:47104\n" \
"v_mfma_f32_16x16x32_bf16 v[24:27], a[152:155], v[60:63], v[24:27]\n" \
"v_mfma_f32_16x16x32_bf16 v[28:31], a[156:159], v[60:63], v[28:31]\n" \
"s_waitcnt lgkmcnt(4)\n" \
"v_mfma_f32_16x16x32_bf16 v[16:19], a[160:163], v[32:35], v[16:19]\n" \
"ds_read_b128 v[56:59], v4 offset:44032\n" \
"v_mfma_f32_16x16x32_bf16 v[20:23], a[164:167], v[32:35], v[20:23]\n" \
"ds_read_b128 v[60:63], v4 offset:48128\n" \
"v_mfma_f32_16x16x32_bf16 v[24:27], a[160:163], v[36:39], v[24:27]\n" \
"v_mfma_f32_16x16x32_bf16 v[28:31], a[164:167], v[36:39], v[28:31]\n" \
"s_waitcnt lgkmcnt(4)\n" \
"v_mfma_f32_16x16x32_bf16 v[16:19], a[168:171], v[40:43], v[16:19]\n" \
"ds_read_b128 v[32:35], v4 offset:49152\n" \
"v_mfma_f32_16x16x32_bf16 v[20:23], a[172:175], v[40:43], v[20:23]\n" \
"ds_read_b128 v[36:39], v4 offset:53248\n" \
"v_mfma_f32_16x16x32_bf16 v[24:27], a[168:171], v[44:47], v[24:27]\n" \
"v_mfma_f32_16x16x32_bf16 v[28:31], a[172:175], v[44:47], v[28:31]\n" \
"s_waitcnt vmcnt(0)\n" \
"v_cmp_gt_u32 vcc, s52, v13\n" \
"s_waitcnt lgkmcnt(4)\n" \
"v_mfma_f32_16x16x32_bf16 v[16:19], a[176:179], v[48:51], v[16:19]\n" \
"ds_read_b128 v[40:43], v4 offset:50176\n" \
"v_mfma_f32_16x16x32_bf16 v[20:23], a[180:183], v[48:51], v[20:23]\n" \
"ds_read_b128 v[44:47], v4 offset:54272\n" \
"v_mfma_f32_16x16x32_bf16 v[24:27], a[176:179], v[52:55], v[24:27]\n" \
"s_cmp_eq_u64 vcc, 0\n" \
"s_cselect_b32 s56, 1, 0\n" \
"v_mfma_f32_16x16x32_bf16 v[28:31], a[180:183], v[52:55], v[28:31]\n" \
"v_mov_b32 v13, s56\n" \
"ds_write_b32 v14, v13\n" \
"s_waitcnt lgkmcnt(5)\n" \
"v_mfma_f32_16x16x32_bf16 v[16:19], a[184:187], v[56:59], v[16:19]\n" \
"ds_read_b128 v[48:51], v4 offset:51200\n" \
"v_mfma_f32_16x16x32_bf16 v[20:23], a[188:191], v[56:59], v[20:23]\n" \
"ds_read_b128 v[52:55], v4 offset:55296\n" \
"v_mfma_f32_16x16x32_bf16 v[24:27], a[184:187], v[60:63], v[24:27]\n" \
"v_mfma_f32_16x16x32_bf16 v[28:31], a[188:191], v[60:63], v[28:31]\n" \
"s_waitcnt lgkmcnt(2)\n" \
"s_barrier\n" \
"v_mfma_f32_16x16x32_bf16 v[16:19], a[192:195], v[32:35], v[16:19]\n" \
"ds_read_b128 v[56:59], v4 offset:52224\n" \
"v_mfma_f32_16x16x32_bf16 v[20:23], a[196:199], v[32:35], v[20:23]\n" \
"ds_read_b128 v[60:63], v4 offset:56320\n" \
"v_mfma_f32_16x16x32_bf16 v[24:27], a[192:195], v[36:39], v[24:27]\n" \
"ds_read_b32 v13, v15\n" \
"v_mfma_f32_16x16x32_bf16 v[28:31], a[196:199], v[36:39], v[28:31]\n" \
"v_mfma_f32_16x16x32_bf16 v[16:19], a[200:203], v[40:43], v[16:19]\n" \
"ds_read_b128 v[32:35], v4 offset:57344\n" \
"v_mfma_f32_16x16x32_bf16 v[20:23], a[204:207], v[40:43], v[20:23]\n" \
"ds_read_b128 v[36:39], v4 offset:61440\n" \
"v_mfma_f32_16x16x32_bf16 v[24:27], a[200:203], v[44:47], v[24:27]\n" \
"s_waitcnt lgkmcnt(2)\n" \
"v_readfirstlane_b32 s56, v13\n" \
"v_mfma_f32_16x16x32_bf16 v[28:31], a[204:207], v[44:47], v[28:31]\n" \
"s_branch WM_%=\n" \
"WB_%=:\n" \
"s_add_u32 s52, s52, 1\n" \
"s_waitcnt vmcnt(6)\n" \
"s_barrier\n" \
"ds_read_b128 v[32:35], v4 offset:0\n" \
"ds_read_b128 v[36:39], v4 offset:4096\n" \
"ds_read_b128 v[40:43], v4 offset:1024\n" \
"ds_read_b128 v[44:47], v4 offset:5120\n" \
"ds_read_b128 v[48:51], v4 offset:2048\n" \
"ds_read_b128 v[52:55], v4 offset:6144\n" \
"s_waitcnt lgkmcnt(4)\n" \
"v_mfma_f32_16x16x32_bf16 v[16:19], a[0:3], v[32:35], 0\n" \
"ds_read_b128 v[56:59], v4 offset:3072\n" \
"v_mfma_f32_16x16x32_bf16 v[20:23], a[4:7], v[32:35], 0\n" \
"ds_read_b128 v[60:63], v4 offset:7168\n" \
"s_add_u32 m0, s54, 24576\n" \
"s_add_u32 s48, s53, 49152\n" \
"v_mfma_f32_16x16x32_bf16 v[24:27], a[0:3], v[36:39], 0\n" \
"buffer_load_dwordx4 v0, s[40:43], s48 offen sc1 lds\n" \
"v_mfma_f32_16x16x32_bf16 v[28:31], a[4:7], v[36:39], 0\n" \
"s_waitcnt vmcnt(5)\n" \
"s_barrier\n" \
"s_waitcnt lgkmcnt(4)\n" \
"v_mfma_f32_16x16x32_bf16 v[16:19], a[8:11], v[40:43], v[16:19]\n" \
"ds_read_b128 v[32:35], v4 offset:8192\n" \
"v_mfma_f32_16x16x32_bf16 v[20:23], a[12:15], v[40:43], v[20:23]\n" \
"ds_read_b128 v[36:39], v4 offset:12288\n" \
"s_add_u32 m0, s54, 28672\n" \
"s_add_u32 s48, s53, 53248\n" \
"v_mfma_f32_16x16x32_bf16 v[24:27], a[8:11], v[44:47], v[24:27]\n" \
"buffer_load_dwordx4 v0, s[40:43], s48 offen sc1 lds\n" \
"v_mfma_f32_16x16x32_bf16 v[28:31], a[12:15], v[44:47], v[28:31]\n" \
"s_waitcnt lgkmcnt(4)\n" \
"v_mfma_f32_16x16x32_bf16 v[16:19], a[16:19], v[48:51], v[16:19]\n" \
"ds_read_b128 v[40:43], v4 offset:9216\n" \
"v_mfma_f32_16x16x32_bf16 v[20:23], a[20:23], v[48:51], v[20:23]\n" \
"ds_read_b128 v[44:47], v4 offset:13312\n" \
"s_add_u32 m0, s54, 32768\n" \
"s_add_u32 s48, s53, 65536\n" \
"v_mfma_f32_16x16x32_bf16 v[24:27], a[16:19], v[52:55], v[24:27]\n" \
"buffer_load_dwordx4 v0, s[40:43], s48 offen sc1 lds\n" \
"v_mfma_f32_16x16x32_bf16 v[28:31], a[20:23], v[52:55], v[28:31]\n" \
"s_waitcnt lgkmcnt(4)\n" \
"v_mfma_f32_16x16x32_bf16 v[16:19], a[24:27], v[56:59], v[16:19]\n" \
"ds_read_b128 v[48:51], v4 offset:10240\n" \
"v_mfma_f32_16x16x32_bf16 v[20:23], a[28:31], v[56:59], v[20:23]\n" \
"ds_read_b128 v[52:55], v4 offset:14336\n" \
"s_add_u32 m0, s54, 36864\n" \
"s_add_u32 s48, s53, 69632\n" \
"v_mfma_f32_16x16x32_bf16 v[24:27], a[24:27], v[60:63], v[24:27]\n" \
"buffer_load_dwordx4 v0, s[40:43], s48 offen sc1 lds\n" \
"v_mfma_f32_16x16x32_bf16 v[28:31], a[28:31], v[60:63], v[28:31]\n" \
"s_waitcnt lgkmcnt(4)\n" \
"v_mfma_f32_16x16x32_bf16 v[16:19], a[32:35], v[32:35], v[16:19]\n" \
"ds_read_b128 v[56:59], v4 offset:11264\n" \
"v_mfma_f32_16x16x32_bf16 v[20:23], a[36:39], v[32:35], v[20:23]\n" \
"ds_read_b128 v[60:63], v4 offset:15360\n" \
"s_add_u32 m0, s54, 40960\n" \
"s_add_u32 s48, s53, 81920\n" \
"v_mfma_f32_16x16x32_bf16 v[24:27], a[32:35], v[36:39], v[24:27]\n" \
"buffer_load_dwordx4 v0, s[40:43], s48 offen sc1 lds\n" \
"v_mfma_f32_16x16x32_bf16 v[28:31], a[36:39], v[36:39], v[28:31]\n" \
"s_waitcnt vmcnt(7)\n" \
"s_barrier\n" \
"s_waitcnt lgkmcnt(4)\n" \
"v_mfma_f32_16x16x32_bf16 v[16:19], a[40:43], v[40:43], v[16:19]\n" \
"ds_read_b128 v[32:35], v4 offset:16384\n" \
"v_mfma_f32_16x16x32_bf16 v[20:23], a[44:47], v[40:43], v[20:23]\n" \
"ds_read_b128 v[36:39], v4 offset:20480\n" \
"s_add_u32 m0, s54, 45056\n" \
"s_add_u32 s48, s53, 86016\n" \
"v_mfma_f32_16x16x32_bf16 v[24:27], a[40:43], v[44:47], v[24:27]\n" \
"buffer_load_dwordx4 v0, s[40:43], s48 offen sc1 lds\n" \
"v_mfma_f32_16x16x32_bf16 v[28:31], a[44:47], v[44:47], v[28:31]\n" \
"s_waitcnt lgkmcnt(4)\n" \
"v_mfma_f32_16x16x32_bf16 v[16:19], a[48:51], v[48:51], v[16:19]\n" \
"ds_read_b128 v[40:43], v4 offset:17408\n" \
"v_mfma_f32_16x16x32_bf16 v[20:23], a[52:55], v[48:51], v[20:23]\n" \
"ds_read_b128 v[44:47], v4 offset:21504\n" \
"s_add_u32 m0, s54, 49152\n" \
"s_add_u32 s48, s53, 98304\n" \
"v_mfma_f32_16x16x32_bf16 v[24:27], a[48:51], v[52:55], v[24:27]\n" \
"buffer_load_dwordx4 v0, s[40:43], s48 offen sc1 lds\n" \
"v_mfma_f32_16x16x32_bf16 v[28:31], a[52:55], v[52:55], v[28:31]\n" \
"s_waitcnt lgkmcnt(4)\n" \
"v_mfma_f32_16x16x32_bf16 v[16:19], a[56:59], v[56:59], v[16:19]\n" \
"ds_read_b128 v[48:51], v4 offset:18432\n" \
"v_mfma_f32_16x16x32_bf16 v[20:23], a[60:63], v[56:59], v[20:23]\n" \
"ds_read_b128 v[52:55], v4 offset:22528\n" \
"s_add_u32 m0, s54, 53248\n" \
"s_add_u32 s48, s53, 102400\n" \
"v_mfma_f32_16x16x32_bf16 v[24:27], a[56:59], v[60:63], v[24:27]\n" \
"buffer_load_dwordx4 v0, s[40:43], s48 offen sc1 lds\n" \
"v_mfma_f32_16x16x32_bf16 v[28:31], a[60:63], v[60:63], v[28:31]\n" \
"s_waitcnt lgkmcnt(4)\n" \
"v_mfma_f32_16x16x32_bf16 v[16:19], a[64:67], v[32:35], v[16:19]\n" \
"ds_read_b128 v[56:59], v4 offset:19456\n" \
"v_mfma_f32_16x16x32_bf16 v[20:23], a[68:71], v[32:35], v[20:23]\n" \
"ds_read_b128 v[60:63], v4 offset:23552\n" \
"s_add_u32 m0, s54, 57344\n" \
"s_add_u32 s48, s53, 114688\n" \
"v_mfma_f32_16x16x32_bf16 v[24:27], a[64:67], v[36:39], v[24:27]\n" \
"buffer_load_dwordx4 v0, s[40:43], s48 offen sc1 lds\n" \
"v_mfma_f32_16x16x32_bf16 v[28:31], a[68:71], v[36:39], v[28:31]\n" \
"s_waitcnt vmcnt(7)\n" \
"s_barrier\n" \
"s_waitcnt lgkmcnt(4)\n" \
"v_mfma_f32_16x16x32_bf16 v[16:19], a[72:75], v[40:43], v[16:19]\n" \
"ds_read_b128 v[32:35], v4 offset:24576\n" \
"v_mfma_f32_16x16x32_bf16 v[20:23], a[76:79], v[40:43], v[20:23]\n" \
"ds_read_b128 v[36:39], v4 offset:28672\n" \
"s_add_u32 m0, s54, 61440\n" \
"s_add_u32 s48, s53, 118784\n" \
"v_mfma_f32_16x16x32_bf16 v[24:27], a[72:75], v[44:47], v[24:27]\n" \
"buffer_load_dwordx4 v0, s[40:43], s48 offen sc1 lds\n" \
"v_mfma_f32_16x16x32_bf16 v[28:31], a[76:79], v[44:47], v[28:31]\n" \
"v_mov_b32 v12, s35\n" \
"s_mov_b64 exec, 15\n" \
"buffer_store_dword v12, v11, s[40:43], s51 offen sc1\n" \
"s_mov_b64 exec, -1\n" \
"s_waitcnt lgkmcnt(4)\n" \
"v_mfma_f32_16x16x32_bf16 v[16:19], a[80:83], v[48:51], v[16:19]\n" \
"ds_read_b128 v[40:43], v4 offset:25600\n" \
"v_mfma_f32_16x16x32_bf16 v[20:23], a[84:87], v[48:51], v[20:23]\n" \
"ds_read_b128 v[44:47], v4 offset:29696\n" \
"v_mfma_f32_16x16x32_bf16 v[24:27], a[80:83], v[52:55], v[24:27]\n" \
"v_mfma_f32_16x16x32_bf16 v[28:31], a[84:87], v[52:55], v[28:31]\n" \
"s_waitcnt lgkmcnt(4)\n" \
"v_mfma_f32_16x16x32_bf16 v[16:19], a[88:91], v[56:59], v[16:19]\n" \
"ds_read_b128 v[48:51], v4 offset:26624\n" \
"v_mfma_f32_16x16x32_bf16 v[20:23], a[92:95], v[56:59], v[20:23]\n" \
"ds_read_b128 v[52:55], v4 offset:30720\n" \
"v_mfma_f32_16x16x32_bf16 v[24:27], a[88:91], v[60:63], v[24:27]\n" \
"v_mfma_f32_16x16x32_bf16 v[28:31], a[92:95], v[60:63], v[28:31]\n" \
"s_waitcnt lgkmcnt(4)\n" \
"v_mfma_f32_16x16x32_bf16 v[16:19], a[96:99], v[32:35], v[16:19]\n" \
"ds_read_b128 v[56:59], v4 offset:27648\n" \
"v_mfma_f32_16x16x32_bf16 v[20:23], a[100:103], v[32:35], v[20:23]\n" \
"ds_read_b128 v[60:63], v4 offset:31744\n" \
"v_mfma_f32_16x16x32_bf16 v[24:27], a[96:99], v[36:39], v[24:27]\n" \
"v_mfma_f32_16x16x32_bf16 v[28:31], a[100:103], v[36:39], v[28:31]\n" \
"s_waitcnt vmcnt(7)\n" \
"s_barrier\n" \
"s_waitcnt lgkmcnt(4)\n" \
"v_mfma_f32_16x16x32_bf16 v[16:19], a[104:107], v[40:43], v[16:19]\n" \
"ds_read_b128 v[32:35], v4 offset:32768\n" \
"v_mfma_f32_16x16x32_bf16 v[20:23], a[108:111], v[40:43], v[20:23]\n" \
"ds_read_b128 v[36:39], v4 offset:36864\n" \
"v_mfma_f32_16x16x32_bf16 v[24:27], a[104:107], v[44:47], v[24:27]\n" \
"v_mfma_f32_16x16x32_bf16 v[28:31], a[108:111], v[44:47], v[28:31]\n" \
"s_waitcnt lgkmcnt(4)\n" \
"v_mfma_f32_16x16x32_bf16 v[16:19], a[112:115], v[48:51], v[16:19]\n" \
"ds_read_b128 v[40:43], v4 offset:33792\n" \
"v_mfma_f32_16x16x32_bf16 v[20:23], a[116:119], v[48:51], v[20:23]\n" \
"ds_read_b128 v[44:47], v4 offset:37888\n" \
"v_mfma_f32_16x16x32_bf16 v[24:27], a[112:115], v[52:55], v[24:27]\n" \
"v_mfma_f32_16x16x32_bf16 v[28:31], a[116:119], v[52:55], v[28:31]\n" \
"s_waitcnt lgkmcnt(4)\n" \
"v_mfma_f32_16x16x32_bf16 v[16:19], a[120:123], v[56:59], v[16:19]\n" \
"ds_read_b128 v[48:51], v4 offset:34816\n" \
"v_mfma_f32_16x16x32_bf16 v[20:23], a[124:127], v[56:59], v[20:23]\n" \
"ds_read_b128 v[52:55], v4 offset:38912\n" \
"v_mfma_f32_16x16x32_bf16 v[24:27], a[120:123], v[60:63], v[24:27]\n" \
"buffer_load_dword v13, v5, s[40:43], 0 offen sc1\n" \
"v_mfma_f32_16x16x32_bf16 v[28:31], a[124:127], v[60:63], v[28:31]\n" \
"s_waitcnt lgkmcnt(4)\n" \
"v_mfma_f32_16x16x32_bf16 v[16:19], a[128:131], v[32:35], v[16:19]\n" \
"ds_read_b128 v[56:59], v4 offset:35840\n" \
"v_mfma_f32_16x16x32_bf16 v[20:23], a[132:135], v[32:35], v[20:23]\n" \
"ds_read_b128 v[60:63], v4 offset:39936\n" \
"v_mfma_f32_16x16x32_bf16 v[24:27], a[128:131], v[36:39], v[24:27]\n" \
"v_mfma_f32_16x16x32_bf16 v[28:31], a[132:135], v[36:39], v[28:31]\n" \
"s_waitcnt vmcnt(2)\n" \
"s_barrier\n" \
"s_waitcnt lgkmcnt(4)\n" \
"v_mfma_f32_16x16x32_bf16 v[16:19], a[136:139], v[40:43], v[16:19]\n" \
"ds_read_b128 v[32:35], v4 offset:40960\n" \
"v_mfma_f32_16x16x32_bf16 v[20:23], a[140:143], v[40:43], v[20:23]\n" \
"ds_read_b128 v[36:39], v4 offset:45056\n" \
"v_mfma_f32_16x16x32_bf16 v[24:27], a[136:139], v[44:47], v[24:27]\n" \
"v_mfma_f32_16x16x32_bf16 v[28:31], a[140:143], v[44:47], v[28:31]\n" \
"s_waitcnt lgkmcnt(4)\n" \
"v_mfma_f32_16x16x32_bf16 v[16:19], a[144:147], v[48:51], v[16:19]\n" \
"ds_read_b128 v[40:43], v4 offset:41984\n" \
"v_mfma_f32_16x16x32_bf16 v[20:23], a[148:151], v[48:51], v[20:23]\n" \
"ds_read_b128 v[44:47], v4 offset:46080\n" \
"v_mfma_f32_16x16x32_bf16 v[24:27], a[144:147], v[52:55], v[24:27]\n" \
"v_mfma_f32_16x16x32_bf16 v[28:31], a[148:151], v[52:55], v[28:31]\n" \
"s_waitcnt lgkmcnt(4)\n" \
"v_mfma_f32_16x16x32_bf16 v[16:19], a[152:155], v[56:59], v[16:19]\n" \
"ds_read_b128 v[48:51], v4 offset:43008\n" \
"v_mfma_f32_16x16x32_bf16 v[20:23], a[156:159], v[56:59], v[20:23]\n" \
"ds_read_b128 v[52:55], v4 offset:47104\n" \
"v_mfma_f32_16x16x32_bf16 v[24:27], a[152:155], v[60:63], v[24:27]\n" \
"v_mfma_f32_16x16x32_bf16 v[28:31], a[156:159], v[60:63], v[28:31]\n" \
"s_waitcnt lgkmcnt(4)\n" \
"v_mfma_f32_16x16x32_bf16 v[16:19], a[160:163], v[32:35], v[16:19]\n" \
"ds_read_b128 v[56:59], v4 offset:44032\n" \
"v_mfma_f32_16x16x32_bf16 v[20:23], a[164:167], v[32:35], v[20:23]\n" \
"ds_read_b128 v[60:63], v4 offset:48128\n" \
"v_mfma_f32_16x16x32_bf16 v[24:27], a[160:163], v[36:39], v[24:27]\n" \
"v_mfma_f32_16x16x32_bf16 v[28:31], a[164:167], v[36:39], v[28:31]\n" \
"s_waitcnt lgkmcnt(4)\n" \
"v_mfma_f32_16x16x32_bf16 v[16:19], a[168:171], v[40:43], v[16:19]\n" \
"ds_read_b128 v[32:35], v4 offset:49152\n" \
"v_mfma_f32_16x16x32_bf16 v[20:23], a[172:175], v[40:43], v[20:23]\n" \
"ds_read_b128 v[36:39], v4 offset:53248\n" \
"v_mfma_f32_16x16x32_bf16 v[24:27], a[168:171], v[44:47], v[24:27]\n" \
"v_mfma_f32_16x16x32_bf16 v[28:31], a[172:175], v[44:47], v[28:31]\n" \
"s_waitcnt vmcnt(0)\n" \
"v_cmp_gt_u32 vcc, s52, v13\n" \
"s_waitcnt lgkmcnt(4)\n" \
"v_mfma_f32_16x16x32_bf16 v[16:19], a[176:179], v[48:51], v[16:19]\n" \
"ds_read_b128 v[40:43], v4 offset:50176\n" \
"v_mfma_f32_16x16x32_bf16 v[20:23], a[180:183], v[48:51], v[20:23]\n" \
"ds_read_b128 v[44:47], v4 offset:54272\n" \
"v_mfma_f32_16x16x32_bf16 v[24:27], a[176:179], v[52:55], v[24:27]\n" \
"s_cmp_eq_u64 vcc, 0\n" \
"s_cselect_b32 s56, 1, 0\n" \
"v_mfma_f32_16x16x32_bf16 v[28:31], a[180:183], v[52:55], v[28:31]\n" \
"v_mov_b32 v13, s56\n" \
"ds_write_b32 v14, v13\n" \
"s_waitcnt lgkmcnt(5)\n" \
"v_mfma_f32_16x16x32_bf16 v[16:19], a[184:187], v[56:59], v[16:19]\n" \
"ds_read_b128 v[48:51], v4 offset:51200\n" \
"v_mfma_f32_16x16x32_bf16 v[20:23], a[188:191], v[56:59], v[20:23]\n" \
"ds_read_b128 v[52:55], v4 offset:55296\n" \
"v_mfma_f32_16x16x32_bf16 v[24:27], a[184:187], v[60:63], v[24:27]\n" \
"v_mfma_f32_16x16x32_bf16 v[28:31], a[188:191], v[60:63], v[28:31]\n" \
"s_waitcnt lgkmcnt(2)\n" \
"s_barrier\n" \
"v_mfma_f32_16x16x32_bf16 v[16:19], a[192:195], v[32:35], v[16:19]\n" \
"ds_read_b128 v[56:59], v4 offset:52224\n" \
"v_mfma_f32_16x16x32_bf16 v[20:23], a[196:199], v[32:35], v[20:23]\n" \
"ds_read_b128 v[60:63], v4 offset:56320\n" \
"v_mfma_f32_16x16x32_bf16 v[24:27], a[192:195], v[36:39], v[24:27]\n" \
"ds_read_b32 v13, v15\n" \
"v_mfma_f32_16x16x32_bf16 v[28:31], a[196:199], v[36:39], v[28:31]\n" \
"v_mfma_f32_16x16x32_bf16 v[16:19], a[200:203], v[40:43], v[16:19]\n" \
"ds_read_b128 v[32:35], v4 offset:57344\n" \
"v_mfma_f32_16x16x32_bf16 v[20:23], a[204:207], v[40:43], v[20:23]\n" \
"ds_read_b128 v[36:39], v4 offset:61440\n" \
"v_mfma_f32_16x16x32_bf16 v[24:27], a[200:203], v[44:47], v[24:27]\n" \
"s_waitcnt lgkmcnt(2)\n" \
"v_readfirstlane_b32 s56, v13\n" \
"v_mfma_f32_16x16x32_bf16 v[28:31], a[204:207], v[44:47], v[28:31]\n" \
"WM_%=:\n" \
"s_cmp_lg_u32 s56, 0\n" \
"s_cbranch_scc1 WN_%=\n" \
"v_mfma_f32_16x16x32_bf16 v[16:19], a[208:211], v[48:51], v[16:19]\n" \
"ds_read_b128 v[40:43], v4 offset:58368\n" \
"v_mfma_f32_16x16x32_bf16 v[20:23], a[212:215], v[48:51], v[20:23]\n" \
"ds_read_b128 v[44:47], v4 offset:62464\n" \
"v_mfma_f32_16x16x32_bf16 v[24:27], a[208:211], v[52:55], v[24:27]\n" \
"v_mfma_f32_16x16x32_bf16 v[28:31], a[212:215], v[52:55], v[28:31]\n" \
"v_mfma_f32_16x16x32_bf16 v[16:19], a[216:219], v[56:59], v[16:19]\n" \
"ds_read_b128 v[48:51], v4 offset:59392\n" \
"v_mfma_f32_16x16x32_bf16 v[20:23], a[220:223], v[56:59], v[20:23]\n" \
"ds_read_b128 v[52:55], v4 offset:63488\n" \
"v_mfma_f32_16x16x32_bf16 v[24:27], a[216:219], v[60:63], v[24:27]\n" \
"v_mfma_f32_16x16x32_bf16 v[28:31], a[220:223], v[60:63], v[28:31]\n" \
"s_waitcnt lgkmcnt(4)\n" \
"v_mfma_f32_16x16x32_bf16 v[16:19], a[224:227], v[32:35], v[16:19]\n" \
"ds_read_b128 v[56:59], v4 offset:60416\n" \
"v_mfma_f32_16x16x32_bf16 v[20:23], a[228:231], v[32:35], v[20:23]\n" \
"ds_read_b128 v[60:63], v4 offset:64512\n" \
"v_mfma_f32_16x16x32_bf16 v[24:27], a[224:227], v[36:39], v[24:27]\n" \
"v_mfma_f32_16x16x32_bf16 v[28:31], a[228:231], v[36:39], v[28:31]\n" \
"s_waitcnt lgkmcnt(4)\n" \
"v_mfma_f32_16x16x32_bf16 v[16:19], a[232:235], v[40:43], v[16:19]\n" \
"v_mfma_f32_16x16x32_bf16 v[20:23], a[236:239], v[40:43], v[20:23]\n" \
"v_mfma_f32_16x16x32_bf16 v[24:27], a[232:235], v[44:47], v[24:27]\n" \
"v_mfma_f32_16x16x32_bf16 v[28:31], a[236:239], v[44:47], v[28:31]\n" \
"s_waitcnt lgkmcnt(2)\n" \
"v_mfma_f32_16x16x32_bf16 v[16:19], a[240:243], v[48:51], v[16:19]\n" \
"v_mfma_f32_16x16x32_bf16 v[20:23], a[244:247], v[48:51], v[20:23]\n" \
"v_mfma_f32_16x16x32_bf16 v[24:27], a[240:243], v[52:55], v[24:27]\n" \
"v_mfma_f32_16x16x32_bf16 v[28:31], a[244:247], v[52:55], v[28:31]\n" \
"s_waitcnt lgkmcnt(0)\n" \
"v_mfma_f32_16x16x32_bf16 v[16:19], a[248:251], v[56:59], v[16:19]\n" \
"v_xor_b32 v4, 0x10000, v4\n" \
"v_mfma_f32_16x16x32_bf16 v[20:23], a[252:255], v[56:59], v[20:23]\n" \
"v_mfma_f32_16x16x32_bf16 v[24:27], a[248:251], v[60:63], v[24:27]\n" \
"v_mfma_f32_16x16x32_bf16 v[28:31], a[252:255], v[60:63], v[28:31]\n" \
"s_branch WT_%=\n" \
"WN_%=:\n" \
"v_mfma_f32_16x16x32_bf16 v[16:19], a[208:211], v[48:51], v[16:19]\n" \
"ds_read_b128 v[40:43], v4 offset:58368\n" \
"v_mfma_f32_16x16x32_bf16 v[20:23], a[212:215], v[48:51], v[20:23]\n" \
"ds_read_b128 v[44:47], v4 offset:62464\n" \
"s_add_u32 m0, s58, 0\n" \
"s_add_u32 s48, s57, 0\n" \
"v_mfma_f32_16x16x32_bf16 v[24:27], a[208:211], v[52:55], v[24:27]\n" \
"buffer_load_dwordx4 v0, s[40:43], s48 offen sc1 lds\n" \
"v_mfma_f32_16x16x32_bf16 v[28:31], a[212:215], v[52:55], v[28:31]\n" \
"v_mfma_f32_16x16x32_bf16 v[16:19], a[216:219], v[56:59], v[16:19]\n" \
"ds_read_b128 v[48:51], v4 offset:59392\n" \
"v_mfma_f32_16x16x32_bf16 v[20:23], a[220:223], v[56:59], v[20:23]\n" \
"ds_read_b128 v[52:55], v4 offset:63488\n" \
"s_add_u32 m0, s58, 4096\n" \
"s_add_u32 s48, s57, 4096\n" \
"v_mfma_f32_16x16x32_bf16 v[24:27], a[216:219], v[60:63], v[24:27]\n" \
"buffer_load_dwordx4 v0, s[40:43], s48 offen sc1 lds\n" \
"v_mfma_f32_16x16x32_bf16 v[28:31], a[220:223], v[60:63], v[28:31]\n" \
"s_waitcnt lgkmcnt(4)\n" \
"v_mfma_f32_16x16x32_bf16 v[16:19], a[224:227], v[32:35], v[16:19]\n" \
"ds_read_b128 v[56:59], v4 offset:60416\n" \
"v_mfma_f32_16x16x32_bf16 v[20:23], a[228:231], v[32:35], v[20:23]\n" \
"ds_read_b128 v[60:63], v4 offset:64512\n" \
"s_add_u32 m0, s58, 8192\n" \
"s_add_u32 s48, s57, 16384\n" \
"v_mfma_f32_16x16x32_bf16 v[24:27], a[224:227], v[36:39], v[24:27]\n" \
"buffer_load_dwordx4 v0, s[40:43], s48 offen sc1 lds\n" \
"v_mfma_f32_16x16x32_bf16 v[28:31], a[228:231], v[36:39], v[28:31]\n" \
"s_waitcnt lgkmcnt(4)\n" \
"v_mfma_f32_16x16x32_bf16 v[16:19], a[232:235], v[40:43], v[16:19]\n" \
"v_mfma_f32_16x16x32_bf16 v[20:23], a[236:239], v[40:43], v[20:23]\n" \
"s_add_u32 m0, s58, 12288\n" \
"s_add_u32 s48, s57, 20480\n" \
"v_mfma_f32_16x16x32_bf16 v[24:27], a[232:235], v[44:47], v[24:27]\n" \
"buffer_load_dwordx4 v0, s[40:43], s48 offen sc1 lds\n" \
"v_mfma_f32_16x16x32_bf16 v[28:31], a[236:239], v[44:47], v[28:31]\n" \
"s_waitcnt lgkmcnt(2)\n" \
"v_mfma_f32_16x16x32_bf16 v[16:19], a[240:243], v[48:51], v[16:19]\n" \
"v_mfma_f32_16x16x32_bf16 v[20:23], a[244:247], v[48:51], v[20:23]\n" \
"s_add_u32 m0, s58, 16384\n" \
"s_add_u32 s48, s57, 32768\n" \
"v_mfma_f32_16x16x32_bf16 v[24:27], a[240:243], v[52:55], v[24:27]\n" \
"buffer_load_dwordx4 v0, s[40:43], s48 offen sc1 lds\n" \
"v_mfma_f32_16x16x32_bf16 v[28:31], a[244:247], v[52:55], v[28:31]\n" \
"s_waitcnt lgkmcnt(0)\n" \
"v_mfma_f32_16x16x32_bf16 v[16:19], a[248:251], v[56:59], v[16:19]\n" \
"v_xor_b32 v4, 0x10000, v4\n" \
"v_mfma_f32_16x16x32_bf16 v[20:23], a[252:255], v[56:59], v[20:23]\n" \
"s_add_u32 m0, s58, 20480\n" \
"s_add_u32 s48, s57, 36864\n" \
"v_mfma_f32_16x16x32_bf16 v[24:27], a[248:251], v[60:63], v[24:27]\n" \
"buffer_load_dwordx4 v0, s[40:43], s48 offen sc1 lds\n" \
"v_mfma_f32_16x16x32_bf16 v[28:31], a[252:255], v[60:63], v[28:31]\n" \
"WT_%=:\n" \
"s_mov_b32 s53, s57\n" \
"s_mov_b32 s54, s58\n" \
"s_nop 5\n" \
"v_cvt_pk_bf16_f32 v64, v16, v17\n" \
"v_cvt_pk_bf16_f32 v65, v18, v19\n" \
"v_cvt_pk_bf16_f32 v66, v20, v21\n" \
"v_cvt_pk_bf16_f32 v67, v22, v23\n" \
"buffer_store_dwordx4 v[64:67], v6, s[40:43], s55 offen offset:0 sc1\n" \
"v_cvt_pk_bf16_f32 v68, v24, v25\n" \
"v_cvt_pk_bf16_f32 v69, v26, v27\n" \
"v_cvt_pk_bf16_f32 v70, v28, v29\n" \
"v_cvt_pk_bf16_f32 v71, v30, v31\n" \
"buffer_store_dwordx4 v[68:71], v6, s[40:43], s55 offen offset:1024 sc1\n" \
"s_add_u32 s55, s55, 0x200000\n" \
"s_xor_b32 s55, s55, s69\n" \
"s_add_u32 s35, s35, 1\n" \
"s_cmp_lt_u32 s35, 64\n" \
"s_cbranch_scc1 WL_%=\n" \
"s_waitcnt vmcnt(0)\n" \
"s_barrier\n" \
"v_mov_b32 v12, 64\n" \
"s_mov_b64 exec, 15\n" \
"buffer_store_dword v12, v11, s[40:43], s51 offen sc1\n" \
"s_mov_b64 exec, -1\n" \
"s_cmp_lg_u32 s50, 0\n" \
"s_cbranch_scc1 WE_%=\n" \
"v_mov_b32 v0, 1\n" \
"buffer_store_dword v0, off, s[40:43], 0 offset:256 sc1\n" \
"WE_%=:\n" \
"WCZ_%=:\n" \
"s_endpgm\n"
#define F1_ASM_TEXT \
"s_getpc_b64 s[46:47]\n" \
"FCW_%=:\n" \
"v_mbcnt_lo_u32_b32 v60, -1, 0\n" \
"v_mbcnt_hi_u32_b32 v60, -1, v60\n" \
"v_lshlrev_b32 v60, 7, v60\n" \
"v_min_u32 v61, FCZ_%=-FCW_%=, v60\n" \
"global_load_dword v62, v61, s[46:47]\n" \
"v_add_u32 v60, 0x2000, v60\n" \
"v_min_u32 v61, FCZ_%=-FCW_%=, v60\n" \
"global_load_dword v62, v61, s[46:47]\n" \
"v_add_u32 v60, 0x2000, v60\n" \
"s_mov_b64 s[40:41], %[ws]\n" \
"s_add_u32 s40, s40, 0x800000\n" \
"s_addc_u32 s41, s41, 0\n" \
"s_and_b32 s41, s41, 0xffff\n" \
"s_mov_b32 s42, 0x9000000\n" \
"s_mov_b32 s43, 0x00020000\n" \
"s_mov_b32 s33, %[wave]\n" \
"v_mbcnt_lo_u32_b32 v48, -1, 0\n" \
"v_mbcnt_hi_u32_b32 v48, -1, v48\n" \
"s_lshl_b32 s48, s33, 6\n" \
"v_add_lshl_u32 v48, v48, s48, 4\n" \
"ds_read_b128 v[0:3], v48 offset:0\n" \
"ds_read_b128 v[4:7], v48 offset:4096\n" \
"ds_read_b128 v[32:35], v48 offset:8192\n" \
"ds_read_b128 v[36:39], v48 offset:12288\n" \
"ds_read_b128 v[40:43], v48 offset:16384\n" \
"s_waitcnt lgkmcnt(0)\n" \
"s_barrier\n" \
"v_readfirstlane_b32 s70, v32\n" \
"v_readfirstlane_b32 s71, v33\n" \
"v_readfirstlane_b32 s72, v34\n" \
"v_readfirstlane_b32 s73, v35\n" \
"v_readfirstlane_b32 s51, v36\n" \
"v_readfirstlane_b32 s53, v37\n" \
"v_readfirstlane_b32 s55, v38\n" \
"s_nop 4\n" \
"s_lshl_b32 s34, s33, 10\n" \
"s_cmp_eq_u32 s33, 0\n" \
"s_cselect_b32 s51, s51, 0x7ff00000\n" \
"s_mov_b32 s50, 0x80000\n" \
"s_mov_b32 s35, 0\n" \
"s_mov_b64 s[36:37], s[70:71]\n" \
"s_and_b32 s37, s37, 0xffff\n" \
"s_mov_b32 s38, 0x21c000\n" \
"s_mov_b32 s39, 0x00020000\n" \
"s_mov_b64 s[44:45], s[72:73]\n" \
"s_and_b32 s45, s45, 0xffff\n" \
"s_mov_b32 s46, 0x21c000\n" \
"s_mov_b32 s47, 0x00020000\n" \
"s_sleep 60\n" \
"s_add_u32 m0, s34, 0\n" \
"s_mov_b32 s48, 0x0\n" \
"buffer_load_dwordx4 v1, s[36:39], s48 offen lds\n" \
"s_add_u32 m0, s34, 4096\n" \
"s_mov_b32 s48, 0x1b000\n" \
"buffer_load_dwordx4 v1, s[36:39], s48 offen lds\n" \
"s_add_u32 m0, s34, 8192\n" \
"s_mov_b32 s48, 0x36000\n" \
"buffer_load_dwordx4 v1, s[36:39], s48 offen lds\n" \
"s_add_u32 m0, s34, 12288\n" \
"s_mov_b32 s48, 0x51000\n" \
"buffer_load_dwordx4 v1, s[36:39], s48 offen lds\n" \
"s_add_u32 m0, s34, 16384\n" \
"s_mov_b32 s48, 0x0\n" \
"buffer_load_dwordx4 v1, s[44:47], s48 offen lds\n" \
"s_add_u32 m0, s34, 20480\n" \
"s_mov_b32 s48, 0x1b000\n" \
"buffer_load_dwordx4 v1, s[44:47], s48 offen lds\n" \
"s_add_u32 m0, s34, 24576\n" \
"s_mov_b32 s48, 0x36000\n" \
"buffer_load_dwordx4 v1, s[44:47], s48 offen lds\n" \
"s_add_u32 m0, s34, 28672\n" \
"s_mov_b32 s48, 0x51000\n" \
"buffer_load_dwordx4 v1, s[44:47], s48 offen lds\n" \
"s_add_u32 m0, s34, 32768\n" \
"s_mov_b32 s48, 0x6c000\n" \
"buffer_load_dwordx4 v1, s[36:39], s48 offen lds\n" \
"s_add_u32 m0, s34, 36864\n" \
"s_mov_b32 s48, 0x87000\n" \
"buffer_load_dwordx4 v1, s[36:39], s48 offen lds\n" \
"s_add_u32 m0, s34, 40960\n" \
"s_mov_b32 s48, 0xa2000\n" \
"buffer_load_dwordx4 v1, s[36:39], s48 offen lds\n" \
"s_add_u32 m0, s34, 45056\n" \
"s_mov_b32 s48, 0xbd000\n" \
"buffer_load_dwordx4 v1, s[36:39], s48 offen lds\n" \
"s_add_u32 m0, s34, 49152\n" \
"s_mov_b32 s48, 0x6c000\n" \
"buffer_load_dwordx4 v1, s[44:47], s48 offen lds\n" \
"s_add_u32 m0, s34, 53248\n" \
"s_mov_b32 s48, 0x87000\n" \
"buffer_load_dwordx4 v1, s[44:47], s48 offen lds\n" \
"s_add_u32 m0, s34, 57344\n" \
"s_mov_b32 s48, 0xa2000\n" \
"buffer_load_dwordx4 v1, s[44:47], s48 offen lds\n" \
"s_add_u32 m0, s34, 61440\n" \
"s_mov_b32 s48, 0xbd000\n" \
"buffer_load_dwordx4 v1, s[44:47], s48 offen lds\n" \
"s_add_u32 m0, s34, 65536\n" \
"s_mov_b32 s48, 0xd8000\n" \
"buffer_load_dwordx4 v1, s[36:39], s48 offen lds\n" \
"s_add_u32 m0, s34, 69632\n" \
"s_mov_b32 s48, 0xf3000\n" \
"buffer_load_dwordx4 v1, s[36:39], s48 offen lds\n" \
"s_add_u32 m0, s34, 73728\n" \
"s_mov_b32 s48, 0x10e000\n" \
"buffer_load_dwordx4 v1, s[36:39], s48 offen lds\n" \
"s_add_u32 m0, s34, 77824\n" \
"s_mov_b32 s48, 0x129000\n" \
"buffer_load_dwordx4 v1, s[36:39], s48 offen lds\n" \
"s_add_u32 m0, s34, 81920\n" \
"s_mov_b32 s48, 0xd8000\n" \
"buffer_load_dwordx4 v1, s[44:47], s48 offen lds\n" \
"s_add_u32 m0, s34, 86016\n" \
"s_mov_b32 s48, 0xf3000\n" \
"buffer_load_dwordx4 v1, s[44:47], s48 offen lds\n" \
"s_add_u32 m0, s34, 90112\n" \
"s_mov_b32 s48, 0x10e000\n" \
"buffer_load_dwordx4 v1, s[44:47], s48 offen lds\n" \
"s_add_u32 m0, s34, 94208\n" \
"s_mov_b32 s48, 0x129000\n" \
"buffer_load_dwordx4 v1, s[44:47], s48 offen lds\n" \
"s_add_u32 m0, s34, 98304\n" \
"s_mov_b32 s48, 0x144000\n" \
"buffer_load_dwordx4 v1, s[36:39], s48 offen lds\n" \
"s_add_u32 m0, s34, 102400\n" \
"s_mov_b32 s48, 0x15f000\n" \
"buffer_load_dwordx4 v1, s[36:39], s48 offen lds\n" \
"s_add_u32 m0, s34, 106496\n" \
"s_mov_b32 s48, 0x17a000\n" \
"buffer_load_dwordx4 v1, s[36:39], s48 offen lds\n" \
"s_add_u32 m0, s34, 110592\n" \
"s_mov_b32 s48, 0x195000\n" \
"buffer_load_dwordx4 v1, s[36:39], s48 offen lds\n" \
"s_add_u32 m0, s34, 114688\n" \
"s_mov_b32 s48, 0x144000\n" \
"buffer_load_dwordx4 v1, s[44:47], s48 offen lds\n" \
"s_add_u32 m0, s34, 118784\n" \
"s_mov_b32 s48, 0x15f000\n" \
"buffer_load_dwordx4 v1, s[44:47], s48 offen lds\n" \
"s_add_u32 m0, s34, 122880\n" \
"s_mov_b32 s48, 0x17a000\n" \
"buffer_load_dwordx4 v1, s[44:47], s48 offen lds\n" \
"s_add_u32 m0, s34, 126976\n" \
"s_mov_b32 s48, 0x195000\n" \
"buffer_load_dwordx4 v1, s[44:47], s48 offen lds\n" \
"s_add_u32 m0, s34, 131072\n" \
"s_mov_b32 s48, 0x1b0000\n" \
"buffer_load_dwordx4 v1, s[36:39], s48 offen lds\n" \
"s_add_u32 m0, s34, 135168\n" \
"s_mov_b32 s48, 0x1cb000\n" \
"buffer_load_dwordx4 v1, s[36:39], s48 offen lds\n" \
"s_add_u32 m0, s34, 139264\n" \
"s_mov_b32 s48, 0x1e6000\n" \
"buffer_load_dwordx4 v1, s[36:39], s48 offen lds\n" \
"s_add_u32 m0, s34, 143360\n" \
"s_mov_b32 s48, 0x201000\n" \
"buffer_load_dwordx4 v1, s[36:39], s48 offen lds\n" \
"s_add_u32 m0, s34, 147456\n" \
"s_mov_b32 s48, 0x1b0000\n" \
"buffer_load_dwordx4 v1, s[44:47], s48 offen lds\n" \
"s_add_u32 m0, s34, 151552\n" \
"s_mov_b32 s48, 0x1cb000\n" \
"buffer_load_dwordx4 v1, s[44:47], s48 offen lds\n" \
"s_add_u32 m0, s34, 155648\n" \
"s_mov_b32 s48, 0x1e6000\n" \
"buffer_load_dwordx4 v1, s[44:47], s48 offen lds\n" \
"s_add_u32 m0, s34, 159744\n" \
"s_mov_b32 s48, 0x201000\n" \
"buffer_load_dwordx4 v1, s[44:47], s48 offen lds\n" \
"s_waitcnt vmcnt(32)\n" \
"s_barrier\n" \
"v_add_u32 v20, 0x0, v2\n" \
"ds_read_b64_tr_b16 a[0:1], v20 offset:0\n" \
"ds_read_b64_tr_b16 a[2:3], v20 offset:512\n" \
"ds_read_b64_tr_b16 a[16:17], v20 offset:4096\n" \
"ds_read_b64_tr_b16 a[18:19], v20 offset:4608\n" \
"ds_read_b64_tr_b16 a[32:33], v20 offset:8192\n" \
"ds_read_b64_tr_b16 a[34:35], v20 offset:8704\n" \
"ds_read_b64_tr_b16 a[48:49], v20 offset:12288\n" \
"ds_read_b64_tr_b16 a[50:51], v20 offset:12800\n" \
"ds_read_b64_tr_b16 a[8:9], v20 offset:16384\n" \
"ds_read_b64_tr_b16 a[10:11], v20 offset:16896\n" \
"ds_read_b64_tr_b16 a[24:25], v20 offset:20480\n" \
"ds_read_b64_tr_b16 a[26:27], v20 offset:20992\n" \
"ds_read_b64_tr_b16 a[40:41], v20 offset:24576\n" \
"ds_read_b64_tr_b16 a[42:43], v20 offset:25088\n" \
"ds_read_b64_tr_b16 a[56:57], v20 offset:28672\n" \
"ds_read_b64_tr_b16 a[58:59], v20 offset:29184\n" \
"v_add_u32 v20, 0x0, v6\n" \
"ds_read_b64_tr_b16 a[4:5], v20 offset:0\n" \
"ds_read_b64_tr_b16 a[6:7], v20 offset:512\n" \
"ds_read_b64_tr_b16 a[20:21], v20 offset:4096\n" \
"ds_read_b64_tr_b16 a[22:23], v20 offset:4608\n" \
"ds_read_b64_tr_b16 a[36:37], v20 offset:8192\n" \
"ds_read_b64_tr_b16 a[38:39], v20 offset:8704\n" \
"ds_read_b64_tr_b16 a[52:53], v20 offset:12288\n" \
"ds_read_b64_tr_b16 a[54:55], v20 offset:12800\n" \
"ds_read_b64_tr_b16 a[12:13], v20 offset:16384\n" \
"ds_read_b64_tr_b16 a[14:15], v20 offset:16896\n" \
"ds_read_b64_tr_b16 a[28:29], v20 offset:20480\n" \
"ds_read_b64_tr_b16 a[30:31], v20 offset:20992\n" \
"ds_read_b64_tr_b16 a[44:45], v20 offset:24576\n" \
"ds_read_b64_tr_b16 a[46:47], v20 offset:25088\n" \
"ds_read_b64_tr_b16 a[60:61], v20 offset:28672\n" \
"ds_read_b64_tr_b16 a[62:63], v20 offset:29184\n" \
"s_waitcnt lgkmcnt(0)\n" \
"s_barrier\n" \
"s_waitcnt vmcnt(24)\n" \
"s_barrier\n" \
"v_add_u32 v20, 0x8000, v2\n" \
"ds_read_b64_tr_b16 a[64:65], v20 offset:0\n" \
"ds_read_b64_tr_b16 a[66:67], v20 offset:512\n" \
"ds_read_b64_tr_b16 a[80:81], v20 offset:4096\n" \
"ds_read_b64_tr_b16 a[82:83], v20 offset:4608\n" \
"ds_read_b64_tr_b16 a[96:97], v20 offset:8192\n" \
"ds_read_b64_tr_b16 a[98:99], v20 offset:8704\n" \
"ds_read_b64_tr_b16 a[112:113], v20 offset:12288\n" \
"ds_read_b64_tr_b16 a[114:115], v20 offset:12800\n" \
"ds_read_b64_tr_b16 a[72:73], v20 offset:16384\n" \
"ds_read_b64_tr_b16 a[74:75], v20 offset:16896\n" \
"ds_read_b64_tr_b16 a[88:89], v20 offset:20480\n" \
"ds_read_b64_tr_b16 a[90:91], v20 offset:20992\n" \
"ds_read_b64_tr_b16 a[104:105], v20 offset:24576\n" \
"ds_read_b64_tr_b16 a[106:107], v20 offset:25088\n" \
"ds_read_b64_tr_b16 a[120:121], v20 offset:28672\n" \
"ds_read_b64_tr_b16 a[122:123], v20 offset:29184\n" \
"v_add_u32 v20, 0x8000, v6\n" \
"ds_read_b64_tr_b16 a[68:69], v20 offset:0\n" \
"ds_read_b64_tr_b16 a[70:71], v20 offset:512\n" \
"ds_read_b64_tr_b16 a[84:85], v20 offset:4096\n" \
"ds_read_b64_tr_b16 a[86:87], v20 offset:4608\n" \
"ds_read_b64_tr_b16 a[100:101], v20 offset:8192\n" \
"ds_read_b64_tr_b16 a[102:103], v20 offset:8704\n" \
"ds_read_b64_tr_b16 a[116:117], v20 offset:12288\n" \
"ds_read_b64_tr_b16 a[118:119], v20 offset:12800\n" \
"ds_read_b64_tr_b16 a[76:77], v20 offset:16384\n" \
"ds_read_b64_tr_b16 a[78:79], v20 offset:16896\n" \
"ds_read_b64_tr_b16 a[92:93], v20 offset:20480\n" \
"ds_read_b64_tr_b16 a[94:95], v20 offset:20992\n" \
"ds_read_b64_tr_b16 a[108:109], v20 offset:24576\n" \
"ds_read_b64_tr_b16 a[110:111], v20 offset:25088\n" \
"ds_read_b64_tr_b16 a[124:125], v20 offset:28672\n" \
"ds_read_b64_tr_b16 a[126:127], v20 offset:29184\n" \
"s_waitcnt lgkmcnt(0)\n" \
"s_barrier\n" \
"s_waitcnt vmcnt(16)\n" \
"s_barrier\n" \
"v_add_u32 v20, 0x10000, v2\n" \
"ds_read_b64_tr_b16 a[128:129], v20 offset:0\n" \
"ds_read_b64_tr_b16 a[130:131], v20 offset:512\n" \
"ds_read_b64_tr_b16 a[144:145], v20 offset:4096\n" \
"ds_read_b64_tr_b16 a[146:147], v20 offset:4608\n" \
"ds_read_b64_tr_b16 a[160:161], v20 offset:8192\n" \
"ds_read_b64_tr_b16 a[162:163], v20 offset:8704\n" \
"ds_read_b64_tr_b16 a[176:177], v20 offset:12288\n" \
"ds_read_b64_tr_b16 a[178:179], v20 offset:12800\n" \
"ds_read_b64_tr_b16 a[136:137], v20 offset:16384\n" \
"ds_read_b64_tr_b16 a[138:139], v20 offset:16896\n" \
"ds_read_b64_tr_b16 a[152:153], v20 offset:20480\n" \
"ds_read_b64_tr_b16 a[154:155], v20 offset:20992\n" \
"ds_read_b64_tr_b16 a[168:169], v20 offset:24576\n" \
"ds_read_b64_tr_b16 a[170:171], v20 offset:25088\n" \
"ds_read_b64_tr_b16 a[184:185], v20 offset:28672\n" \
"ds_read_b64_tr_b16 a[186:187], v20 offset:29184\n" \
"v_add_u32 v20, 0x10000, v6\n" \
"ds_read_b64_tr_b16 a[132:133], v20 offset:0\n" \
"ds_read_b64_tr_b16 a[134:135], v20 offset:512\n" \
"ds_read_b64_tr_b16 a[148:149], v20 offset:4096\n" \
"ds_read_b64_tr_b16 a[150:151], v20 offset:4608\n" \
"ds_read_b64_tr_b16 a[164:165], v20 offset:8192\n" \
"ds_read_b64_tr_b16 a[166:167], v20 offset:8704\n" \
"ds_read_b64_tr_b16 a[180:181], v20 offset:12288\n" \
"ds_read_b64_tr_b16 a[182:183], v20 offset:12800\n" \
"ds_read_b64_tr_b16 a[140:141], v20 offset:16384\n" \
"ds_read_b64_tr_b16 a[142:143], v20 offset:16896\n" \
"ds_read_b64_tr_b16 a[156:157], v20 offset:20480\n" \
"ds_read_b64_tr_b16 a[158:159], v20 offset:20992\n" \
"ds_read_b64_tr_b16 a[172:173], v20 offset:24576\n" \
"ds_read_b64_tr_b16 a[174:175], v20 offset:25088\n" \
"ds_read_b64_tr_b16 a[188:189], v20 offset:28672\n" \
"ds_read_b64_tr_b16 a[190:191], v20 offset:29184\n" \
"s_waitcnt lgkmcnt(0)\n" \
"s_barrier\n" \
"s_waitcnt vmcnt(8)\n" \
"s_barrier\n" \
"v_add_u32 v20, 0x18000, v2\n" \
"ds_read_b64_tr_b16 a[192:193], v20 offset:0\n" \
"ds_read_b64_tr_b16 a[194:195], v20 offset:512\n" \
"ds_read_b64_tr_b16 a[208:209], v20 offset:4096\n" \
"ds_read_b64_tr_b16 a[210:211], v20 offset:4608\n" \
"ds_read_b64_tr_b16 a[224:225], v20 offset:8192\n" \
"ds_read_b64_tr_b16 a[226:227], v20 offset:8704\n" \
"ds_read_b64_tr_b16 a[240:241], v20 offset:12288\n" \
"ds_read_b64_tr_b16 a[242:243], v20 offset:12800\n" \
"ds_read_b64_tr_b16 a[200:201], v20 offset:16384\n" \
"ds_read_b64_tr_b16 a[202:203], v20 offset:16896\n" \
"ds_read_b64_tr_b16 a[216:217], v20 offset:20480\n" \
"ds_read_b64_tr_b16 a[218:219], v20 offset:20992\n" \
"ds_read_b64_tr_b16 a[232:233], v20 offset:24576\n" \
"ds_read_b64_tr_b16 a[234:235], v20 offset:25088\n" \
"ds_read_b64_tr_b16 a[248:249], v20 offset:28672\n" \
"ds_read_b64_tr_b16 a[250:251], v20 offset:29184\n" \
"v_add_u32 v20, 0x18000, v6\n" \
"ds_read_b64_tr_b16 a[196:197], v20 offset:0\n" \
"ds_read_b64_tr_b16 a[198:199], v20 offset:512\n" \
"ds_read_b64_tr_b16 a[212:213], v20 offset:4096\n" \
"ds_read_b64_tr_b16 a[214:215], v20 offset:4608\n" \
"ds_read_b64_tr_b16 a[228:229], v20 offset:8192\n" \
"ds_read_b64_tr_b16 a[230:231], v20 offset:8704\n" \
"ds_read_b64_tr_b16 a[244:245], v20 offset:12288\n" \
"ds_read_b64_tr_b16 a[246:247], v20 offset:12800\n" \
"ds_read_b64_tr_b16 a[204:205], v20 offset:16384\n" \
"ds_read_b64_tr_b16 a[206:207], v20 offset:16896\n" \
"ds_read_b64_tr_b16 a[220:221], v20 offset:20480\n" \
"ds_read_b64_tr_b16 a[222:223], v20 offset:20992\n" \
"ds_read_b64_tr_b16 a[236:237], v20 offset:24576\n" \
"ds_read_b64_tr_b16 a[238:239], v20 offset:25088\n" \
"ds_read_b64_tr_b16 a[252:253], v20 offset:28672\n" \
"ds_read_b64_tr_b16 a[254:255], v20 offset:29184\n" \
"s_waitcnt lgkmcnt(0)\n" \
"s_barrier\n" \
"s_waitcnt vmcnt(0)\n" \
"s_barrier\n" \
"v_add_u32 v20, 0x20000, v2\n" \
"ds_read_b64_tr_b16 v[192:193], v20 offset:0\n" \
"ds_read_b64_tr_b16 v[194:195], v20 offset:512\n" \
"ds_read_b64_tr_b16 v[208:209], v20 offset:4096\n" \
"ds_read_b64_tr_b16 v[210:211], v20 offset:4608\n" \
"ds_read_b64_tr_b16 v[224:225], v20 offset:8192\n" \
"ds_read_b64_tr_b16 v[226:227], v20 offset:8704\n" \
"ds_read_b64_tr_b16 v[240:241], v20 offset:12288\n" \
"ds_read_b64_tr_b16 v[242:243], v20 offset:12800\n" \
"ds_read_b64_tr_b16 v[200:201], v20 offset:16384\n" \
"ds_read_b64_tr_b16 v[202:203], v20 offset:16896\n" \
"ds_read_b64_tr_b16 v[216:217], v20 offset:20480\n" \
"ds_read_b64_tr_b16 v[218:219], v20 offset:20992\n" \
"ds_read_b64_tr_b16 v[232:233], v20 offset:24576\n" \
"ds_read_b64_tr_b16 v[234:235], v20 offset:25088\n" \
"ds_read_b64_tr_b16 v[248:249], v20 offset:28672\n" \
"ds_read_b64_tr_b16 v[250:251], v20 offset:29184\n" \
"v_add_u32 v20, 0x20000, v6\n" \
"ds_read_b64_tr_b16 v[196:197], v20 offset:0\n" \
"ds_read_b64_tr_b16 v[198:199], v20 offset:512\n" \
"ds_read_b64_tr_b16 v[212:213], v20 offset:4096\n" \
"ds_read_b64_tr_b16 v[214:215], v20 offset:4608\n" \
"ds_read_b64_tr_b16 v[228:229], v20 offset:8192\n" \
"ds_read_b64_tr_b16 v[230:231], v20 offset:8704\n" \
"ds_read_b64_tr_b16 v[244:245], v20 offset:12288\n" \
"ds_read_b64_tr_b16 v[246:247], v20 offset:12800\n" \
"ds_read_b64_tr_b16 v[204:205], v20 offset:16384\n" \
"ds_read_b64_tr_b16 v[206:207], v20 offset:16896\n" \
"ds_read_b64_tr_b16 v[220:221], v20 offset:20480\n" \
"ds_read_b64_tr_b16 v[222:223], v20 offset:20992\n" \
"ds_read_b64_tr_b16 v[236:237], v20 offset:24576\n" \
"ds_read_b64_tr_b16 v[238:239], v20 offset:25088\n" \
"ds_read_b64_tr_b16 v[252:253], v20 offset:28672\n" \
"ds_read_b64_tr_b16 v[254:255], v20 offset:29184\n" \
"s_waitcnt lgkmcnt(0)\n" \
"s_barrier\n" \
"s_mov_b32 s54, s34\n" \
"s_mov_b32 s56, 0\n" \
"v_lshlrev_b32 v11, 10, v0\n" \
"v_mov_b32 v15, 0x22000\n" \
"s_lshl_b32 s48, s33, 2\n" \
"v_add_u32 v14, s48, v15\n" \
"FL_%=:\n" \
"s_add_u32 s52, s35, 1\n" \
"s_bitcmp1_b64 %[dpar], s35\n" \
"s_cselect_b32 s69, 0x2000, 0\n" \
"s_add_u32 s57, s53, 0x200000\n" \
"s_xor_b32 s57, s57, s69\n" \
"s_xor_b32 s58, s54, 0x10000\n" \
"s_cmp_lg_u32 s56, 0\n" \
"s_cbranch_scc1 FB_%=\n" \
"buffer_load_dword v13, v4, s[40:43], 0 offen sc1\n" \
"s_waitcnt vmcnt(0)\n" \
"s_barrier\n" \
"v_mov_b32 v12, s35\n" \
"s_mov_b64 exec, 15\n" \
"buffer_store_dword v12, v11, s[40:43], s51 offen sc1\n" \
"s_mov_b64 exec, -1\n" \
"s_cmp_lg_u32 s33, 0\n" \
"s_cbranch_scc1 FPD_%=\n" \
"s_branch FPC_%=\n" \
"FP_%=:\n" \
"buffer_load_dword v13, v4, s[40:43], 0 offen sc1\n" \
"s_waitcnt vmcnt(0)\n" \
"FPC_%=:\n" \
"v_cmp_gt_u32 vcc, s52, v13\n" \
"s_cbranch_vccz FPD_%=\n" \
"s_sleep 2\n" \
"s_sub_u32 s50, s50, 1\n" \
"s_cbranch_scc0 FP_%=\n" \
"s_mov_b32 s50, 0\n" \
"FPD_%=:\n" \
"s_barrier\n" \
"s_add_u32 m0, s54, 0\n" \
"s_add_u32 s48, s53, 0\n" \
"buffer_load_dwordx4 v5, s[40:43], s48 offen sc1 lds\n" \
"s_add_u32 m0, s54, 4096\n" \
"s_add_u32 s48, s53, 128\n" \
"buffer_load_dwordx4 v5, s[40:43], s48 offen sc1 lds\n" \
"s_add_u32 m0, s54, 8192\n" \
"s_add_u32 s48, s53, 256\n" \
"buffer_load_dwordx4 v5, s[40:43], s48 offen sc1 lds\n" \
"s_add_u32 m0, s54, 12288\n" \
"s_add_u32 s48, s53, 384\n" \
"buffer_load_dwordx4 v5, s[40:43], s48 offen sc1 lds\n" \
"s_add_u32 m0, s54, 16384\n" \
"s_add_u32 s48, s53, 512\n" \
"buffer_load_dwordx4 v5, s[40:43], s48 offen sc1 lds\n" \
"s_add_u32 m0, s54, 20480\n" \
"s_add_u32 s48, s53, 640\n" \
"buffer_load_dwordx4 v5, s[40:43], s48 offen sc1 lds\n" \
"s_add_u32 s52, s52, 1\n" \
"s_waitcnt vmcnt(4)\n" \
"s_barrier\n" \
"ds_read_b128 v[32:35], v3 offset:0\n" \
"ds_read_b128 v[36:39], v3 offset:2048\n" \
"ds_read_b128 v[40:43], v3 offset:4096\n" \
"s_waitcnt lgkmcnt(2)\n" \
"v_mfma_f32_16x16x32_bf16 v[16:19], a[0:3], v[32:35], 0\n" \
"ds_read_b128 v[44:47], v3 offset:6144\n" \
"v_mfma_f32_16x16x32_bf16 v[20:23], a[4:7], v[32:35], 0\n" \
"s_add_u32 m0, s54, 24576\n" \
"s_add_u32 s48, s53, 768\n" \
"v_mfma_f32_16x16x32_bf16 v[24:27], a[8:11], v[32:35], 0\n" \
"buffer_load_dwordx4 v5, s[40:43], s48 offen sc1 lds\n" \
"v_mfma_f32_16x16x32_bf16 v[28:31], a[12:15], v[32:35], 0\n" \
"s_waitcnt vmcnt(3)\n" \
"s_barrier\n" \
"s_waitcnt lgkmcnt(2)\n" \
"v_mfma_f32_16x16x32_bf16 v[16:19], a[16:19], v[36:39], v[16:19]\n" \
"ds_read_b128 v[32:35], v3 offset:8192\n" \
"v_mfma_f32_16x16x32_bf16 v[20:23], a[20:23], v[36:39], v[20:23]\n" \
"s_add_u32 m0, s54, 28672\n" \
"s_add_u32 s48, s53, 896\n" \
"v_mfma_f32_16x16x32_bf16 v[24:27], a[24:27], v[36:39], v[24:27]\n" \
"buffer_load_dwordx4 v5, s[40:43], s48 offen sc1 lds\n" \
"v_mfma_f32_16x16x32_bf16 v[28:31], a[28:31], v[36:39], v[28:31]\n" \
"s_waitcnt lgkmcnt(2)\n" \
"v_mfma_f32_16x16x32_bf16 v[16:19], a[32:35], v[40:43], v[16:19]\n" \
"ds_read_b128 v[36:39], v3 offset:10240\n" \
"v_mfma_f32_16x16x32_bf16 v[20:23], a[36:39], v[40:43], v[20:23]\n" \
"s_add_u32 m0, s54, 32768\n" \
"s_add_u32 s48, s53, 1024\n" \
"v_mfma_f32_16x16x32_bf16 v[24:27], a[40:43], v[40:43], v[24:27]\n" \
"buffer_load_dwordx4 v5, s[40:43], s48 offen sc1 lds\n" \
"v_mfma_f32_16x16x32_bf16 v[28:31], a[44:47], v[40:43], v[28:31]\n" \
"s_waitcnt lgkmcnt(2)\n" \
"v_mfma_f32_16x16x32_bf16 v[16:19], a[48:51], v[44:47], v[16:19]\n" \
"ds_read_b128 v[40:43], v3 offset:12288\n" \
"v_mfma_f32_16x16x32_bf16 v[20:23], a[52:55], v[44:47], v[20:23]\n" \
"s_add_u32 m0, s54, 36864\n" \
"s_add_u32 s48, s53, 1152\n" \
"v_mfma_f32_16x16x32_bf16 v[24:27], a[56:59], v[44:47], v[24:27]\n" \
"buffer_load_dwordx4 v5, s[40:43], s48 offen sc1 lds\n" \
"v_mfma_f32_16x16x32_bf16 v[28:31], a[60:63], v[44:47], v[28:31]\n" \
"s_waitcnt lgkmcnt(2)\n" \
"v_mfma_f32_16x16x32_bf16 v[16:19], a[64:67], v[32:35], v[16:19]\n" \
"ds_read_b128 v[44:47], v3 offset:14336\n" \
"v_mfma_f32_16x16x32_bf16 v[20:23], a[68:71], v[32:35], v[20:23]\n" \
"v_mfma_f32_16x16x32_bf16 v[24:27], a[72:75], v[32:35], v[24:27]\n" \
"v_mfma_f32_16x16x32_bf16 v[28:31], a[76:79], v[32:35], v[28:31]\n" \
"s_waitcnt vmcnt(4)\n" \
"s_barrier\n" \
"s_waitcnt lgkmcnt(2)\n" \
"v_mfma_f32_16x16x32_bf16 v[16:19], a[80:83], v[36:39], v[16:19]\n" \
"ds_read_b128 v[32:35], v3 offset:16384\n" \
"v_mfma_f32_16x16x32_bf16 v[20:23], a[84:87], v[36:39], v[20:23]\n" \
"v_mfma_f32_16x16x32_bf16 v[24:27], a[88:91], v[36:39], v[24:27]\n" \
"v_mfma_f32_16x16x32_bf16 v[28:31], a[92:95], v[36:39], v[28:31]\n" \
"s_waitcnt lgkmcnt(2)\n" \
"v_mfma_f32_16x16x32_bf16 v[16:19], a[96:99], v[40:43], v[16:19]\n" \
"ds_read_b128 v[36:39], v3 offset:18432\n" \
"v_mfma_f32_16x16x32_bf16 v[20:23], a[100:103], v[40:43], v[20:23]\n" \
"v_mfma_f32_16x16x32_bf16 v[24:27], a[104:107], v[40:43], v[24:27]\n" \
"buffer_load_dword v13, v4, s[40:43], 0 offen sc1\n" \
"v_mfma_f32_16x16x32_bf16 v[28:31], a[108:111], v[40:43], v[28:31]\n" \
"s_waitcnt lgkmcnt(2)\n" \
"v_mfma_f32_16x16x32_bf16 v[16:19], a[112:115], v[44:47], v[16:19]\n" \
"ds_read_b128 v[40:43], v3 offset:20480\n" \
"v_mfma_f32_16x16x32_bf16 v[20:23], a[116:119], v[44:47], v[20:23]\n" \
"v_mfma_f32_16x16x32_bf16 v[24:27], a[120:123], v[44:47], v[24:27]\n" \
"v_mfma_f32_16x16x32_bf16 v[28:31], a[124:127], v[44:47], v[28:31]\n" \
"s_waitcnt lgkmcnt(2)\n" \
"v_mfma_f32_16x16x32_bf16 v[16:19], a[128:131], v[32:35], v[16:19]\n" \
"ds_read_b128 v[44:47], v3 offset:22528\n" \
"v_mfma_f32_16x16x32_bf16 v[20:23], a[132:135], v[32:35], v[20:23]\n" \
"v_mfma_f32_16x16x32_bf16 v[24:27], a[136:139], v[32:35], v[24:27]\n" \
"v_mfma_f32_16x16x32_bf16 v[28:31], a[140:143], v[32:35], v[28:31]\n" \
"s_waitcnt vmcnt(1)\n" \
"s_barrier\n" \
"s_waitcnt lgkmcnt(2)\n" \
"v_mfma_f32_16x16x32_bf16 v[16:19], a[144:147], v[36:39], v[16:19]\n" \
"ds_read_b128 v[32:35], v3 offset:24576\n" \
"v_mfma_f32_16x16x32_bf16 v[20:23], a[148:151], v[36:39], v[20:23]\n" \
"v_mfma_f32_16x16x32_bf16 v[24:27], a[152:155], v[36:39], v[24:27]\n" \
"v_mfma_f32_16x16x32_bf16 v[28:31], a[156:159], v[36:39], v[28:31]\n" \
"s_waitcnt lgkmcnt(2)\n" \
"v_mfma_f32_16x16x32_bf16 v[16:19], a[160:163], v[40:43], v[16:19]\n" \
"ds_read_b128 v[36:39], v3 offset:26624\n" \
"v_mfma_f32_16x16x32_bf16 v[20:23], a[164:167], v[40:43], v[20:23]\n" \
"v_mfma_f32_16x16x32_bf16 v[24:27], a[168:171], v[40:43], v[24:27]\n" \
"v_mfma_f32_16x16x32_bf16 v[28:31], a[172:175], v[40:43], v[28:31]\n" \
"s_waitcnt lgkmcnt(2)\n" \
"v_mfma_f32_16x16x32_bf16 v[16:19], a[176:179], v[44:47], v[16:19]\n" \
"ds_read_b128 v[40:43], v3 offset:28672\n" \
"v_mfma_f32_16x16x32_bf16 v[20:23], a[180:183], v[44:47], v[20:23]\n" \
"v_mfma_f32_16x16x32_bf16 v[24:27], a[184:187], v[44:47], v[24:27]\n" \
"v_mfma_f32_16x16x32_bf16 v[28:31], a[188:191], v[44:47], v[28:31]\n" \
"s_waitcnt vmcnt(0)\n" \
"v_cmp_gt_u32 vcc, s52, v13\n" \
"s_waitcnt lgkmcnt(2)\n" \
"v_mfma_f32_16x16x32_bf16 v[16:19], a[192:195], v[32:35], v[16:19]\n" \
"ds_read_b128 v[44:47], v3 offset:30720\n" \
"v_mfma_f32_16x16x32_bf16 v[20:23], a[196:199], v[32:35], v[20:23]\n" \
"v_mfma_f32_16x16x32_bf16 v[24:27], a[200:203], v[32:35], v[24:27]\n" \
"s_cmp_eq_u64 vcc, 0\n" \
"s_cselect_b32 s56, 1, 0\n" \
"v_mfma_f32_16x16x32_bf16 v[28:31], a[204:207], v[32:35], v[28:31]\n" \
"v_mov_b32 v13, s56\n" \
"ds_write_b32 v14, v13\n" \
"s_waitcnt lgkmcnt(3)\n" \
"v_mfma_f32_16x16x32_bf16 v[16:19], a[208:211], v[36:39], v[16:19]\n" \
"ds_read_b128 v[32:35], v3 offset:32768\n" \
"v_mfma_f32_16x16x32_bf16 v[20:23], a[212:215], v[36:39], v[20:23]\n" \
"v_mfma_f32_16x16x32_bf16 v[24:27], a[216:219], v[36:39], v[24:27]\n" \
"v_mfma_f32_16x16x32_bf16 v[28:31], a[220:223], v[36:39], v[28:31]\n" \
"s_waitcnt lgkmcnt(1)\n" \
"s_barrier\n" \
"v_mfma_f32_16x16x32_bf16 v[16:19], a[224:227], v[40:43], v[16:19]\n" \
"ds_read_b128 v[36:39], v3 offset:34816\n" \
"v_mfma_f32_16x16x32_bf16 v[20:23], a[228:231], v[40:43], v[20:23]\n" \
"v_mfma_f32_16x16x32_bf16 v[24:27], a[232:235], v[40:43], v[24:27]\n" \
"ds_read_b32 v13, v15\n" \
"v_mfma_f32_16x16x32_bf16 v[28:31], a[236:239], v[40:43], v[28:31]\n" \
"v_mfma_f32_16x16x32_bf16 v[16:19], a[240:243], v[44:47], v[16:19]\n" \
"ds_read_b128 v[40:43], v3 offset:36864\n" \
"v_mfma_f32_16x16x32_bf16 v[20:23], a[244:247], v[44:47], v[20:23]\n" \
"v_mfma_f32_16x16x32_bf16 v[24:27], a[248:251], v[44:47], v[24:27]\n" \
"s_waitcnt lgkmcnt(1)\n" \
"v_readfirstlane_b32 s56, v13\n" \
"v_mfma_f32_16x16x32_bf16 v[28:31], a[252:255], v[44:47], v[28:31]\n" \
"s_branch FM_%=\n" \
"FB_%=:\n" \
"s_add_u32 s52, s52, 1\n" \
"s_waitcnt vmcnt(5)\n" \
"s_barrier\n" \
"ds_read_b128 v[32:35], v3 offset:0\n" \
"ds_read_b128 v[36:39], v3 offset:2048\n" \
"ds_read_b128 v[40:43], v3 offset:4096\n" \
"s_waitcnt lgkmcnt(2)\n" \
"v_mfma_f32_16x16x32_bf16 v[16:19], a[0:3], v[32:35], 0\n" \
"ds_read_b128 v[44:47], v3 offset:6144\n" \
"v_mfma_f32_16x16x32_bf16 v[20:23], a[4:7], v[32:35], 0\n" \
"s_add_u32 m0, s54, 24576\n" \
"s_add_u32 s48, s53, 768\n" \
"v_mfma_f32_16x16x32_bf16 v[24:27], a[8:11], v[32:35], 0\n" \
"buffer_load_dwordx4 v5, s[40:43], s48 offen sc1 lds\n" \
"v_mfma_f32_16x16x32_bf16 v[28:31], a[12:15], v[32:35], 0\n" \
"s_waitcnt vmcnt(4)\n" \
"s_barrier\n" \
"s_waitcnt lgkmcnt(2)\n" \
"v_mfma_f32_16x16x32_bf16 v[16:19], a[16:19], v[36:39], v[16:19]\n" \
"ds_read_b128 v[32:35], v3 offset:8192\n" \
"v_mfma_f32_16x16x32_bf16 v[20:23], a[20:23], v[36:39], v[20:23]\n" \
"s_add_u32 m0, s54, 28672\n" \
"s_add_u32 s48, s53, 896\n" \
"v_mfma_f32_16x16x32_bf16 v[24:27], a[24:27], v[36:39], v[24:27]\n" \
"buffer_load_dwordx4 v5, s[40:43], s48 offen sc1 lds\n" \
"v_mfma_f32_16x16x32_bf16 v[28:31], a[28:31], v[36:39], v[28:31]\n" \
"s_waitcnt lgkmcnt(2)\n" \
"v_mfma_f32_16x16x32_bf16 v[16:19], a[32:35], v[40:43], v[16:19]\n" \
"ds_read_b128 v[36:39], v3 offset:10240\n" \
"v_mfma_f32_16x16x32_bf16 v[20:23], a[36:39], v[40:43], v[20:23]\n" \
"s_add_u32 m0, s54, 32768\n" \
"s_add_u32 s48, s53, 1024\n" \
"v_mfma_f32_16x16x32_bf16 v[24:27], a[40:43], v[40:43], v[24:27]\n" \
"buffer_load_dwordx4 v5, s[40:43], s48 offen sc1 lds\n" \
"v_mfma_f32_16x16x32_bf16 v[28:31], a[44:47], v[40:43], v[28:31]\n" \
"s_waitcnt lgkmcnt(2)\n" \
"v_mfma_f32_16x16x32_bf16 v[16:19], a[48:51], v[44:47], v[16:19]\n" \
"ds_read_b128 v[40:43], v3 offset:12288\n" \
"v_mfma_f32_16x16x32_bf16 v[20:23], a[52:55], v[44:47], v[20:23]\n" \
"s_add_u32 m0, s54, 36864\n" \
"s_add_u32 s48, s53, 1152\n" \
"v_mfma_f32_16x16x32_bf16 v[24:27], a[56:59], v[44:47], v[24:27]\n" \
"buffer_load_dwordx4 v5, s[40:43], s48 offen sc1 lds\n" \
"v_mfma_f32_16x16x32_bf16 v[28:31], a[60:63], v[44:47], v[28:31]\n" \
"s_waitcnt lgkmcnt(2)\n" \
"v_mfma_f32_16x16x32_bf16 v[16:19], a[64:67], v[32:35], v[16:19]\n" \
"ds_read_b128 v[44:47], v3 offset:14336\n" \
"v_mfma_f32_16x16x32_bf16 v[20:23], a[68:71], v[32:35], v[20:23]\n" \
"v_mfma_f32_16x16x32_bf16 v[24:27], a[72:75], v[32:35], v[24:27]\n" \
"v_mfma_f32_16x16x32_bf16 v[28:31], a[76:79], v[32:35], v[28:31]\n" \
"s_waitcnt vmcnt(5)\n" \
"s_barrier\n" \
"s_waitcnt lgkmcnt(2)\n" \
"v_mfma_f32_16x16x32_bf16 v[16:19], a[80:83], v[36:39], v[16:19]\n" \
"ds_read_b128 v[32:35], v3 offset:16384\n" \
"v_mfma_f32_16x16x32_bf16 v[20:23], a[84:87], v[36:39], v[20:23]\n" \
"v_mfma_f32_16x16x32_bf16 v[24:27], a[88:91], v[36:39], v[24:27]\n" \
"v_mfma_f32_16x16x32_bf16 v[28:31], a[92:95], v[36:39], v[28:31]\n" \
"s_waitcnt lgkmcnt(2)\n" \
"v_mfma_f32_16x16x32_bf16 v[16:19], a[96:99], v[40:43], v[16:19]\n" \
"ds_read_b128 v[36:39], v3 offset:18432\n" \
"v_mfma_f32_16x16x32_bf16 v[20:23], a[100:103], v[40:43], v[20:23]\n" \
"v_mfma_f32_16x16x32_bf16 v[24:27], a[104:107], v[40:43], v[24:27]\n" \
"buffer_load_dword v13, v4, s[40:43], 0 offen sc1\n" \
"v_mfma_f32_16x16x32_bf16 v[28:31], a[108:111], v[40:43], v[28:31]\n" \
"s_waitcnt lgkmcnt(2)\n" \
"v_mfma_f32_16x16x32_bf16 v[16:19], a[112:115], v[44:47], v[16:19]\n" \
"ds_read_b128 v[40:43], v3 offset:20480\n" \
"v_mfma_f32_16x16x32_bf16 v[20:23], a[116:119], v[44:47], v[20:23]\n" \
"v_mfma_f32_16x16x32_bf16 v[24:27], a[120:123], v[44:47], v[24:27]\n" \
"v_mfma_f32_16x16x32_bf16 v[28:31], a[124:127], v[44:47], v[28:31]\n" \
"s_waitcnt lgkmcnt(2)\n" \
"v_mfma_f32_16x16x32_bf16 v[16:19], a[128:131], v[32:35], v[16:19]\n" \
"ds_read_b128 v[44:47], v3 offset:22528\n" \
"v_mfma_f32_16x16x32_bf16 v[20:23], a[132:135], v[32:35], v[20:23]\n" \
"v_mfma_f32_16x16x32_bf16 v[24:27], a[136:139], v[32:35], v[24:27]\n" \
"v_mfma_f32_16x16x32_bf16 v[28:31], a[140:143], v[32:35], v[28:31]\n" \
"s_waitcnt vmcnt(1)\n" \
"s_barrier\n" \
"s_waitcnt lgkmcnt(2)\n" \
"v_mfma_f32_16x16x32_bf16 v[16:19], a[144:147], v[36:39], v[16:19]\n" \
"ds_read_b128 v[32:35], v3 offset:24576\n" \
"v_mfma_f32_16x16x32_bf16 v[20:23], a[148:151], v[36:39], v[20:23]\n" \
"v_mfma_f32_16x16x32_bf16 v[24:27], a[152:155], v[36:39], v[24:27]\n" \
"v_mfma_f32_16x16x32_bf16 v[28:31], a[156:159], v[36:39], v[28:31]\n" \
"v_mov_b32 v12, s35\n" \
"s_mov_b64 exec, 15\n" \
"buffer_store_dword v12, v11, s[40:43], s51 offen sc1\n" \
"s_mov_b64 exec, -1\n" \
"s_waitcnt lgkmcnt(2)\n" \
"v_mfma_f32_16x16x32_bf16 v[16:19], a[160:163], v[40:43], v[16:19]\n" \
"ds_read_b128 v[36:39], v3 offset:26624\n" \
"v_mfma_f32_16x16x32_bf16 v[20:23], a[164:167], v[40:43], v[20:23]\n" \
"v_mfma_f32_16x16x32_bf16 v[24:27], a[168:171], v[40:43], v[24:27]\n" \
"v_mfma_f32_16x16x32_bf16 v[28:31], a[172:175], v[40:43], v[28:31]\n" \
"s_waitcnt lgkmcnt(2)\n" \
"v_mfma_f32_16x16x32_bf16 v[16:19], a[176:179], v[44:47], v[16:19]\n" \
"ds_read_b128 v[40:43], v3 offset:28672\n" \
"v_mfma_f32_16x16x32_bf16 v[20:23], a[180:183], v[44:47], v[20:23]\n" \
"v_mfma_f32_16x16x32_bf16 v[24:27], a[184:187], v[44:47], v[24:27]\n" \
"v_mfma_f32_16x16x32_bf16 v[28:31], a[188:191], v[44:47], v[28:31]\n" \
"s_waitcnt vmcnt(1)\n" \
"v_cmp_gt_u32 vcc, s52, v13\n" \
"s_waitcnt lgkmcnt(2)\n" \
"v_mfma_f32_16x16x32_bf16 v[16:19], a[192:195], v[32:35], v[16:19]\n" \
"ds_read_b128 v[44:47], v3 offset:30720\n" \
"v_mfma_f32_16x16x32_bf16 v[20:23], a[196:199], v[32:35], v[20:23]\n" \
"v_mfma_f32_16x16x32_bf16 v[24:27], a[200:203], v[32:35], v[24:27]\n" \
"s_cmp_eq_u64 vcc, 0\n" \
"s_cselect_b32 s56, 1, 0\n" \
"v_mfma_f32_16x16x32_bf16 v[28:31], a[204:207], v[32:35], v[28:31]\n" \
"v_mov_b32 v13, s56\n" \
"ds_write_b32 v14, v13\n" \
"s_waitcnt lgkmcnt(3)\n" \
"v_mfma_f32_16x16x32_bf16 v[16:19], a[208:211], v[36:39], v[16:19]\n" \
"ds_read_b128 v[32:35], v3 offset:32768\n" \
"v_mfma_f32_16x16x32_bf16 v[20:23], a[212:215], v[36:39], v[20:23]\n" \
"v_mfma_f32_16x16x32_bf16 v[24:27], a[216:219], v[36:39], v[24:27]\n" \
"v_mfma_f32_16x16x32_bf16 v[28:31], a[220:223], v[36:39], v[28:31]\n" \
"s_waitcnt lgkmcnt(1)\n" \
"s_barrier\n" \
"v_mfma_f32_16x16x32_bf16 v[16:19], a[224:227], v[40:43], v[16:19]\n" \
"ds_read_b128 v[36:39], v3 offset:34816\n" \
"v_mfma_f32_16x16x32_bf16 v[20:23], a[228:231], v[40:43], v[20:23]\n" \
"v_mfma_f32_16x16x32_bf16 v[24:27], a[232:235], v[40:43], v[24:27]\n" \
"ds_read_b32 v13, v15\n" \
"v_mfma_f32_16x16x32_bf16 v[28:31], a[236:239], v[40:43], v[28:31]\n" \
"v_mfma_f32_16x16x32_bf16 v[16:19], a[240:243], v[44:47], v[16:19]\n" \
"ds_read_b128 v[40:43], v3 offset:36864\n" \
"v_mfma_f32_16x16x32_bf16 v[20:23], a[244:247], v[44:47], v[20:23]\n" \
"v_mfma_f32_16x16x32_bf16 v[24:27], a[248:251], v[44:47], v[24:27]\n" \
"s_waitcnt lgkmcnt(1)\n" \
"v_readfirstlane_b32 s56, v13\n" \
"v_mfma_f32_16x16x32_bf16 v[28:31], a[252:255], v[44:47], v[28:31]\n" \
"FM_%=:\n" \
"s_cmp_lg_u32 s56, 0\n" \
"s_cbranch_scc1 FN_%=\n" \
"v_mfma_f32_16x16x32_bf16 v[16:19], v[192:195], v[32:35], v[16:19]\n" \
"ds_read_b128 v[44:47], v3 offset:38912\n" \
"v_mfma_f32_16x16x32_bf16 v[20:23], v[196:199], v[32:35], v[20:23]\n" \
"v_mfma_f32_16x16x32_bf16 v[24:27], v[200:203], v[32:35], v[24:27]\n" \
"v_mfma_f32_16x16x32_bf16 v[28:31], v[204:207], v[32:35], v[28:31]\n" \
"v_mfma_f32_16x16x32_bf16 v[16:19], v[208:211], v[36:39], v[16:19]\n" \
"v_mfma_f32_16x16x32_bf16 v[20:23], v[212:215], v[36:39], v[20:23]\n" \
"v_mfma_f32_16x16x32_bf16 v[24:27], v[216:219], v[36:39], v[24:27]\n" \
"v_mfma_f32_16x16x32_bf16 v[28:31], v[220:223], v[36:39], v[28:31]\n" \
"s_waitcnt lgkmcnt(1)\n" \
"v_mfma_f32_16x16x32_bf16 v[16:19], v[224:227], v[40:43], v[16:19]\n" \
"v_mfma_f32_16x16x32_bf16 v[20:23], v[228:231], v[40:43], v[20:23]\n" \
"v_mfma_f32_16x16x32_bf16 v[24:27], v[232:235], v[40:43], v[24:27]\n" \
"v_mfma_f32_16x16x32_bf16 v[28:31], v[236:239], v[40:43], v[28:31]\n" \
"s_waitcnt lgkmcnt(0)\n" \
"v_mfma_f32_16x16x32_bf16 v[16:19], v[240:243], v[44:47], v[16:19]\n" \
"v_xor_b32 v3, 0x10000, v3\n" \
"v_mfma_f32_16x16x32_bf16 v[20:23], v[244:247], v[44:47], v[20:23]\n" \
"v_mfma_f32_16x16x32_bf16 v[24:27], v[248:251], v[44:47], v[24:27]\n" \
"v_mfma_f32_16x16x32_bf16 v[28:31], v[252:255], v[44:47], v[28:31]\n" \
"s_branch FT_%=\n" \
"FN_%=:\n" \
"v_mfma_f32_16x16x32_bf16 v[16:19], v[192:195], v[32:35], v[16:19]\n" \
"ds_read_b128 v[44:47], v3 offset:38912\n" \
"s_add_u32 m0, s58, 0\n" \
"s_add_u32 s48, s57, 0\n" \
"v_mfma_f32_16x16x32_bf16 v[20:23], v[196:199], v[32:35], v[20:23]\n" \
"buffer_load_dwordx4 v5, s[40:43], s48 offen sc1 lds\n" \
"v_mfma_f32_16x16x32_bf16 v[24:27], v[200:203], v[32:35], v[24:27]\n" \
"s_add_u32 m0, s58, 4096\n" \
"s_add_u32 s48, s57, 128\n" \
"v_mfma_f32_16x16x32_bf16 v[28:31], v[204:207], v[32:35], v[28:31]\n" \
"buffer_load_dwordx4 v5, s[40:43], s48 offen sc1 lds\n" \
"v_mfma_f32_16x16x32_bf16 v[16:19], v[208:211], v[36:39], v[16:19]\n" \
"v_mfma_f32_16x16x32_bf16 v[20:23], v[212:215], v[36:39], v[20:23]\n" \
"s_add_u32 m0, s58, 8192\n" \
"s_add_u32 s48, s57, 256\n" \
"v_mfma_f32_16x16x32_bf16 v[24:27], v[216:219], v[36:39], v[24:27]\n" \
"buffer_load_dwordx4 v5, s[40:43], s48 offen sc1 lds\n" \
"v_mfma_f32_16x16x32_bf16 v[28:31], v[220:223], v[36:39], v[28:31]\n" \
"s_waitcnt lgkmcnt(1)\n" \
"v_mfma_f32_16x16x32_bf16 v[16:19], v[224:227], v[40:43], v[16:19]\n" \
"s_add_u32 m0, s58, 12288\n" \
"s_add_u32 s48, s57, 384\n" \
"v_mfma_f32_16x16x32_bf16 v[20:23], v[228:231], v[40:43], v[20:23]\n" \
"buffer_load_dwordx4 v5, s[40:43], s48 offen sc1 lds\n" \
"v_mfma_f32_16x16x32_bf16 v[24:27], v[232:235], v[40:43], v[24:27]\n" \
"v_mfma_f32_16x16x32_bf16 v[28:31], v[236:239], v[40:43], v[28:31]\n" \
"s_add_u32 m0, s58, 16384\n" \
"s_add_u32 s48, s57, 512\n" \
"s_waitcnt lgkmcnt(0)\n" \
"v_mfma_f32_16x16x32_bf16 v[16:19], v[240:243], v[44:47], v[16:19]\n" \
"buffer_load_dwordx4 v5, s[40:43], s48 offen sc1 lds\n" \
"v_xor_b32 v3, 0x10000, v3\n" \
"v_mfma_f32_16x16x32_bf16 v[20:23], v[244:247], v[44:47], v[20:23]\n" \
"v_mfma_f32_16x16x32_bf16 v[24:27], v[248:251], v[44:47], v[24:27]\n" \
"s_add_u32 m0, s58, 20480\n" \
"s_add_u32 s48, s57, 640\n" \
"v_mfma_f32_16x16x32_bf16 v[28:31], v[252:255], v[44:47], v[28:31]\n" \
"buffer_load_dwordx4 v5, s[40:43], s48 offen sc1 lds\n" \
"FT_%=:\n" \
"s_mov_b32 s53, s57\n" \
"s_mov_b32 s54, s58\n" \
"s_nop 5\n" \
"v_cvt_pk_bf16_f32 v65, v16, v17\n" \
"v_cvt_pk_bf16_f32 v67, v18, v19\n" \
"v_cvt_pk_bf16_f32 v69, v20, v21\n" \
"v_cvt_pk_bf16_f32 v71, v22, v23\n" \
"v_lshlrev_b32 v64, 16, v65\n" \
"v_and_b32 v65, 0xffff0000, v65\n" \
"v_lshlrev_b32 v66, 16, v67\n" \
"v_and_b32 v67, 0xffff0000, v67\n" \
"v_lshlrev_b32 v68, 16, v69\n" \
"v_and_b32 v69, 0xffff0000, v69\n" \
"v_lshlrev_b32 v70, 16, v71\n" \
"v_and_b32 v71, 0xffff0000, v71\n" \
"v_mul_f32 v80, 0xbfb8aa3b, v64\n" \
"v_mul_f32 v81, 0xbfb8aa3b, v65\n" \
"v_mul_f32 v82, 0xbfb8aa3b, v66\n" \
"v_mul_f32 v83, 0xbfb8aa3b, v67\n" \
"v_mul_f32 v84, 0xbfb8aa3b, v68\n" \
"v_mul_f32 v85, 0xbfb8aa3b, v69\n" \
"v_mul_f32 v86, 0xbfb8aa3b, v70\n" \
"v_mul_f32 v87, 0xbfb8aa3b, v71\n" \
"v_exp_f32 v80, v80\n" \
"v_exp_f32 v81, v81\n" \
"v_exp_f32 v82, v82\n" \
"v_exp_f32 v83, v83\n" \
"v_exp_f32 v84, v84\n" \
"v_exp_f32 v85, v85\n" \
"v_exp_f32 v86, v86\n" \
"v_exp_f32 v87, v87\n" \
"v_cvt_pk_bf16_f32 v73, v24, v25\n" \
"v_cvt_pk_bf16_f32 v75, v26, v27\n" \
"v_cvt_pk_bf16_f32 v77, v28, v29\n" \
"v_cvt_pk_bf16_f32 v79, v30, v31\n" \
"v_lshlrev_b32 v72, 16, v73\n" \
"v_and_b32 v73, 0xffff0000, v73\n" \
"v_lshlrev_b32 v74, 16, v75\n" \
"v_and_b32 v75, 0xffff0000, v75\n" \
"v_lshlrev_b32 v76, 16, v77\n" \
"v_and_b32 v77, 0xffff0000, v77\n" \
"v_lshlrev_b32 v78, 16, v79\n" \
"v_and_b32 v79, 0xffff0000, v79\n" \
"v_add_f32 v80, 1.0, v80\n" \
"v_add_f32 v81, 1.0, v81\n" \
"v_add_f32 v82, 1.0, v82\n" \
"v_add_f32 v83, 1.0, v83\n" \
"v_add_f32 v84, 1.0, v84\n" \
"v_add_f32 v85, 1.0, v85\n" \
"v_add_f32 v86, 1.0, v86\n" \
"v_add_f32 v87, 1.0, v87\n" \
"v_rcp_f32 v80, v80\n" \
"v_rcp_f32 v81, v81\n" \
"v_rcp_f32 v82, v82\n" \
"v_rcp_f32 v83, v83\n" \
"v_rcp_f32 v84, v84\n" \
"v_rcp_f32 v85, v85\n" \
"v_rcp_f32 v86, v86\n" \
"v_rcp_f32 v87, v87\n" \
"v_pk_mul_f32 v[80:81], v[80:81], v[64:65]\n" \
"v_pk_mul_f32 v[82:83], v[82:83], v[66:67]\n" \
"v_pk_mul_f32 v[84:85], v[84:85], v[68:69]\n" \
"v_pk_mul_f32 v[86:87], v[86:87], v[70:71]\n" \
"v_cvt_pk_bf16_f32 v65, v80, v81\n" \
"v_cvt_pk_bf16_f32 v67, v82, v83\n" \
"v_cvt_pk_bf16_f32 v69, v84, v85\n" \
"v_cvt_pk_bf16_f32 v71, v86, v87\n" \
"v_lshlrev_b32 v64, 16, v65\n" \
"v_and_b32 v65, 0xffff0000, v65\n" \
"v_lshlrev_b32 v66, 16, v67\n" \
"v_and_b32 v67, 0xffff0000, v67\n" \
"v_lshlrev_b32 v68, 16, v69\n" \
"v_and_b32 v69, 0xffff0000, v69\n" \
"v_lshlrev_b32 v70, 16, v71\n" \
"v_and_b32 v71, 0xffff0000, v71\n" \
"v_pk_mul_f32 v[64:65], v[64:65], v[72:73]\n" \
"v_pk_mul_f32 v[66:67], v[66:67], v[74:75]\n" \
"v_pk_mul_f32 v[68:69], v[68:69], v[76:77]\n" \
"v_pk_mul_f32 v[70:71], v[70:71], v[78:79]\n" \
"v_cvt_pk_bf16_f32 v88, v64, v65\n" \
"v_cvt_pk_bf16_f32 v89, v66, v67\n" \
"v_cvt_pk_bf16_f32 v90, v68, v69\n" \
"v_cvt_pk_bf16_f32 v91, v70, v71\n" \
"buffer_store_dwordx4 v[88:91], v0, s[40:43], s55 offen sc1\n" \
"s_add_u32 s55, s55, 0x200000\n" \
"s_xor_b32 s55, s55, s69\n" \
"s_add_u32 s35, s35, 1\n" \
"s_cmp_lt_u32 s35, 64\n" \
"s_cbranch_scc1 FL_%=\n" \
"s_waitcnt vmcnt(0)\n" \
"s_barrier\n" \
"v_mov_b32 v12, 64\n" \
"s_mov_b64 exec, 15\n" \
"buffer_store_dword v12, v11, s[40:43], s51 offen sc1\n" \
"s_mov_b64 exec, -1\n" \
"s_cmp_lg_u32 s50, 0\n" \
"s_cbranch_scc1 FE_%=\n" \
"v_mov_b32 v0, 1\n" \
"buffer_store_dword v0, off, s[40:43], 0 offset:256 sc1\n" \
"FE_%=:\n" \
"FCZ_%=:\n" \
"s_endpgm\n"
#define F2_ASM_TEXT \
"s_getpc_b64 s[46:47]\n" \
"DCW_%=:\n" \
"v_mbcnt_lo_u32_b32 v60, -1, 0\n" \
"v_mbcnt_hi_u32_b32 v60, -1, v60\n" \
"v_lshlrev_b32 v60, 7, v60\n" \
"v_min_u32 v61, DCZ_%=-DCW_%=, v60\n" \
"global_load_dword v62, v61, s[46:47]\n" \
"v_add_u32 v60, 0x2000, v60\n" \
"v_min_u32 v61, DCZ_%=-DCW_%=, v60\n" \
"global_load_dword v62, v61, s[46:47]\n" \
"v_add_u32 v60, 0x2000, v60\n" \
"s_mov_b64 s[40:41], %[ws]\n" \
"s_add_u32 s40, s40, 0x800000\n" \
"s_addc_u32 s41, s41, 0\n" \
"s_and_b32 s41, s41, 0xffff\n" \
"s_mov_b32 s42, 0x9000000\n" \
"s_mov_b32 s43, 0x00020000\n" \
"s_mov_b32 s33, %[wave]\n" \
"v_mbcnt_lo_u32_b32 v48, -1, 0\n" \
"v_mbcnt_hi_u32_b32 v48, -1, v48\n" \
"s_lshl_b32 s48, s33, 6\n" \
"v_add_lshl_u32 v48, v48, s48, 4\n" \
"ds_read_b128 v[0:3], v48 offset:0\n" \
"ds_read_b128 v[4:7], v48 offset:4096\n" \
"ds_read_b128 v[8:11], v48 offset:8192\n" \
"ds_read_b128 v[12:15], v48 offset:12288\n" \
"ds_read_b128 v[32:35], v48 offset:16384\n" \
"ds_read_b128 v[36:39], v48 offset:20480\n" \
"ds_read_b128 v[40:43], v48 offset:24576\n" \
"s_waitcnt lgkmcnt(0)\n" \
"s_barrier\n" \
"v_readfirstlane_b32 s70, v32\n" \
"v_readfirstlane_b32 s71, v33\n" \
"v_readfirstlane_b32 s72, v34\n" \
"v_readfirstlane_b32 s73, v35\n" \
"v_readfirstlane_b32 s56, v36\n" \
"v_readfirstlane_b32 s53, v37\n" \
"v_readfirstlane_b32 s54, v38\n" \
"v_readfirstlane_b32 s55, v39\n" \
"s_nop 4\n" \
"s_lshl_b32 s34, s33, 10\n" \
"s_cmp_eq_u32 s33, 0\n" \
"s_cselect_b32 s51, s51, 0x7ff00000\n" \
"s_mov_b32 s50, 0x80000\n" \
"s_mov_b32 s35, 0\n" \
"s_mov_b64 s[44:45], s[70:71]\n" \
"s_and_b32 s45, s45, 0xffff\n" \
"s_mov_b32 s46, 0x21c000\n" \
"s_mov_b32 s47, 0x00020000\n" \
"s_mov_b64 s[36:37], s[72:73]\n" \
"s_and_b32 s37, s37, 0xffff\n" \
"s_mov_b32 s38, 0x280000\n" \
"s_mov_b32 s39, 0x00020000\n" \
"s_sleep 66\n" \
"s_add_u32 m0, s34, 0\n" \
"s_mov_b32 s48, 0x0\n" \
"buffer_load_dwordx4 v1, s[44:47], s48 offen lds\n" \
"s_add_u32 m0, s34, 4096\n" \
"s_mov_b32 s48, 0x5000\n" \
"buffer_load_dwordx4 v1, s[44:47], s48 offen lds\n" \
"s_add_u32 m0, s34, 8192\n" \
"s_mov_b32 s48, 0xa000\n" \
"buffer_load_dwordx4 v1, s[44:47], s48 offen lds\n" \
"s_add_u32 m0, s34, 12288\n" \
"s_mov_b32 s48, 0xf000\n" \
"buffer_load_dwordx4 v1, s[44:47], s48 offen lds\n" \
"s_add_u32 m0, s34, 16384\n" \
"s_mov_b32 s48, 0x14000\n" \
"buffer_load_dwordx4 v1, s[44:47], s48 offen lds\n" \
"s_add_u32 m0, s34, 20480\n" \
"s_mov_b32 s48, 0x19000\n" \
"buffer_load_dwordx4 v1, s[44:47], s48 offen lds\n" \
"s_add_u32 m0, s34, 24576\n" \
"s_mov_b32 s48, 0x1e000\n" \
"buffer_load_dwordx4 v1, s[44:47], s48 offen lds\n" \
"s_add_u32 m0, s34, 28672\n" \
"s_mov_b32 s48, 0x23000\n" \
"buffer_load_dwordx4 v1, s[44:47], s48 offen lds\n" \
"s_add_u32 m0, s34, 32768\n" \
"s_mov_b32 s48, 0x28000\n" \
"buffer_load_dwordx4 v1, s[44:47], s48 offen lds\n" \
"s_add_u32 m0, s34, 36864\n" \
"s_mov_b32 s48, 0x2d000\n" \
"buffer_load_dwordx4 v1, s[44:47], s48 offen lds\n" \
"s_add_u32 m0, s34, 40960\n" \
"s_mov_b32 s48, 0x32000\n" \
"buffer_load_dwordx4 v1, s[44:47], s48 offen lds\n" \
"s_add_u32 m0, s34, 45056\n" \
"s_mov_b32 s48, 0x37000\n" \
"buffer_load_dwordx4 v1, s[44:47], s48 offen lds\n" \
"s_add_u32 m0, s34, 49152\n" \
"s_mov_b32 s48, 0x3c000\n" \
"buffer_load_dwordx4 v1, s[44:47], s48 offen lds\n" \
"s_add_u32 m0, s34, 53248\n" \
"s_mov_b32 s48, 0x41000\n" \
"buffer_load_dwordx4 v1, s[44:47], s48 offen lds\n" \
"s_add_u32 m0, s34, 57344\n" \
"s_mov_b32 s48, 0x46000\n" \
"buffer_load_dwordx4 v1, s[44:47], s48 offen lds\n" \
"s_add_u32 m0, s34, 61440\n" \
"s_mov_b32 s48, 0x4b000\n" \
"buffer_load_dwordx4 v1, s[44:47], s48 offen lds\n" \
"s_add_u32 m0, s34, 65536\n" \
"s_mov_b32 s48, 0x50000\n" \
"buffer_load_dwordx4 v1, s[44:47], s48 offen lds\n" \
"s_add_u32 m0, s34, 69632\n" \
"s_mov_b32 s48, 0x55000\n" \
"buffer_load_dwordx4 v1, s[44:47], s48 offen lds\n" \
"s_add_u32 m0, s34, 73728\n" \
"s_mov_b32 s48, 0x5a000\n" \
"buffer_load_dwordx4 v1, s[44:47], s48 offen lds\n" \
"s_add_u32 m0, s34, 77824\n" \
"s_mov_b32 s48, 0x5f000\n" \
"buffer_load_dwordx4 v1, s[44:47], s48 offen lds\n" \
"s_add_u32 m0, s34, 81920\n" \
"s_mov_b32 s48, 0x64000\n" \
"buffer_load_dwordx4 v1, s[44:47], s48 offen lds\n" \
"s_add_u32 m0, s34, 86016\n" \
"s_mov_b32 s48, 0x69000\n" \
"buffer_load_dwordx4 v1, s[44:47], s48 offen lds\n" \
"s_add_u32 m0, s34, 90112\n" \
"s_mov_b32 s48, 0x6e000\n" \
"buffer_load_dwordx4 v1, s[44:47], s48 offen lds\n" \
"s_add_u32 m0, s34, 94208\n" \
"s_mov_b32 s48, 0x73000\n" \
"buffer_load_dwordx4 v1, s[44:47], s48 offen lds\n" \
"s_add_u32 m0, s34, 98304\n" \
"s_mov_b32 s48, 0x78000\n" \
"buffer_load_dwordx4 v1, s[44:47], s48 offen lds\n" \
"s_add_u32 m0, s34, 102400\n" \
"s_mov_b32 s48, 0x7d000\n" \
"buffer_load_dwordx4 v1, s[44:47], s48 offen lds\n" \
"s_add_u32 m0, s34, 106496\n" \
"s_mov_b32 s48, 0x82000\n" \
"buffer_load_dwordx4 v1, s[44:47], s48 offen lds\n" \
"s_add_u32 m0, s34, 110592\n" \
"s_mov_b32 s48, 0x87000\n" \
"buffer_load_dwordx4 v1, s[44:47], s48 offen lds\n" \
"s_add_u32 m0, s34, 114688\n" \
"s_mov_b32 s48, 0x8c000\n" \
"buffer_load_dwordx4 v1, s[44:47], s48 offen lds\n" \
"s_add_u32 m0, s34, 118784\n" \
"s_mov_b32 s48, 0x91000\n" \
"buffer_load_dwordx4 v1, s[44:47], s48 offen lds\n" \
"s_add_u32 m0, s34, 122880\n" \
"s_mov_b32 s48, 0x96000\n" \
"buffer_load_dwordx4 v1, s[44:47], s48 offen lds\n" \
"s_add_u32 m0, s34, 126976\n" \
"s_mov_b32 s48, 0x9b000\n" \
"buffer_load_dwordx4 v1, s[44:47], s48 offen lds\n" \
"s_add_u32 m0, s34, 131072\n" \
"s_mov_b32 s48, 0xa0000\n" \
"buffer_load_dwordx4 v1, s[44:47], s48 offen lds\n" \
"s_add_u32 m0, s34, 135168\n" \
"s_mov_b32 s48, 0xa5000\n" \
"buffer_load_dwordx4 v1, s[44:47], s48 offen lds\n" \
"s_add_u32 m0, s34, 139264\n" \
"s_mov_b32 s48, 0xaa000\n" \
"buffer_load_dwordx4 v1, s[44:47], s48 offen lds\n" \
"s_add_u32 m0, s34, 143360\n" \
"s_mov_b32 s48, 0xaf000\n" \
"buffer_load_dwordx4 v1, s[44:47], s48 offen lds\n" \
"s_add_u32 m0, s34, 147456\n" \
"s_mov_b32 s48, 0xb4000\n" \
"buffer_load_dwordx4 v1, s[44:47], s48 offen lds\n" \
"s_add_u32 m0, s34, 151552\n" \
"s_mov_b32 s48, 0xb9000\n" \
"buffer_load_dwordx4 v1, s[44:47], s48 offen lds\n" \
"s_add_u32 m0, s34, 155648\n" \
"s_mov_b32 s48, 0xbe000\n" \
"buffer_load_dwordx4 v1, s[44:47], s48 offen lds\n" \
"s_add_u32 m0, s34, 159744\n" \
"s_mov_b32 s48, 0xc3000\n" \
"buffer_load_dwordx4 v1, s[44:47], s48 offen lds\n" \
"s_waitcnt vmcnt(32)\n" \
"s_barrier\n" \
"v_add_u32 v20, 0x0, v4\n" \
"ds_read_b64_tr_b16 v[252:253], v20 offset:0\n" \
"ds_read_b64_tr_b16 v[254:255], v20 offset:4096\n" \
"ds_read_b64_tr_b16 v[248:249], v20 offset:8\n" \
"ds_read_b64_tr_b16 v[250:251], v20 offset:4104\n" \
"v_add_u32 v20, 0x0, v5\n" \
"ds_read_b64_tr_b16 v[244:245], v20 offset:0\n" \
"ds_read_b64_tr_b16 v[246:247], v20 offset:4096\n" \
"ds_read_b64_tr_b16 v[240:241], v20 offset:8\n" \
"ds_read_b64_tr_b16 v[242:243], v20 offset:4104\n" \
"v_add_u32 v20, 0x0, v6\n" \
"ds_read_b64_tr_b16 v[236:237], v20 offset:0\n" \
"ds_read_b64_tr_b16 v[238:239], v20 offset:4096\n" \
"ds_read_b64_tr_b16 v[232:233], v20 offset:8\n" \
"ds_read_b64_tr_b16 v[234:235], v20 offset:4104\n" \
"v_add_u32 v20, 0x0, v7\n" \
"ds_read_b64_tr_b16 v[228:229], v20 offset:0\n" \
"ds_read_b64_tr_b16 v[230:231], v20 offset:4096\n" \
"ds_read_b64_tr_b16 v[224:225], v20 offset:8\n" \
"ds_read_b64_tr_b16 v[226:227], v20 offset:4104\n" \
"s_waitcnt lgkmcnt(0)\n" \
"s_barrier\n" \
"s_add_u32 m0, s34, 0\n" \
"s_mov_b32 s48, 0xc8000\n" \
"buffer_load_dwordx4 v1, s[44:47], s48 offen lds\n" \
"s_add_u32 m0, s34, 4096\n" \
"s_mov_b32 s48, 0xcd000\n" \
"buffer_load_dwordx4 v1, s[44:47], s48 offen lds\n" \
"s_add_u32 m0, s34, 8192\n" \
"s_mov_b32 s48, 0xd2000\n" \
"buffer_load_dwordx4 v1, s[44:47], s48 offen lds\n" \
"s_add_u32 m0, s34, 12288\n" \
"s_mov_b32 s48, 0xd7000\n" \
"buffer_load_dwordx4 v1, s[44:47], s48 offen lds\n" \
"s_add_u32 m0, s34, 16384\n" \
"s_mov_b32 s48, 0xdc000\n" \
"buffer_load_dwordx4 v1, s[44:47], s48 offen lds\n" \
"s_add_u32 m0, s34, 20480\n" \
"s_mov_b32 s48, 0xe1000\n" \
"buffer_load_dwordx4 v1, s[44:47], s48 offen lds\n" \
"s_add_u32 m0, s34, 24576\n" \
"s_mov_b32 s48, 0xe6000\n" \
"buffer_load_dwordx4 v1, s[44:47], s48 offen lds\n" \
"s_add_u32 m0, s34, 28672\n" \
"s_mov_b32 s48, 0xeb000\n" \
"buffer_load_dwordx4 v1, s[44:47], s48 offen lds\n" \
"s_waitcnt vmcnt(32)\n" \
"s_barrier\n" \
"v_add_u32 v20, 0x8000, v4\n" \
"ds_read_b64_tr_b16 v[24:25], v20 offset:0\n" \
"ds_read_b64_tr_b16 v[26:27], v20 offset:4096\n" \
"ds_read_b64_tr_b16 v[28:29], v20 offset:8\n" \
"ds_read_b64_tr_b16 v[30:31], v20 offset:4104\n" \
"v_add_u32 v20, 0x8000, v5\n" \
"ds_read_b64_tr_b16 v[220:221], v20 offset:0\n" \
"ds_read_b64_tr_b16 v[222:223], v20 offset:4096\n" \
"ds_read_b64_tr_b16 v[216:217], v20 offset:8\n" \
"ds_read_b64_tr_b16 v[218:219], v20 offset:4104\n" \
"v_add_u32 v20, 0x8000, v6\n" \
"ds_read_b64_tr_b16 v[212:213], v20 offset:0\n" \
"ds_read_b64_tr_b16 v[214:215], v20 offset:4096\n" \
"ds_read_b64_tr_b16 v[208:209], v20 offset:8\n" \
"ds_read_b64_tr_b16 v[210:211], v20 offset:4104\n" \
"v_add_u32 v20, 0x8000, v7\n" \
"ds_read_b64_tr_b16 v[204:205], v20 offset:0\n" \
"ds_read_b64_tr_b16 v[206:207], v20 offset:4096\n" \
"ds_read_b64_tr_b16 v[200:201], v20 offset:8\n" \
"ds_read_b64_tr_b16 v[202:203], v20 offset:4104\n" \
"s_waitcnt lgkmcnt(0)\n" \
"s_barrier\n" \
"s_add_u32 m0, s34, 32768\n" \
"s_mov_b32 s48, 0xf0000\n" \
"buffer_load_dwordx4 v1, s[44:47], s48 offen lds\n" \
"s_add_u32 m0, s34, 36864\n" \
"s_mov_b32 s48, 0xf5000\n" \
"buffer_load_dwordx4 v1, s[44:47], s48 offen lds\n" \
"s_add_u32 m0, s34, 40960\n" \
"s_mov_b32 s48, 0xfa000\n" \
"buffer_load_dwordx4 v1, s[44:47], s48 offen lds\n" \
"s_add_u32 m0, s34, 45056\n" \
"s_mov_b32 s48, 0xff000\n" \
"buffer_load_dwordx4 v1, s[44:47], s48 offen lds\n" \
"s_add_u32 m0, s34, 49152\n" \
"s_mov_b32 s48, 0x104000\n" \
"buffer_load_dwordx4 v1, s[44:47], s48 offen lds\n" \
"s_add_u32 m0, s34, 53248\n" \
"s_mov_b32 s48, 0x109000\n" \
"buffer_load_dwordx4 v1, s[44:47], s48 offen lds\n" \
"s_add_u32 m0, s34, 57344\n" \
"s_mov_b32 s48, 0x10e000\n" \
"buffer_load_dwordx4 v1, s[44:47], s48 offen lds\n" \
"s_add_u32 m0, s34, 61440\n" \
"s_mov_b32 s48, 0x113000\n" \
"buffer_load_dwordx4 v1, s[44:47], s48 offen lds\n" \
"s_waitcnt vmcnt(32)\n" \
"s_barrier\n" \
"v_add_u32 v20, 0x10000, v4\n" \
"ds_read_b64_tr_b16 v[32:33], v20 offset:0\n" \
"ds_read_b64_tr_b16 v[34:35], v20 offset:4096\n" \
"ds_read_b64_tr_b16 v[36:37], v20 offset:8\n" \
"ds_read_b64_tr_b16 v[38:39], v20 offset:4104\n" \
"v_add_u32 v20, 0x10000, v5\n" \
"ds_read_b64_tr_b16 v[196:197], v20 offset:0\n" \
"ds_read_b64_tr_b16 v[198:199], v20 offset:4096\n" \
"ds_read_b64_tr_b16 v[192:193], v20 offset:8\n" \
"ds_read_b64_tr_b16 v[194:195], v20 offset:4104\n" \
"v_add_u32 v20, 0x10000, v6\n" \
"ds_read_b64_tr_b16 v[188:189], v20 offset:0\n" \
"ds_read_b64_tr_b16 v[190:191], v20 offset:4096\n" \
"ds_read_b64_tr_b16 v[184:185], v20 offset:8\n" \
"ds_read_b64_tr_b16 v[186:187], v20 offset:4104\n" \
"v_add_u32 v20, 0x10000, v7\n" \
"ds_read_b64_tr_b16 v[180:181], v20 offset:0\n" \
"ds_read_b64_tr_b16 v[182:183], v20 offset:4096\n" \
"ds_read_b64_tr_b16 v[176:177], v20 offset:8\n" \
"ds_read_b64_tr_b16 v[178:179], v20 offset:4104\n" \
"s_waitcnt lgkmcnt(0)\n" \
"s_barrier\n" \
"s_add_u32 m0, s34, 65536\n" \
"s_mov_b32 s48, 0x118000\n" \
"buffer_load_dwordx4 v1, s[44:47], s48 offen lds\n" \
"s_add_u32 m0, s34, 69632\n" \
"s_mov_b32 s48, 0x11d000\n" \
"buffer_load_dwordx4 v1, s[44:47], s48 offen lds\n" \
"s_add_u32 m0, s34, 73728\n" \
"s_mov_b32 s48, 0x122000\n" \
"buffer_load_dwordx4 v1, s[44:47], s48 offen lds\n" \
"s_add_u32 m0, s34, 77824\n" \
"s_mov_b32 s48, 0x127000\n" \
"buffer_load_dwordx4 v1, s[44:47], s48 offen lds\n" \
"s_add_u32 m0, s34, 81920\n" \
"s_mov_b32 s48, 0x12c000\n" \
"buffer_load_dwordx4 v1, s[44:47], s48 offen lds\n" \
"s_add_u32 m0, s34, 86016\n" \
"s_mov_b32 s48, 0x131000\n" \
"buffer_load_dwordx4 v1, s[44:47], s48 offen lds\n" \
"s_add_u32 m0, s34, 90112\n" \
"s_mov_b32 s48, 0x136000\n" \
"buffer_load_dwordx4 v1, s[44:47], s48 offen lds\n" \
"s_add_u32 m0, s34, 94208\n" \
"s_mov_b32 s48, 0x13b000\n" \
"buffer_load_dwordx4 v1, s[44:47], s48 offen lds\n" \
"s_waitcnt vmcnt(32)\n" \
"s_barrier\n" \
"v_add_u32 v20, 0x18000, v4\n" \
"ds_read_b64_tr_b16 v[40:41], v20 offset:0\n" \
"ds_read_b64_tr_b16 v[42:43], v20 offset:4096\n" \
"ds_read_b64_tr_b16 v[44:45], v20 offset:8\n" \
"ds_read_b64_tr_b16 v[46:47], v20 offset:4104\n" \
"v_add_u32 v20, 0x18000, v5\n" \
"ds_read_b64_tr_b16 v[172:173], v20 offset:0\n" \
"ds_read_b64_tr_b16 v[174:175], v20 offset:4096\n" \
"ds_read_b64_tr_b16 v[168:169], v20 offset:8\n" \
"ds_read_b64_tr_b16 v[170:171], v20 offset:4104\n" \
"v_add_u32 v20, 0x18000, v6\n" \
"ds_read_b64_tr_b16 v[164:165], v20 offset:0\n" \
"ds_read_b64_tr_b16 v[166:167], v20 offset:4096\n" \
"ds_read_b64_tr_b16 v[160:161], v20 offset:8\n" \
"ds_read_b64_tr_b16 v[162:163], v20 offset:4104\n" \
"v_add_u32 v20, 0x18000, v7\n" \
"ds_read_b64_tr_b16 v[156:157], v20 offset:0\n" \
"ds_read_b64_tr_b16 v[158:159], v20 offset:4096\n" \
"ds_read_b64_tr_b16 v[152:153], v20 offset:8\n" \
"ds_read_b64_tr_b16 v[154:155], v20 offset:4104\n" \
"s_waitcnt lgkmcnt(0)\n" \
"s_barrier\n" \
"s_add_u32 m0, s34, 98304\n" \
"s_mov_b32 s48, 0x140000\n" \
"buffer_load_dwordx4 v1, s[44:47], s48 offen lds\n" \
"s_add_u32 m0, s34, 102400\n" \
"s_mov_b32 s48, 0x145000\n" \
"buffer_load_dwordx4 v1, s[44:47], s48 offen lds\n" \
"s_add_u32 m0, s34, 106496\n" \
"s_mov_b32 s48, 0x14a000\n" \
"buffer_load_dwordx4 v1, s[44:47], s48 offen lds\n" \
"s_add_u32 m0, s34, 110592\n" \
"s_mov_b32 s48, 0x14f000\n" \
"buffer_load_dwordx4 v1, s[44:47], s48 offen lds\n" \
"s_add_u32 m0, s34, 114688\n" \
"s_mov_b32 s48, 0x154000\n" \
"buffer_load_dwordx4 v1, s[44:47], s48 offen lds\n" \
"s_add_u32 m0, s34, 118784\n" \
"s_mov_b32 s48, 0x159000\n" \
"buffer_load_dwordx4 v1, s[44:47], s48 offen lds\n" \
"s_add_u32 m0, s34, 122880\n" \
"s_mov_b32 s48, 0x15e000\n" \
"buffer_load_dwordx4 v1, s[44:47], s48 offen lds\n" \
"s_add_u32 m0, s34, 126976\n" \
"s_mov_b32 s48, 0x163000\n" \
"buffer_load_dwordx4 v1, s[44:47], s48 offen lds\n" \
"s_waitcnt vmcnt(32)\n" \
"s_barrier\n" \
"v_add_u32 v20, 0x20000, v4\n" \
"ds_read_b64_tr_b16 v[48:49], v20 offset:0\n" \
"ds_read_b64_tr_b16 v[50:51], v20 offset:4096\n" \
"ds_read_b64_tr_b16 v[52:53], v20 offset:8\n" \
"ds_read_b64_tr_b16 v[54:55], v20 offset:4104\n" \
"v_add_u32 v20, 0x20000, v5\n" \
"ds_read_b64_tr_b16 v[148:149], v20 offset:0\n" \
"ds_read_b64_tr_b16 v[150:151], v20 offset:4096\n" \
"ds_read_b64_tr_b16 v[144:145], v20 offset:8\n" \
"ds_read_b64_tr_b16 v[146:147], v20 offset:4104\n" \
"v_add_u32 v20, 0x20000, v6\n" \
"ds_read_b64_tr_b16 v[140:141], v20 offset:0\n" \
"ds_read_b64_tr_b16 v[142:143], v20 offset:4096\n" \
"ds_read_b64_tr_b16 v[136:137], v20 offset:8\n" \
"ds_read_b64_tr_b16 v[138:139], v20 offset:4104\n" \
"v_add_u32 v20, 0x20000, v7\n" \
"ds_read_b64_tr_b16 v[132:133], v20 offset:0\n" \
"ds_read_b64_tr_b16 v[134:135], v20 offset:4096\n" \
"ds_read_b64_tr_b16 v[128:129], v20 offset:8\n" \
"ds_read_b64_tr_b16 v[130:131], v20 offset:4104\n" \
"s_waitcnt lgkmcnt(0)\n" \
"s_barrier\n" \
"s_add_u32 m0, s34, 131072\n" \
"s_mov_b32 s48, 0x168000\n" \
"buffer_load_dwordx4 v1, s[44:47], s48 offen lds\n" \
"s_add_u32 m0, s34, 135168\n" \
"s_mov_b32 s48, 0x16d000\n" \
"buffer_load_dwordx4 v1, s[44:47], s48 offen lds\n" \
"s_add_u32 m0, s34, 139264\n" \
"s_mov_b32 s48, 0x172000\n" \
"buffer_load_dwordx4 v1, s[44:47], s48 offen lds\n" \
"s_add_u32 m0, s34, 143360\n" \
"s_mov_b32 s48, 0x177000\n" \
"buffer_load_dwordx4 v1, s[44:47], s48 offen lds\n" \
"s_add_u32 m0, s34, 147456\n" \
"s_mov_b32 s48, 0x17c000\n" \
"buffer_load_dwordx4 v1, s[44:47], s48 offen lds\n" \
"s_add_u32 m0, s34, 151552\n" \
"s_mov_b32 s48, 0x181000\n" \
"buffer_load_dwordx4 v1, s[44:47], s48 offen lds\n" \
"s_add_u32 m0, s34, 155648\n" \
"s_mov_b32 s48, 0x186000\n" \
"buffer_load_dwordx4 v1, s[44:47], s48 offen lds\n" \
"s_add_u32 m0, s34, 159744\n" \
"s_mov_b32 s48, 0x18b000\n" \
"buffer_load_dwordx4 v1, s[44:47], s48 offen lds\n" \
"s_waitcnt vmcnt(32)\n" \
"s_barrier\n" \
"v_add_u32 v20, 0x0, v4\n" \
"ds_read_b64_tr_b16 v[56:57], v20 offset:0\n" \
"ds_read_b64_tr_b16 v[58:59], v20 offset:4096\n" \
"ds_read_b64_tr_b16 v[60:61], v20 offset:8\n" \
"ds_read_b64_tr_b16 v[62:63], v20 offset:4104\n" \
"v_add_u32 v20, 0x0, v5\n" \
"ds_read_b64_tr_b16 a[252:253], v20 offset:0\n" \
"ds_read_b64_tr_b16 a[254:255], v20 offset:4096\n" \
"ds_read_b64_tr_b16 a[248:249], v20 offset:8\n" \
"ds_read_b64_tr_b16 a[250:251], v20 offset:4104\n" \
"v_add_u32 v20, 0x0, v6\n" \
"ds_read_b64_tr_b16 a[244:245], v20 offset:0\n" \
"ds_read_b64_tr_b16 a[246:247], v20 offset:4096\n" \
"ds_read_b64_tr_b16 a[240:241], v20 offset:8\n" \
"ds_read_b64_tr_b16 a[242:243], v20 offset:4104\n" \
"v_add_u32 v20, 0x0, v7\n" \
"ds_read_b64_tr_b16 a[236:237], v20 offset:0\n" \
"ds_read_b64_tr_b16 a[238:239], v20 offset:4096\n" \
"ds_read_b64_tr_b16 a[232:233], v20 offset:8\n" \
"ds_read_b64_tr_b16 a[234:235], v20 offset:4104\n" \
"s_waitcnt lgkmcnt(0)\n" \
"s_barrier\n" \
"s_add_u32 m0, s34, 0\n" \
"s_mov_b32 s48, 0x190000\n" \
"buffer_load_dwordx4 v1, s[44:47], s48 offen lds\n" \
"s_add_u32 m0, s34, 4096\n" \
"s_mov_b32 s48, 0x195000\n" \
"buffer_load_dwordx4 v1, s[44:47], s48 offen lds\n" \
"s_add_u32 m0, s34, 8192\n" \
"s_mov_b32 s48, 0x19a000\n" \
"buffer_load_dwordx4 v1, s[44:47], s48 offen lds\n" \
"s_add_u32 m0, s34, 12288\n" \
"s_mov_b32 s48, 0x19f000\n" \
"buffer_load_dwordx4 v1, s[44:47], s48 offen lds\n" \
"s_add_u32 m0, s34, 16384\n" \
"s_mov_b32 s48, 0x1a4000\n" \
"buffer_load_dwordx4 v1, s[44:47], s48 offen lds\n" \
"s_add_u32 m0, s34, 20480\n" \
"s_mov_b32 s48, 0x1a9000\n" \
"buffer_load_dwordx4 v1, s[44:47], s48 offen lds\n" \
"s_add_u32 m0, s34, 24576\n" \
"s_mov_b32 s48, 0x1ae000\n" \
"buffer_load_dwordx4 v1, s[44:47], s48 offen lds\n" \
"s_add_u32 m0, s34, 28672\n" \
"s_mov_b32 s48, 0x1b3000\n" \
"buffer_load_dwordx4 v1, s[44:47], s48 offen lds\n" \
"s_waitcnt vmcnt(32)\n" \
"s_barrier\n" \
"v_add_u32 v20, 0x8000, v4\n" \
"ds_read_b64_tr_b16 v[64:65], v20 offset:0\n" \
"ds_read_b64_tr_b16 v[66:67], v20 offset:4096\n" \
"ds_read_b64_tr_b16 v[68:69], v20 offset:8\n" \
"ds_read_b64_tr_b16 v[70:71], v20 offset:4104\n" \
"v_add_u32 v20, 0x8000, v5\n" \
"ds_read_b64_tr_b16 a[228:229], v20 offset:0\n" \
"ds_read_b64_tr_b16 a[230:231], v20 offset:4096\n" \
"ds_read_b64_tr_b16 a[224:225], v20 offset:8\n" \
"ds_read_b64_tr_b16 a[226:227], v20 offset:4104\n" \
"v_add_u32 v20, 0x8000, v6\n" \
"ds_read_b64_tr_b16 a[220:221], v20 offset:0\n" \
"ds_read_b64_tr_b16 a[222:223], v20 offset:4096\n" \
"ds_read_b64_tr_b16 a[216:217], v20 offset:8\n" \
"ds_read_b64_tr_b16 a[218:219], v20 offset:4104\n" \
"v_add_u32 v20, 0x8000, v7\n" \
"ds_read_b64_tr_b16 a[212:213], v20 offset:0\n" \
"ds_read_b64_tr_b16 a[214:215], v20 offset:4096\n" \
"ds_read_b64_tr_b16 a[208:209], v20 offset:8\n" \
"ds_read_b64_tr_b16 a[210:211], v20 offset:4104\n" \
"s_waitcnt lgkmcnt(0)\n" \
"s_barrier\n" \
"s_add_u32 m0, s34, 32768\n" \
"s_mov_b32 s48, 0x1b8000\n" \
"buffer_load_dwordx4 v1, s[44:47], s48 offen lds\n" \
"s_add_u32 m0, s34, 36864\n" \
"s_mov_b32 s48, 0x1bd000\n" \
"buffer_load_dwordx4 v1, s[44:47], s48 offen lds\n" \
"s_add_u32 m0, s34, 40960\n" \
"s_mov_b32 s48, 0x1c2000\n" \
"buffer_load_dwordx4 v1, s[44:47], s48 offen lds\n" \
"s_add_u32 m0, s34, 45056\n" \
"s_mov_b32 s48, 0x1c7000\n" \
"buffer_load_dwordx4 v1, s[44:47], s48 offen lds\n" \
"s_add_u32 m0, s34, 49152\n" \
"s_mov_b32 s48, 0x1cc000\n" \
"buffer_load_dwordx4 v1, s[44:47], s48 offen lds\n" \
"s_add_u32 m0, s34, 53248\n" \
"s_mov_b32 s48, 0x1d1000\n" \
"buffer_load_dwordx4 v1, s[44:47], s48 offen lds\n" \
"s_add_u32 m0, s34, 57344\n" \
"s_mov_b32 s48, 0x1d6000\n" \
"buffer_load_dwordx4 v1, s[44:47], s48 offen lds\n" \
"s_add_u32 m0, s34, 61440\n" \
"s_mov_b32 s48, 0x1db000\n" \
"buffer_load_dwordx4 v1, s[44:47], s48 offen lds\n" \
"s_waitcnt vmcnt(32)\n" \
"s_barrier\n" \
"v_add_u32 v20, 0x10000, v4\n" \
"ds_read_b64_tr_b16 v[72:73], v20 offset:0\n" \
"ds_read_b64_tr_b16 v[74:75], v20 offset:4096\n" \
"ds_read_b64_tr_b16 v[76:77], v20 offset:8\n" \
"ds_read_b64_tr_b16 v[78:79], v20 offset:4104\n" \
"v_add_u32 v20, 0x10000, v5\n" \
"ds_read_b64_tr_b16 a[204:205], v20 offset:0\n" \
"ds_read_b64_tr_b16 a[206:207], v20 offset:4096\n" \
"ds_read_b64_tr_b16 a[200:201], v20 offset:8\n" \
"ds_read_b64_tr_b16 a[202:203], v20 offset:4104\n" \
"v_add_u32 v20, 0x10000, v6\n" \
"ds_read_b64_tr_b16 a[196:197], v20 offset:0\n" \
"ds_read_b64_tr_b16 a[198:199], v20 offset:4096\n" \
"ds_read_b64_tr_b16 a[192:193], v20 offset:8\n" \
"ds_read_b64_tr_b16 a[194:195], v20 offset:4104\n" \
"v_add_u32 v20, 0x10000, v7\n" \
"ds_read_b64_tr_b16 a[188:189], v20 offset:0\n" \
"ds_read_b64_tr_b16 a[190:191], v20 offset:4096\n" \
"ds_read_b64_tr_b16 a[184:185], v20 offset:8\n" \
"ds_read_b64_tr_b16 a[186:187], v20 offset:4104\n" \
"s_waitcnt lgkmcnt(0)\n" \
"s_barrier\n" \
"s_add_u32 m0, s34, 65536\n" \
"s_mov_b32 s48, 0x1e0000\n" \
"buffer_load_dwordx4 v1, s[44:47], s48 offen lds\n" \
"s_add_u32 m0, s34, 69632\n" \
"s_mov_b32 s48, 0x1e5000\n" \
"buffer_load_dwordx4 v1, s[44:47], s48 offen lds\n" \
"s_add_u32 m0, s34, 73728\n" \
"s_mov_b32 s48, 0x1ea000\n" \
"buffer_load_dwordx4 v1, s[44:47], s48 offen lds\n" \
"s_add_u32 m0, s34, 77824\n" \
"s_mov_b32 s48, 0x1ef000\n" \
"buffer_load_dwordx4 v1, s[44:47], s48 offen lds\n" \
"s_add_u32 m0, s34, 81920\n" \
"s_mov_b32 s48, 0x1f4000\n" \
"buffer_load_dwordx4 v1, s[44:47], s48 offen lds\n" \
"s_add_u32 m0, s34, 86016\n" \
"s_mov_b32 s48, 0x1f9000\n" \
"buffer_load_dwordx4 v1, s[44:47], s48 offen lds\n" \
"s_add_u32 m0, s34, 90112\n" \
"s_mov_b32 s48, 0x1fe000\n" \
"buffer_load_dwordx4 v1, s[44:47], s48 offen lds\n" \
"s_add_u32 m0, s34, 94208\n" \
"s_mov_b32 s48, 0x203000\n" \
"buffer_load_dwordx4 v1, s[44:47], s48 offen lds\n" \
"s_waitcnt vmcnt(32)\n" \
"s_barrier\n" \
"v_add_u32 v20, 0x18000, v4\n" \
"ds_read_b64_tr_b16 v[80:81], v20 offset:0\n" \
"ds_read_b64_tr_b16 v[82:83], v20 offset:4096\n" \
"ds_read_b64_tr_b16 v[84:85], v20 offset:8\n" \
"ds_read_b64_tr_b16 v[86:87], v20 offset:4104\n" \
"v_add_u32 v20, 0x18000, v5\n" \
"ds_read_b64_tr_b16 a[180:181], v20 offset:0\n" \
"ds_read_b64_tr_b16 a[182:183], v20 offset:4096\n" \
"ds_read_b64_tr_b16 a[176:177], v20 offset:8\n" \
"ds_read_b64_tr_b16 a[178:179], v20 offset:4104\n" \
"v_add_u32 v20, 0x18000, v6\n" \
"ds_read_b64_tr_b16 a[172:173], v20 offset:0\n" \
"ds_read_b64_tr_b16 a[174:175], v20 offset:4096\n" \
"ds_read_b64_tr_b16 a[168:169], v20 offset:8\n" \
"ds_read_b64_tr_b16 a[170:171], v20 offset:4104\n" \
"v_add_u32 v20, 0x18000, v7\n" \
"ds_read_b64_tr_b16 a[164:165], v20 offset:0\n" \
"ds_read_b64_tr_b16 a[166:167], v20 offset:4096\n" \
"ds_read_b64_tr_b16 a[160:161], v20 offset:8\n" \
"ds_read_b64_tr_b16 a[162:163], v20 offset:4104\n" \
"s_waitcnt lgkmcnt(0)\n" \
"s_barrier\n" \
"s_add_u32 m0, s34, 98304\n" \
"s_mov_b32 s48, 0x208000\n" \
"buffer_load_dwordx4 v1, s[44:47], s48 offen lds\n" \
"s_add_u32 m0, s34, 102400\n" \
"s_mov_b32 s48, 0x20d000\n" \
"buffer_load_dwordx4 v1, s[44:47], s48 offen lds\n" \
"s_add_u32 m0, s34, 106496\n" \
"s_mov_b32 s48, 0x212000\n" \
"buffer_load_dwordx4 v1, s[44:47], s48 offen lds\n" \
"s_add_u32 m0, s34, 110592\n" \
"s_mov_b32 s48, 0x217000\n" \
"buffer_load_dwordx4 v1, s[44:47], s48 offen lds\n" \
"s_waitcnt vmcnt(28)\n" \
"s_barrier\n" \
"v_add_u32 v20, 0x20000, v4\n" \
"ds_read_b64_tr_b16 a[156:157], v20 offset:0\n" \
"ds_read_b64_tr_b16 a[158:159], v20 offset:4096\n" \
"ds_read_b64_tr_b16 a[152:153], v20 offset:8\n" \
"ds_read_b64_tr_b16 a[154:155], v20 offset:4104\n" \
"v_add_u32 v20, 0x20000, v5\n" \
"ds_read_b64_tr_b16 a[148:149], v20 offset:0\n" \
"ds_read_b64_tr_b16 a[150:151], v20 offset:4096\n" \
"ds_read_b64_tr_b16 a[144:145], v20 offset:8\n" \
"ds_read_b64_tr_b16 a[146:147], v20 offset:4104\n" \
"v_add_u32 v20, 0x20000, v6\n" \
"ds_read_b64_tr_b16 a[140:141], v20 offset:0\n" \
"ds_read_b64_tr_b16 a[142:143], v20 offset:4096\n" \
"ds_read_b64_tr_b16 a[136:137], v20 offset:8\n" \
"ds_read_b64_tr_b16 a[138:139], v20 offset:4104\n" \
"v_add_u32 v20, 0x20000, v7\n" \
"ds_read_b64_tr_b16 a[132:133], v20 offset:0\n" \
"ds_read_b64_tr_b16 a[134:135], v20 offset:4096\n" \
"ds_read_b64_tr_b16 a[128:129], v20 offset:8\n" \
"ds_read_b64_tr_b16 a[130:131], v20 offset:4104\n" \
"s_waitcnt lgkmcnt(0)\n" \
"s_barrier\n" \
"s_waitcnt vmcnt(20)\n" \
"s_barrier\n" \
"v_add_u32 v20, 0x0, v4\n" \
"ds_read_b64_tr_b16 a[124:125], v20 offset:0\n" \
"ds_read_b64_tr_b16 a[126:127], v20 offset:4096\n" \
"ds_read_b64_tr_b16 a[120:121], v20 offset:8\n" \
"ds_read_b64_tr_b16 a[122:123], v20 offset:4104\n" \
"v_add_u32 v20, 0x0, v5\n" \
"ds_read_b64_tr_b16 a[116:117], v20 offset:0\n" \
"ds_read_b64_tr_b16 a[118:119], v20 offset:4096\n" \
"ds_read_b64_tr_b16 a[112:113], v20 offset:8\n" \
"ds_read_b64_tr_b16 a[114:115], v20 offset:4104\n" \
"v_add_u32 v20, 0x0, v6\n" \
"ds_read_b64_tr_b16 a[108:109], v20 offset:0\n" \
"ds_read_b64_tr_b16 a[110:111], v20 offset:4096\n" \
"ds_read_b64_tr_b16 a[104:105], v20 offset:8\n" \
"ds_read_b64_tr_b16 a[106:107], v20 offset:4104\n" \
"v_add_u32 v20, 0x0, v7\n" \
"ds_read_b64_tr_b16 a[100:101], v20 offset:0\n" \
"ds_read_b64_tr_b16 a[102:103], v20 offset:4096\n" \
"ds_read_b64_tr_b16 a[96:97], v20 offset:8\n" \
"ds_read_b64_tr_b16 a[98:99], v20 offset:4104\n" \
"s_waitcnt lgkmcnt(0)\n" \
"s_barrier\n" \
"s_waitcnt vmcnt(12)\n" \
"s_barrier\n" \
"v_add_u32 v20, 0x8000, v4\n" \
"ds_read_b64_tr_b16 a[92:93], v20 offset:0\n" \
"ds_read_b64_tr_b16 a[94:95], v20 offset:4096\n" \
"ds_read_b64_tr_b16 a[88:89], v20 offset:8\n" \
"ds_read_b64_tr_b16 a[90:91], v20 offset:4104\n" \
"v_add_u32 v20, 0x8000, v5\n" \
"ds_read_b64_tr_b16 a[84:85], v20 offset:0\n" \
"ds_read_b64_tr_b16 a[86:87], v20 offset:4096\n" \
"ds_read_b64_tr_b16 a[80:81], v20 offset:8\n" \
"ds_read_b64_tr_b16 a[82:83], v20 offset:4104\n" \
"v_add_u32 v20, 0x8000, v6\n" \
"ds_read_b64_tr_b16 a[76:77], v20 offset:0\n" \
"ds_read_b64_tr_b16 a[78:79], v20 offset:4096\n" \
"ds_read_b64_tr_b16 a[72:73], v20 offset:8\n" \
"ds_read_b64_tr_b16 a[74:75], v20 offset:4104\n" \
"v_add_u32 v20, 0x8000, v7\n" \
"ds_read_b64_tr_b16 a[68:69], v20 offset:0\n" \
"ds_read_b64_tr_b16 a[70:71], v20 offset:4096\n" \
"ds_read_b64_tr_b16 a[64:65], v20 offset:8\n" \
"ds_read_b64_tr_b16 a[66:67], v20 offset:4104\n" \
"s_waitcnt lgkmcnt(0)\n" \
"s_barrier\n" \
"s_waitcnt vmcnt(4)\n" \
"s_barrier\n" \
"v_add_u32 v20, 0x10000, v4\n" \
"ds_read_b64_tr_b16 a[60:61], v20 offset:0\n" \
"ds_read_b64_tr_b16 a[62:63], v20 offset:4096\n" \
"ds_read_b64_tr_b16 a[56:57], v20 offset:8\n" \
"ds_read_b64_tr_b16 a[58:59], v20 offset:4104\n" \
"v_add_u32 v20, 0x10000, v5\n" \
"ds_read_b64_tr_b16 a[52:53], v20 offset:0\n" \
"ds_read_b64_tr_b16 a[54:55], v20 offset:4096\n" \
"ds_read_b64_tr_b16 a[48:49], v20 offset:8\n" \
"ds_read_b64_tr_b16 a[50:51], v20 offset:4104\n" \
"v_add_u32 v20, 0x10000, v6\n" \
"ds_read_b64_tr_b16 a[44:45], v20 offset:0\n" \
"ds_read_b64_tr_b16 a[46:47], v20 offset:4096\n" \
"ds_read_b64_tr_b16 a[40:41], v20 offset:8\n" \
"ds_read_b64_tr_b16 a[42:43], v20 offset:4104\n" \
"v_add_u32 v20, 0x10000, v7\n" \
"ds_read_b64_tr_b16 a[36:37], v20 offset:0\n" \
"ds_read_b64_tr_b16 a[38:39], v20 offset:4096\n" \
"ds_read_b64_tr_b16 a[32:33], v20 offset:8\n" \
"ds_read_b64_tr_b16 a[34:35], v20 offset:4104\n" \
"s_waitcnt lgkmcnt(0)\n" \
"s_barrier\n" \
"s_waitcnt vmcnt(0)\n" \
"s_barrier\n" \
"v_add_u32 v20, 0x18000, v4\n" \
"ds_read_b64_tr_b16 a[28:29], v20 offset:0\n" \
"ds_read_b64_tr_b16 a[30:31], v20 offset:4096\n" \
"ds_read_b64_tr_b16 a[24:25], v20 offset:8\n" \
"ds_read_b64_tr_b16 a[26:27], v20 offset:4104\n" \
"v_add_u32 v20, 0x18000, v5\n" \
"ds_read_b64_tr_b16 a[20:21], v20 offset:0\n" \
"ds_read_b64_tr_b16 a[22:23], v20 offset:4096\n" \
"ds_read_b64_tr_b16 a[16:17], v20 offset:8\n" \
"ds_read_b64_tr_b16 a[18:19], v20 offset:4104\n" \
"v_add_u32 v20, 0x18000, v6\n" \
"ds_read_b64_tr_b16 a[12:13], v20 offset:0\n" \
"ds_read_b64_tr_b16 a[14:15], v20 offset:4096\n" \
"ds_read_b64_tr_b16 a[8:9], v20 offset:8\n" \
"ds_read_b64_tr_b16 a[10:11], v20 offset:4104\n" \
"v_add_u32 v20, 0x18000, v7\n" \
"ds_read_b64_tr_b16 a[4:5], v20 offset:0\n" \
"ds_read_b64_tr_b16 a[6:7], v20 offset:4096\n" \
"ds_read_b64_tr_b16 a[0:1], v20 offset:8\n" \
"ds_read_b64_tr_b16 a[2:3], v20 offset:4104\n" \
"s_waitcnt lgkmcnt(0)\n" \
"s_barrier\n" \
"ds_write_b128 v15, v[24:27] offset:0\n" \
"ds_write_b128 v15, v[28:31] offset:1024\n" \
"ds_write_b128 v15, v[32:35] offset:2048\n" \
"ds_write_b128 v15, v[36:39] offset:3072\n" \
"ds_write_b128 v15, v[40:43] offset:4096\n" \
"ds_write_b128 v15, v[44:47] offset:5120\n" \
"ds_write_b128 v15, v[48:51] offset:6144\n" \
"ds_write_b128 v15, v[52:55] offset:7168\n" \
"ds_write_b128 v15, v[56:59] offset:8192\n" \
"ds_write_b128 v15, v[60:63] offset:9216\n" \
"ds_write_b128 v15, v[64:67] offset:10240\n" \
"ds_write_b128 v15, v[68:71] offset:11264\n" \
"ds_write_b128 v15, v[72:75] offset:12288\n" \
"ds_write_b128 v15, v[76:79] offset:13312\n" \
"ds_write_b128 v15, v[80:83] offset:14336\n" \
"ds_write_b128 v15, v[84:87] offset:15360\n" \
"s_cmp_lt_u32 s33, 2\n" \
"s_cbranch_scc1 DZ_%=\n" \
"v_accvgpr_write_b32 a28, 0\n" \
"v_accvgpr_write_b32 a29, 0\n" \
"v_accvgpr_write_b32 a30, 0\n" \
"v_accvgpr_write_b32 a31, 0\n" \
"v_accvgpr_write_b32 a24, 0\n" \
"v_accvgpr_write_b32 a25, 0\n" \
"v_accvgpr_write_b32 a26, 0\n" \
"v_accvgpr_write_b32 a27, 0\n" \
"v_accvgpr_write_b32 a20, 0\n" \
"v_accvgpr_write_b32 a21, 0\n" \
"v_accvgpr_write_b32 a22, 0\n" \
"v_accvgpr_write_b32 a23, 0\n" \
"v_accvgpr_write_b32 a16, 0\n" \
"v_accvgpr_write_b32 a17, 0\n" \
"v_accvgpr_write_b32 a18, 0\n" \
"v_accvgpr_write_b32 a19, 0\n" \
"v_accvgpr_write_b32 a12, 0\n" \
"v_accvgpr_write_b32 a13, 0\n" \
"v_accvgpr_write_b32 a14, 0\n" \
"v_accvgpr_write_b32 a15, 0\n" \
"v_accvgpr_write_b32 a8, 0\n" \
"v_accvgpr_write_b32 a9, 0\n" \
"v_accvgpr_write_b32 a10, 0\n" \
"v_accvgpr_write_b32 a11, 0\n" \
"v_accvgpr_write_b32 a4, 0\n" \
"v_accvgpr_write_b32 a5, 0\n" \
"v_accvgpr_write_b32 a6, 0\n" \
"v_accvgpr_write_b32 a7, 0\n" \
"v_accvgpr_write_b32 a0, 0\n" \
"v_accvgpr_write_b32 a1, 0\n" \
"v_accvgpr_write_b32 a2, 0\n" \
"v_accvgpr_write_b32 a3, 0\n" \
"DZ_%=:\n" \
"s_waitcnt lgkmcnt(0)\n" \
"s_mov_b32 s57, 0\n" \
"DL_%=:\n" \
"s_add_u32 s52, s35, 1\n" \
"s_cmp_lg_u32 s57, 0\n" \
"s_cbranch_scc1 DG_%=\n" \
"DP_%=:\n" \
"buffer_load_dword v13, v2, s[40:43], 0 offen sc1\n" \
"s_waitcnt vmcnt(0)\n" \
"DPC_%=:\n" \
"v_cmp_gt_u32 vcc, s52, v13\n" \
"s_cbranch_vccz DPD_%=\n" \
"s_sleep 2\n" \
"s_sub_u32 s50, s50, 1\n" \
"s_cbranch_scc0 DP_%=\n" \
"s_mov_b32 s50, 0\n" \
"DPD_%=:\n" \
"s_add_u32 s48, s53, 0\n" \
"buffer_load_dwordx4 v[48:51], v0, s[40:43], s48 offen sc1\n" \
"s_add_u32 s48, s53, 16384\n" \
"buffer_load_dwordx4 v[52:55], v0, s[40:43], s48 offen sc1\n" \
"s_add_u32 s48, s53, 32768\n" \
"buffer_load_dwordx4 v[56:59], v0, s[40:43], s48 offen sc1\n" \
"s_add_u32 s48, s53, 49152\n" \
"buffer_load_dwordx4 v[60:63], v0, s[40:43], s48 offen sc1\n" \
"s_add_u32 s48, s53, 65536\n" \
"buffer_load_dwordx4 v[64:67], v0, s[40:43], s48 offen sc1\n" \
"s_add_u32 s48, s53, 81920\n" \
"buffer_load_dwordx4 v[68:71], v0, s[40:43], s48 offen sc1\n" \
"DG_%=:\n" \
"s_add_u32 s52, s52, 1\n" \
"s_bitcmp1_b64 %[dpar], s35\n" \
"s_cselect_b32 s69, 0x2000, 0\n" \
"s_waitcnt vmcnt(5)\n" \
"v_mfma_f32_16x16x32_bf16 v[44:47], v[224:227], v[48:51], 0\n" \
"ds_read_b128 v[112:115], v15 offset:0\n" \
"v_mfma_f32_16x16x32_bf16 v[40:43], v[228:231], v[48:51], 0\n" \
"ds_read_b128 v[116:119], v15 offset:1024\n" \
"v_mfma_f32_16x16x32_bf16 v[36:39], v[232:235], v[48:51], 0\n" \
"s_add_u32 s48, s53, 98304\n" \
"v_mfma_f32_16x16x32_bf16 v[32:35], v[236:239], v[48:51], 0\n" \
"buffer_load_dwordx4 v[72:75], v0, s[40:43], s48 offen sc1\n" \
"v_mfma_f32_16x16x32_bf16 v[28:31], v[240:243], v[48:51], 0\n" \
"v_mfma_f32_16x16x32_bf16 v[24:27], v[244:247], v[48:51], 0\n" \
"v_mfma_f32_16x16x32_bf16 v[20:23], v[248:251], v[48:51], 0\n" \
"v_mfma_f32_16x16x32_bf16 v[16:19], v[252:255], v[48:51], 0\n" \
"s_add_u32 s48, s53, 114688\n" \
"s_waitcnt vmcnt(5)\n" \
"v_mfma_f32_16x16x32_bf16 v[44:47], v[200:203], v[52:55], v[44:47]\n" \
"buffer_load_dwordx4 v[76:79], v0, s[40:43], s48 offen sc1\n" \
"ds_read_b128 v[104:107], v15 offset:2048\n" \
"v_mfma_f32_16x16x32_bf16 v[40:43], v[204:207], v[52:55], v[40:43]\n" \
"ds_read_b128 v[108:111], v15 offset:3072\n" \
"v_mfma_f32_16x16x32_bf16 v[36:39], v[208:211], v[52:55], v[36:39]\n" \
"v_mfma_f32_16x16x32_bf16 v[32:35], v[212:215], v[52:55], v[32:35]\n" \
"v_mfma_f32_16x16x32_bf16 v[28:31], v[216:219], v[52:55], v[28:31]\n" \
"s_add_u32 s48, s53, 131072\n" \
"v_mfma_f32_16x16x32_bf16 v[24:27], v[220:223], v[52:55], v[24:27]\n" \
"buffer_load_dwordx4 v[80:83], v0, s[40:43], s48 offen sc1\n" \
"s_waitcnt lgkmcnt(2)\n" \
"v_mfma_f32_16x16x32_bf16 v[20:23], v[116:119], v[52:55], v[20:23]\n" \
"v_mfma_f32_16x16x32_bf16 v[16:19], v[112:115], v[52:55], v[16:19]\n" \
"s_waitcnt vmcnt(6)\n" \
"v_mfma_f32_16x16x32_bf16 v[44:47], v[176:179], v[56:59], v[44:47]\n" \
"ds_read_b128 v[112:115], v15 offset:4096\n" \
"v_mfma_f32_16x16x32_bf16 v[40:43], v[180:183], v[56:59], v[40:43]\n" \
"s_add_u32 s48, s53, 147456\n" \
"ds_read_b128 v[116:119], v15 offset:5120\n" \
"v_mfma_f32_16x16x32_bf16 v[36:39], v[184:187], v[56:59], v[36:39]\n" \
"buffer_load_dwordx4 v[84:87], v0, s[40:43], s48 offen sc1\n" \
"v_mfma_f32_16x16x32_bf16 v[32:35], v[188:191], v[56:59], v[32:35]\n" \
"v_mfma_f32_16x16x32_bf16 v[28:31], v[192:195], v[56:59], v[28:31]\n" \
"v_mfma_f32_16x16x32_bf16 v[24:27], v[196:199], v[56:59], v[24:27]\n" \
"s_waitcnt lgkmcnt(2)\n" \
"v_mfma_f32_16x16x32_bf16 v[20:23], v[108:111], v[56:59], v[20:23]\n" \
"s_add_u32 s48, s53, 163840\n" \
"v_mfma_f32_16x16x32_bf16 v[16:19], v[104:107], v[56:59], v[16:19]\n" \
"buffer_load_dwordx4 v[88:91], v0, s[40:43], s48 offen sc1\n" \
"s_waitcnt vmcnt(7)\n" \
"v_mfma_f32_16x16x32_bf16 v[44:47], v[152:155], v[60:63], v[44:47]\n" \
"ds_read_b128 v[104:107], v15 offset:6144\n" \
"v_mfma_f32_16x16x32_bf16 v[40:43], v[156:159], v[60:63], v[40:43]\n" \
"ds_read_b128 v[108:111], v15 offset:7168\n" \
"v_mfma_f32_16x16x32_bf16 v[36:39], v[160:163], v[60:63], v[36:39]\n" \
"v_mfma_f32_16x16x32_bf16 v[32:35], v[164:167], v[60:63], v[32:35]\n" \
"s_add_u32 s48, s53, 180224\n" \
"v_mfma_f32_16x16x32_bf16 v[28:31], v[168:171], v[60:63], v[28:31]\n" \
"buffer_load_dwordx4 v[92:95], v0, s[40:43], s48 offen sc1\n" \
"v_mfma_f32_16x16x32_bf16 v[24:27], v[172:175], v[60:63], v[24:27]\n" \
"s_waitcnt lgkmcnt(2)\n" \
"v_mfma_f32_16x16x32_bf16 v[20:23], v[116:119], v[60:63], v[20:23]\n" \
"v_mfma_f32_16x16x32_bf16 v[16:19], v[112:115], v[60:63], v[16:19]\n" \
"s_waitcnt vmcnt(7)\n" \
"v_mfma_f32_16x16x32_bf16 v[44:47], v[128:131], v[64:67], v[44:47]\n" \
"s_add_u32 s48, s53, 196608\n" \
"ds_read_b128 v[112:115], v15 offset:8192\n" \
"v_mfma_f32_16x16x32_bf16 v[40:43], v[132:135], v[64:67], v[40:43]\n" \
"buffer_load_dwordx4 v[96:99], v0, s[40:43], s48 offen sc1\n" \
"ds_read_b128 v[116:119], v15 offset:9216\n" \
"v_mfma_f32_16x16x32_bf16 v[36:39], v[136:139], v[64:67], v[36:39]\n" \
"v_mfma_f32_16x16x32_bf16 v[32:35], v[140:143], v[64:67], v[32:35]\n" \
"v_mfma_f32_16x16x32_bf16 v[28:31], v[144:147], v[64:67], v[28:31]\n" \
"v_mfma_f32_16x16x32_bf16 v[24:27], v[148:151], v[64:67], v[24:27]\n" \
"s_add_u32 s48, s53, s54\n" \
"s_waitcnt lgkmcnt(2)\n" \
"v_mfma_f32_16x16x32_bf16 v[20:23], v[108:111], v[64:67], v[20:23]\n" \
"buffer_load_dwordx4 v[100:103], v0, s[40:43], s48 offen sc1\n" \
"v_mfma_f32_16x16x32_bf16 v[16:19], v[104:107], v[64:67], v[16:19]\n" \
"s_waitcnt vmcnt(8)\n" \
"v_mfma_f32_16x16x32_bf16 v[44:47], a[232:235], v[68:71], v[44:47]\n" \
"ds_read_b128 v[104:107], v15 offset:10240\n" \
"v_mfma_f32_16x16x32_bf16 v[40:43], a[236:239], v[68:71], v[40:43]\n" \
"ds_read_b128 v[108:111], v15 offset:11264\n" \
"v_mfma_f32_16x16x32_bf16 v[36:39], a[240:243], v[68:71], v[36:39]\n" \
"v_mfma_f32_16x16x32_bf16 v[32:35], a[244:247], v[68:71], v[32:35]\n" \
"buffer_load_dwordx4 v[120:123], v3, s[40:43], s55 offen sc1\n" \
"v_mfma_f32_16x16x32_bf16 v[28:31], a[248:251], v[68:71], v[28:31]\n" \
"v_mfma_f32_16x16x32_bf16 v[24:27], a[252:255], v[68:71], v[24:27]\n" \
"s_waitcnt lgkmcnt(2)\n" \
"v_mfma_f32_16x16x32_bf16 v[20:23], v[116:119], v[68:71], v[20:23]\n" \
"buffer_load_dword v13, v2, s[40:43], 0 offen sc1\n" \
"v_mfma_f32_16x16x32_bf16 v[16:19], v[112:115], v[68:71], v[16:19]\n" \
"s_waitcnt vmcnt(9)\n" \
"v_mfma_f32_16x16x32_bf16 v[44:47], a[208:211], v[72:75], v[44:47]\n" \
"ds_read_b128 v[112:115], v15 offset:12288\n" \
"v_mfma_f32_16x16x32_bf16 v[40:43], a[212:215], v[72:75], v[40:43]\n" \
"ds_read_b128 v[116:119], v15 offset:13312\n" \
"v_mfma_f32_16x16x32_bf16 v[36:39], a[216:219], v[72:75], v[36:39]\n" \
"s_add_u32 s53, s53, 0x200000\n" \
"s_xor_b32 s53, s53, s69\n" \
"v_mfma_f32_16x16x32_bf16 v[32:35], a[220:223], v[72:75], v[32:35]\n" \
"v_mfma_f32_16x16x32_bf16 v[28:31], a[224:227], v[72:75], v[28:31]\n" \
"s_add_u32 s55, s55, 0x200000\n" \
"s_xor_b32 s55, s55, s69\n" \
"v_mfma_f32_16x16x32_bf16 v[24:27], a[228:231], v[72:75], v[24:27]\n" \
"s_waitcnt lgkmcnt(2)\n" \
"v_mfma_f32_16x16x32_bf16 v[20:23], v[108:111], v[72:75], v[20:23]\n" \
"v_mfma_f32_16x16x32_bf16 v[16:19], v[104:107], v[72:75], v[16:19]\n" \
"s_waitcnt vmcnt(8)\n" \
"v_mfma_f32_16x16x32_bf16 v[44:47], a[184:187], v[76:79], v[44:47]\n" \
"ds_read_b128 v[104:107], v15 offset:14336\n" \
"v_mfma_f32_16x16x32_bf16 v[40:43], a[188:191], v[76:79], v[40:43]\n" \
"ds_read_b128 v[108:111], v15 offset:15360\n" \
"v_mfma_f32_16x16x32_bf16 v[36:39], a[192:195], v[76:79], v[36:39]\n" \
"v_mfma_f32_16x16x32_bf16 v[32:35], a[196:199], v[76:79], v[32:35]\n" \
"v_mfma_f32_16x16x32_bf16 v[28:31], a[200:203], v[76:79], v[28:31]\n" \
"v_mfma_f32_16x16x32_bf16 v[24:27], a[204:207], v[76:79], v[24:27]\n" \
"s_waitcnt lgkmcnt(2)\n" \
"v_mfma_f32_16x16x32_bf16 v[20:23], v[116:119], v[76:79], v[20:23]\n" \
"v_mfma_f32_16x16x32_bf16 v[16:19], v[112:115], v[76:79], v[16:19]\n" \
"s_waitcnt vmcnt(7)\n" \
"v_mfma_f32_16x16x32_bf16 v[44:47], a[160:163], v[80:83], v[44:47]\n" \
"v_mfma_f32_16x16x32_bf16 v[40:43], a[164:167], v[80:83], v[40:43]\n" \
"v_mfma_f32_16x16x32_bf16 v[36:39], a[168:171], v[80:83], v[36:39]\n" \
"v_mfma_f32_16x16x32_bf16 v[32:35], a[172:175], v[80:83], v[32:35]\n" \
"v_mfma_f32_16x16x32_bf16 v[28:31], a[176:179], v[80:83], v[28:31]\n" \
"v_mfma_f32_16x16x32_bf16 v[24:27], a[180:183], v[80:83], v[24:27]\n" \
"s_waitcnt lgkmcnt(0)\n" \
"v_mfma_f32_16x16x32_bf16 v[20:23], v[108:111], v[80:83], v[20:23]\n" \
"v_mfma_f32_16x16x32_bf16 v[16:19], v[104:107], v[80:83], v[16:19]\n" \
"s_waitcnt vmcnt(6)\n" \
"v_mfma_f32_16x16x32_bf16 v[44:47], a[128:131], v[84:87], v[44:47]\n" \
"v_mfma_f32_16x16x32_bf16 v[40:43], a[132:135], v[84:87], v[40:43]\n" \
"v_mfma_f32_16x16x32_bf16 v[36:39], a[136:139], v[84:87], v[36:39]\n" \
"v_mfma_f32_16x16x32_bf16 v[32:35], a[140:143], v[84:87], v[32:35]\n" \
"v_mfma_f32_16x16x32_bf16 v[28:31], a[144:147], v[84:87], v[28:31]\n" \
"s_waitcnt vmcnt(0)\n" \
"v_cmp_gt_u32 vcc, s52, v13\n" \
"v_mfma_f32_16x16x32_bf16 v[24:27], a[148:151], v[84:87], v[24:27]\n" \
"v_mfma_f32_16x16x32_bf16 v[20:23], a[152:155], v[84:87], v[20:23]\n" \
"v_mfma_f32_16x16x32_bf16 v[16:19], a[156:159], v[84:87], v[16:19]\n" \
"s_cmp_eq_u64 vcc, 0\n" \
"s_cselect_b32 s57, 1, 0\n" \
"s_cbranch_scc1 DH_%=\n" \
"v_mfma_f32_16x16x32_bf16 v[44:47], a[96:99], v[88:91], v[44:47]\n" \
"v_mfma_f32_16x16x32_bf16 v[40:43], a[100:103], v[88:91], v[40:43]\n" \
"v_lshlrev_b32 v104, 16, v120\n" \
"v_and_b32 v105, 0xffff0000, v120\n" \
"v_mfma_f32_16x16x32_bf16 v[36:39], a[104:107], v[88:91], v[36:39]\n" \
"v_lshlrev_b32 v106, 16, v121\n" \
"v_and_b32 v107, 0xffff0000, v121\n" \
"v_mfma_f32_16x16x32_bf16 v[32:35], a[108:111], v[88:91], v[32:35]\n" \
"v_lshlrev_b32 v108, 16, v122\n" \
"v_and_b32 v109, 0xffff0000, v122\n" \
"v_mfma_f32_16x16x32_bf16 v[28:31], a[112:115], v[88:91], v[28:31]\n" \
"v_lshlrev_b32 v110, 16, v123\n" \
"v_and_b32 v111, 0xffff0000, v123\n" \
"v_mfma_f32_16x16x32_bf16 v[24:27], a[116:119], v[88:91], v[24:27]\n" \
"v_mfma_f32_16x16x32_bf16 v[20:23], a[120:123], v[88:91], v[20:23]\n" \
"v_mfma_f32_16x16x32_bf16 v[16:19], a[124:127], v[88:91], v[16:19]\n" \
"v_mfma_f32_16x16x32_bf16 v[44:47], a[64:67], v[92:95], v[44:47]\n" \
"v_mfma_f32_16x16x32_bf16 v[44:47], a[32:35], v[96:99], v[44:47]\n" \
"v_mfma_f32_16x16x32_bf16 v[44:47], a[0:3], v[100:103], v[44:47]\n" \
"v_mfma_f32_16x16x32_bf16 v[40:43], a[68:71], v[92:95], v[40:43]\n" \
"v_mfma_f32_16x16x32_bf16 v[40:43], a[36:39], v[96:99], v[40:43]\n" \
"v_mfma_f32_16x16x32_bf16 v[40:43], a[4:7], v[100:103], v[40:43]\n" \
"ds_write_b128 v10, v[44:47] offset:4096\n" \
"v_mfma_f32_16x16x32_bf16 v[36:39], a[72:75], v[92:95], v[36:39]\n" \
"v_mfma_f32_16x16x32_bf16 v[36:39], a[40:43], v[96:99], v[36:39]\n" \
"v_mfma_f32_16x16x32_bf16 v[36:39], a[8:11], v[100:103], v[36:39]\n" \
"ds_write_b128 v10, v[40:43] offset:0\n" \
"v_mfma_f32_16x16x32_bf16 v[32:35], a[76:79], v[92:95], v[32:35]\n" \
"v_mfma_f32_16x16x32_bf16 v[32:35], a[44:47], v[96:99], v[32:35]\n" \
"v_mfma_f32_16x16x32_bf16 v[32:35], a[12:15], v[100:103], v[32:35]\n" \
"ds_write_b128 v9, v[36:39] offset:4096\n" \
"v_mfma_f32_16x16x32_bf16 v[28:31], a[80:83], v[92:95], v[28:31]\n" \
"v_mfma_f32_16x16x32_bf16 v[28:31], a[48:51], v[96:99], v[28:31]\n" \
"v_mfma_f32_16x16x32_bf16 v[28:31], a[16:19], v[100:103], v[28:31]\n" \
"ds_write_b128 v9, v[32:35] offset:0\n" \
"v_mfma_f32_16x16x32_bf16 v[24:27], a[84:87], v[92:95], v[24:27]\n" \
"v_mfma_f32_16x16x32_bf16 v[24:27], a[52:55], v[96:99], v[24:27]\n" \
"v_mfma_f32_16x16x32_bf16 v[24:27], a[20:23], v[100:103], v[24:27]\n" \
"ds_write_b128 v8, v[28:31] offset:4096\n" \
"v_mfma_f32_16x16x32_bf16 v[20:23], a[88:91], v[92:95], v[20:23]\n" \
"v_mfma_f32_16x16x32_bf16 v[20:23], a[56:59], v[96:99], v[20:23]\n" \
"v_mfma_f32_16x16x32_bf16 v[20:23], a[24:27], v[100:103], v[20:23]\n" \
"ds_write_b128 v8, v[24:27] offset:0\n" \
"v_mfma_f32_16x16x32_bf16 v[16:19], a[92:95], v[92:95], v[16:19]\n" \
"v_mfma_f32_16x16x32_bf16 v[16:19], a[60:63], v[96:99], v[16:19]\n" \
"v_mfma_f32_16x16x32_bf16 v[16:19], a[28:31], v[100:103], v[16:19]\n" \
"s_waitcnt lgkmcnt(0)\n" \
"s_barrier\n" \
"ds_read_b128 v[76:79], v11 offset:1024\n" \
"ds_read_b128 v[80:83], v11 offset:2048\n" \
"ds_read_b128 v[84:87], v11 offset:3072\n" \
"ds_read_b128 v[88:91], v11 offset:5120\n" \
"ds_read_b128 v[92:95], v11 offset:6144\n" \
"ds_read_b128 v[96:99], v11 offset:7168\n" \
"s_branch DJ_%=\n" \
"DH_%=:\n" \
"v_mfma_f32_16x16x32_bf16 v[44:47], a[96:99], v[88:91], v[44:47]\n" \
"s_add_u32 s48, s53, 0\n" \
"buffer_load_dwordx4 v[48:51], v0, s[40:43], s48 offen sc1\n" \
"v_mfma_f32_16x16x32_bf16 v[40:43], a[100:103], v[88:91], v[40:43]\n" \
"v_lshlrev_b32 v104, 16, v120\n" \
"v_and_b32 v105, 0xffff0000, v120\n" \
"v_mfma_f32_16x16x32_bf16 v[36:39], a[104:107], v[88:91], v[36:39]\n" \
"v_lshlrev_b32 v106, 16, v121\n" \
"v_and_b32 v107, 0xffff0000, v121\n" \
"v_mfma_f32_16x16x32_bf16 v[32:35], a[108:111], v[88:91], v[32:35]\n" \
"v_lshlrev_b32 v108, 16, v122\n" \
"v_and_b32 v109, 0xffff0000, v122\n" \
"v_mfma_f32_16x16x32_bf16 v[28:31], a[112:115], v[88:91], v[28:31]\n" \
"v_lshlrev_b32 v110, 16, v123\n" \
"v_and_b32 v111, 0xffff0000, v123\n" \
"s_add_u32 s48, s53, 16384\n" \
"buffer_load_dwordx4 v[52:55], v0, s[40:43], s48 offen sc1\n" \
"v_mfma_f32_16x16x32_bf16 v[24:27], a[116:119], v[88:91], v[24:27]\n" \
"v_mfma_f32_16x16x32_bf16 v[20:23], a[120:123], v[88:91], v[20:23]\n" \
"v_mfma_f32_16x16x32_bf16 v[16:19], a[124:127], v[88:91], v[16:19]\n" \
"v_mfma_f32_16x16x32_bf16 v[44:47], a[64:67], v[92:95], v[44:47]\n" \
"s_add_u32 s48, s53, 32768\n" \
"buffer_load_dwordx4 v[56:59], v0, s[40:43], s48 offen sc1\n" \
"v_mfma_f32_16x16x32_bf16 v[44:47], a[32:35], v[96:99], v[44:47]\n" \
"v_mfma_f32_16x16x32_bf16 v[44:47], a[0:3], v[100:103], v[44:47]\n" \
"v_mfma_f32_16x16x32_bf16 v[40:43], a[68:71], v[92:95], v[40:43]\n" \
"v_mfma_f32_16x16x32_bf16 v[40:43], a[36:39], v[96:99], v[40:43]\n" \
"v_mfma_f32_16x16x32_bf16 v[40:43], a[4:7], v[100:103], v[40:43]\n" \
"ds_write_b128 v10, v[44:47] offset:4096\n" \
"v_mfma_f32_16x16x32_bf16 v[36:39], a[72:75], v[92:95], v[36:39]\n" \
"v_mfma_f32_16x16x32_bf16 v[36:39], a[40:43], v[96:99], v[36:39]\n" \
"v_mfma_f32_16x16x32_bf16 v[36:39], a[8:11], v[100:103], v[36:39]\n" \
"ds_write_b128 v10, v[40:43] offset:0\n" \
"v_mfma_f32_16x16x32_bf16 v[32:35], a[76:79], v[92:95], v[32:35]\n" \
"v_mfma_f32_16x16x32_bf16 v[32:35], a[44:47], v[96:99], v[32:35]\n" \
"v_mfma_f32_16x16x32_bf16 v[32:35], a[12:15], v[100:103], v[32:35]\n" \
"ds_write_b128 v9, v[36:39] offset:4096\n" \
"v_mfma_f32_16x16x32_bf16 v[28:31], a[80:83], v[92:95], v[28:31]\n" \
"v_mfma_f32_16x16x32_bf16 v[28:31], a[48:51], v[96:99], v[28:31]\n" \
"v_mfma_f32_16x16x32_bf16 v[28:31], a[16:19], v[100:103], v[28:31]\n" \
"ds_write_b128 v9, v[32:35] offset:0\n" \
"v_mfma_f32_16x16x32_bf16 v[24:27], a[84:87], v[92:95], v[24:27]\n" \
"v_mfma_f32_16x16x32_bf16 v[24:27], a[52:55], v[96:99], v[24:27]\n" \
"v_mfma_f32_16x16x32_bf16 v[24:27], a[20:23], v[100:103], v[24:27]\n" \
"ds_write_b128 v8, v[28:31] offset:4096\n" \
"v_mfma_f32_16x16x32_bf16 v[20:23], a[88:91], v[92:95], v[20:23]\n" \
"v_mfma_f32_16x16x32_bf16 v[20:23], a[56:59], v[96:99], v[20:23]\n" \
"v_mfma_f32_16x16x32_bf16 v[20:23], a[24:27], v[100:103], v[20:23]\n" \
"ds_write_b128 v8, v[24:27] offset:0\n" \
"v_mfma_f32_16x16x32_bf16 v[16:19], a[92:95], v[92:95], v[16:19]\n" \
"v_mfma_f32_16x16x32_bf16 v[16:19], a[60:63], v[96:99], v[16:19]\n" \
"v_mfma_f32_16x16x32_bf16 v[16:19], a[28:31], v[100:103], v[16:19]\n" \
"s_waitcnt lgkmcnt(0)\n" \
"s_barrier\n" \
"ds_read_b128 v[76:79], v11 offset:1024\n" \
"ds_read_b128 v[80:83], v11 offset:2048\n" \
"ds_read_b128 v[84:87], v11 offset:3072\n" \
"ds_read_b128 v[88:91], v11 offset:5120\n" \
"ds_read_b128 v[92:95], v11 offset:6144\n" \
"ds_read_b128 v[96:99], v11 offset:7168\n" \
"s_add_u32 s48, s53, 49152\n" \
"buffer_load_dwordx4 v[60:63], v0, s[40:43], s48 offen sc1\n" \
"s_add_u32 s48, s53, 65536\n" \
"buffer_load_dwordx4 v[64:67], v0, s[40:43], s48 offen sc1\n" \
"s_add_u32 s48, s53, 81920\n" \
"buffer_load_dwordx4 v[68:71], v0, s[40:43], s48 offen sc1\n" \
"DJ_%=:\n" \
"v_xor_b32 v8, 0x8000, v8\n" \
"v_xor_b32 v9, 0x8000, v9\n" \
"v_xor_b32 v10, 0x8000, v10\n" \
"v_xor_b32 v11, 0x8000, v11\n" \
"s_waitcnt lgkmcnt(3)\n" \
"v_pk_add_f32 v[16:17], v[16:17], v[76:77]\n" \
"v_pk_add_f32 v[18:19], v[18:19], v[78:79]\n" \
"v_pk_add_f32 v[16:17], v[16:17], v[80:81]\n" \
"v_pk_add_f32 v[18:19], v[18:19], v[82:83]\n" \
"v_pk_add_f32 v[16:17], v[16:17], v[84:85]\n" \
"v_pk_add_f32 v[18:19], v[18:19], v[86:87]\n" \
"s_waitcnt lgkmcnt(0)\n" \
"v_pk_add_f32 v[20:21], v[20:21], v[88:89]\n" \
"v_pk_add_f32 v[22:23], v[22:23], v[90:91]\n" \
"v_pk_add_f32 v[20:21], v[20:21], v[92:93]\n" \
"v_pk_add_f32 v[22:23], v[22:23], v[94:95]\n" \
"v_pk_add_f32 v[20:21], v[20:21], v[96:97]\n" \
"v_pk_add_f32 v[22:23], v[22:23], v[98:99]\n" \
"v_cvt_pk_bf16_f32 v76, v16, v17\n" \
"v_cvt_pk_bf16_f32 v77, v18, v19\n" \
"v_cvt_pk_bf16_f32 v78, v20, v21\n" \
"v_cvt_pk_bf16_f32 v79, v22, v23\n" \
"v_lshlrev_b32 v16, 16, v76\n" \
"v_and_b32 v17, 0xffff0000, v76\n" \
"v_lshlrev_b32 v18, 16, v77\n" \
"v_and_b32 v19, 0xffff0000, v77\n" \
"v_lshlrev_b32 v20, 16, v78\n" \
"v_and_b32 v21, 0xffff0000, v78\n" \
"v_lshlrev_b32 v22, 16, v79\n" \
"v_and_b32 v23, 0xffff0000, v79\n" \
"v_pk_add_f32 v[16:17], v[16:17], v[104:105]\n" \
"v_pk_add_f32 v[18:19], v[18:19], v[106:107]\n" \
"v_pk_add_f32 v[20:21], v[20:21], v[108:109]\n" \
"v_pk_add_f32 v[22:23], v[22:23], v[110:111]\n" \
"v_cvt_pk_bf16_f32 v76, v16, v17\n" \
"v_cvt_pk_bf16_f32 v77, v18, v19\n" \
"v_cvt_pk_bf16_f32 v78, v20, v21\n" \
"v_cvt_pk_bf16_f32 v79, v22, v23\n" \
"buffer_store_dwordx4 v[76:79], v14, s[36:39], s56 offen\n" \
"s_add_u32 s56, s56, 40960\n" \
"s_add_u32 s35, s35, 1\n" \
"s_cmp_lt_u32 s35, 64\n" \
"s_cbranch_scc1 DL_%=\n" \
"s_waitcnt vmcnt(0)\n" \
"s_barrier\n" \
"s_cmp_lg_u32 s50, 0\n" \
"s_cbranch_scc1 DE_%=\n" \
"v_mov_b32 v0, 1\n" \
"buffer_store_dword v0, off, s[40:43], 0 offset:256 sc1\n" \
"DE_%=:\n" \
"DCZ_%=:\n" \
"s_endpgm\n"
#define R_ASM_TEXT \
"s_getpc_b64 s[46:47]\n" \
"RCW_%=:\n" \
"v_mbcnt_lo_u32_b32 v60, -1, 0\n" \
"v_mbcnt_hi_u32_b32 v60, -1, v60\n" \
"v_lshlrev_b32 v60, 7, v60\n" \
"v_min_u32 v61, RCZ_%=-RCW_%=, v60\n" \
"global_load_dword v62, v61, s[46:47]\n" \
"v_add_u32 v60, 0x2000, v60\n" \
"v_min_u32 v61, RCZ_%=-RCW_%=, v60\n" \
"global_load_dword v62, v61, s[46:47]\n" \
"v_add_u32 v60, 0x2000, v60\n" \
"s_mov_b64 s[40:41], %[ws]\n" \
"s_add_u32 s40, s40, 0x800000\n" \
"s_addc_u32 s41, s41, 0\n" \
"s_and_b32 s41, s41, 0xffff\n" \
"s_mov_b32 s42, 0x9000000\n" \
"s_mov_b32 s43, 0x00020000\n" \
"s_mov_b32 s33, %[wave]\n" \
"v_mbcnt_lo_u32_b32 v48, -1, 0\n" \
"v_mbcnt_hi_u32_b32 v48, -1, v48\n" \
"s_lshl_b32 s48, s33, 6\n" \
"v_add_lshl_u32 v48, v48, s48, 4\n" \
"ds_read_b128 v[0:3], v48 offset:0\n" \
"ds_read_b128 v[4:7], v48 offset:4096\n" \
"ds_read_b128 v[32:35], v48 offset:8192\n" \
"ds_read_b128 v[36:39], v48 offset:12288\n" \
"ds_read_b128 v[40:43], v48 offset:16384\n" \
"s_waitcnt lgkmcnt(0)\n" \
"s_barrier\n" \
"v_readfirstlane_b32 s70, v32\n" \
"v_readfirstlane_b32 s71, v33\n" \
"v_readfirstlane_b32 s72, v34\n" \
"v_readfirstlane_b32 s73, v35\n" \
"v_readfirstlane_b32 s57, v36\n" \
"v_readfirstlane_b32 s53, v37\n" \
"v_readfirstlane_b32 s54, v38\n" \
"v_readfirstlane_b32 s55, v39\n" \
"s_nop 4\n" \
"s_lshl_b32 s34, s33, 10\n" \
"s_cmp_eq_u32 s33, 0\n" \
"s_cselect_b32 s51, s51, 0x7ff00000\n" \
"s_mov_b32 s50, 0x80000\n" \
"s_mov_b32 s35, 0\n" \
"s_mov_b64 s[36:37], s[70:71]\n" \
"s_and_b32 s37, s37, 0xffff\n" \
"s_mov_b32 s38, 0x280000\n" \
"s_mov_b32 s39, 0x00020000\n" \
"buffer_load_dwordx4 v[36:39], v4, s[36:39], s55 offen\n" \
"buffer_load_dword v40, v5, s[36:39], s55 offen\n" \
"s_add_u32 s55, s55, 40960\n" \
"buffer_load_dword v12, v2, s[40:43], 0 offen sc1\n" \
"s_waitcnt vmcnt(0)\n" \
"RL_%=:\n" \
"s_add_u32 s52, s35, 1\n" \
"v_lshlrev_b32 v42, 16, v36\n" \
"v_and_b32 v43, 0xffff0000, v36\n" \
"v_lshlrev_b32 v44, 16, v37\n" \
"v_and_b32 v45, 0xffff0000, v37\n" \
"v_lshlrev_b32 v46, 16, v38\n" \
"v_and_b32 v47, 0xffff0000, v38\n" \
"v_lshlrev_b32 v48, 16, v39\n" \
"v_and_b32 v49, 0xffff0000, v39\n" \
"v_lshlrev_b32 v50, 16, v40\n" \
"v_and_b32 v51, 0xffff0000, v40\n" \
"s_cmp_lg_u32 s33, 0\n" \
"s_cbranch_scc1 RPD_%=\n" \
"s_branch RPC_%=\n" \
"RP_%=:\n" \
"buffer_load_dword v12, v2, s[40:43], 0 offen sc1\n" \
"s_waitcnt vmcnt(0)\n" \
"RPC_%=:\n" \
"v_cmp_gt_u32 vcc, s52, v12\n" \
"s_cbranch_vccz RPD_%=\n" \
"s_sleep 2\n" \
"s_sub_u32 s50, s50, 1\n" \
"s_cbranch_scc0 RP_%=\n" \
"s_mov_b32 s50, 0\n" \
"RPD_%=:\n" \
"s_barrier\n" \
"buffer_load_dwordx4 v[16:19], v0, s[40:43], s53 offen sc1\n" \
"buffer_load_dword v32, v1, s[40:43], s53 offen sc1\n" \
"s_add_u32 s48, s53, 81920\n" \
"buffer_load_dwordx4 v[20:23], v0, s[40:43], s48 offen sc1\n" \
"buffer_load_dword v33, v1, s[40:43], s48 offen sc1\n" \
"s_add_u32 s48, s53, 163840\n" \
"buffer_load_dwordx4 v[24:27], v0, s[40:43], s48 offen sc1\n" \
"buffer_load_dword v34, v1, s[40:43], s48 offen sc1\n" \
"s_add_u32 s48, s53, 245760\n" \
"buffer_load_dwordx4 v[28:31], v0, s[40:43], s48 offen sc1\n" \
"buffer_load_dword v35, v1, s[40:43], s48 offen sc1\n" \
"buffer_load_dwordx4 v[36:39], v4, s[36:39], s55 offen\n" \
"buffer_load_dword v40, v5, s[36:39], s55 offen\n" \
"s_add_u32 s55, s55, 40960\n" \
"s_bitcmp1_b64 %[dpar], s35\n" \
"s_cselect_b32 s69, 0x2000, 0\n" \
"s_waitcnt vmcnt(8)\n" \
"v_lshlrev_b32 v52, 16, v16\n" \
"v_and_b32 v53, 0xffff0000, v16\n" \
"v_lshlrev_b32 v54, 16, v17\n" \
"v_and_b32 v55, 0xffff0000, v17\n" \
"v_lshlrev_b32 v56, 16, v18\n" \
"v_and_b32 v57, 0xffff0000, v18\n" \
"v_lshlrev_b32 v58, 16, v19\n" \
"v_and_b32 v59, 0xffff0000, v19\n" \
"v_lshlrev_b32 v60, 16, v32\n" \
"v_and_b32 v61, 0xffff0000, v32\n" \
"s_waitcnt vmcnt(6)\n" \
"v_lshlrev_b32 v70, 16, v20\n" \
"v_and_b32 v71, 0xffff0000, v20\n" \
"v_lshlrev_b32 v72, 16, v21\n" \
"v_and_b32 v73, 0xffff0000, v21\n" \
"v_lshlrev_b32 v74, 16, v22\n" \
"v_and_b32 v75, 0xffff0000, v22\n" \
"v_lshlrev_b32 v76, 16, v23\n" \
"v_and_b32 v77, 0xffff0000, v23\n" \
"v_lshlrev_b32 v78, 16, v33\n" \
"v_and_b32 v79, 0xffff0000, v33\n" \
"v_pk_add_f32 v[52:53], v[52:53], v[70:71]\n" \
"v_pk_add_f32 v[54:55], v[54:55], v[72:73]\n" \
"v_pk_add_f32 v[56:57], v[56:57], v[74:75]\n" \
"v_pk_add_f32 v[58:59], v[58:59], v[76:77]\n" \
"v_pk_add_f32 v[60:61], v[60:61], v[78:79]\n" \
"s_waitcnt vmcnt(4)\n" \
"v_lshlrev_b32 v70, 16, v24\n" \
"v_and_b32 v71, 0xffff0000, v24\n" \
"v_lshlrev_b32 v72, 16, v25\n" \
"v_and_b32 v73, 0xffff0000, v25\n" \
"v_lshlrev_b32 v74, 16, v26\n" \
"v_and_b32 v75, 0xffff0000, v26\n" \
"v_lshlrev_b32 v76, 16, v27\n" \
"v_and_b32 v77, 0xffff0000, v27\n" \
"v_lshlrev_b32 v78, 16, v34\n" \
"v_and_b32 v79, 0xffff0000, v34\n" \
"v_pk_add_f32 v[52:53], v[52:53], v[70:71]\n" \
"v_pk_add_f32 v[54:55], v[54:55], v[72:73]\n" \
"v_pk_add_f32 v[56:57], v[56:57], v[74:75]\n" \
"v_pk_add_f32 v[58:59], v[58:59], v[76:77]\n" \
"v_pk_add_f32 v[60:61], v[60:61], v[78:79]\n" \
"s_waitcnt vmcnt(2)\n" \
"v_lshlrev_b32 v70, 16, v28\n" \
"v_and_b32 v71, 0xffff0000, v28\n" \
"v_lshlrev_b32 v72, 16, v29\n" \
"v_and_b32 v73, 0xffff0000, v29\n" \
"v_lshlrev_b32 v74, 16, v30\n" \
"v_and_b32 v75, 0xffff0000, v30\n" \
"v_lshlrev_b32 v76, 16, v31\n" \
"v_and_b32 v77, 0xffff0000, v31\n" \
"v_lshlrev_b32 v78, 16, v35\n" \
"v_and_b32 v79, 0xffff0000, v35\n" \
"v_pk_add_f32 v[52:53], v[52:53], v[70:71]\n" \
"v_pk_add_f32 v[54:55], v[54:55], v[72:73]\n" \
"v_pk_add_f32 v[56:57], v[56:57], v[74:75]\n" \
"v_pk_add_f32 v[58:59], v[58:59], v[76:77]\n" \
"v_pk_add_f32 v[60:61], v[60:61], v[78:79]\n" \
"v_cvt_pk_bf16_f32 v64, v52, v53\n" \
"v_cvt_pk_bf16_f32 v65, v54, v55\n" \
"v_cvt_pk_bf16_f32 v66, v56, v57\n" \
"v_cvt_pk_bf16_f32 v67, v58, v59\n" \
"v_cvt_pk_bf16_f32 v68, v60, v61\n" \
"v_lshlrev_b32 v52, 16, v64\n" \
"v_and_b32 v53, 0xffff0000, v64\n" \
"v_lshlrev_b32 v54, 16, v65\n" \
"v_and_b32 v55, 0xffff0000, v65\n" \
"v_lshlrev_b32 v56, 16, v66\n" \
"v_and_b32 v57, 0xffff0000, v66\n" \
"v_lshlrev_b32 v58, 16, v67\n" \
"v_and_b32 v59, 0xffff0000, v67\n" \
"v_lshlrev_b32 v60, 16, v68\n" \
"v_and_b32 v61, 0xffff0000, v68\n" \
"v_pk_add_f32 v[52:53], v[52:53], v[42:43]\n" \
"v_pk_add_f32 v[54:55], v[54:55], v[44:45]\n" \
"v_pk_add_f32 v[56:57], v[56:57], v[46:47]\n" \
"v_pk_add_f32 v[58:59], v[58:59], v[48:49]\n" \
"v_pk_add_f32 v[60:61], v[60:61], v[50:51]\n" \
"v_cvt_pk_bf16_f32 v64, v52, v53\n" \
"v_cvt_pk_bf16_f32 v65, v54, v55\n" \
"v_cvt_pk_bf16_f32 v66, v56, v57\n" \
"v_cvt_pk_bf16_f32 v67, v58, v59\n" \
"v_cvt_pk_bf16_f32 v68, v60, v61\n" \
"v_lshlrev_b32 v52, 16, v64\n" \
"v_and_b32 v53, 0xffff0000, v64\n" \
"v_lshlrev_b32 v54, 16, v65\n" \
"v_and_b32 v55, 0xffff0000, v65\n" \
"v_lshlrev_b32 v56, 16, v66\n" \
"v_and_b32 v57, 0xffff0000, v66\n" \
"v_lshlrev_b32 v58, 16, v67\n" \
"v_and_b32 v59, 0xffff0000, v67\n" \
"v_lshlrev_b32 v60, 16, v68\n" \
"v_and_b32 v61, 0xffff0000, v68\n" \
"v_pk_add_f32 v[70:71], v[52:53], v[54:55]\n" \
"v_pk_add_f32 v[72:73], v[56:57], v[58:59]\n" \
"v_pk_add_f32 v[70:71], v[70:71], v[60:61]\n" \
"s_nop 0\n" \
"v_pk_add_f32 v[70:71], v[70:71], v[72:73]\n" \
"s_nop 0\n" \
"v_add_f32 v70, v70, v71\n" \
"buffer_store_dwordx4 v[64:67], v4, s[40:43], s57 offen sc1\n" \
"buffer_store_dword v68, v5, s[40:43], s57 offen sc1\n" \
"v_add_f32_dpp v70, v70, v70 row_ror:1 row_mask:0xf bank_mask:0xf\n" \
"s_nop 1\n" \
"v_add_f32_dpp v70, v70, v70 row_ror:2 row_mask:0xf bank_mask:0xf\n" \
"s_nop 1\n" \
"v_add_f32_dpp v70, v70, v70 row_ror:4 row_mask:0xf bank_mask:0xf\n" \
"s_nop 1\n" \
"v_add_f32_dpp v70, v70, v70 row_ror:8 row_mask:0xf bank_mask:0xf\n" \
"v_mov_b32 v71, v70\n" \
"s_nop 1\n" \
"v_permlane32_swap_b32 v70, v71\n" \
"v_add_f32 v70, v70, v71\n" \
"v_mov_b32 v71, v70\n" \
"s_nop 1\n" \
"v_permlane16_swap_b32 v70, v71\n" \
"v_add_f32 v70, v70, v71\n" \
"v_mul_f32 v82, 0x3acccccd, v70\n" \
"v_pk_add_f32 v[72:73], v[52:53], v[82:83] op_sel_hi:[1,0] neg_lo:[0,1] neg_hi:[0,1]\n" \
"v_pk_add_f32 v[74:75], v[54:55], v[82:83] op_sel_hi:[1,0] neg_lo:[0,1] neg_hi:[0,1]\n" \
"v_pk_add_f32 v[76:77], v[56:57], v[82:83] op_sel_hi:[1,0] neg_lo:[0,1] neg_hi:[0,1]\n" \
"v_pk_add_f32 v[78:79], v[58:59], v[82:83] op_sel_hi:[1,0] neg_lo:[0,1] neg_hi:[0,1]\n" \
"v_pk_add_f32 v[80:81], v[60:61], v[82:83] op_sel_hi:[1,0] neg_lo:[0,1] neg_hi:[0,1]\n" \
"v_pk_mul_f32 v[70:71], v[72:73], v[72:73]\n" \
"v_cvt_pk_bf16_f32 v82, v82, v82\n" \
"v_and_b32 v82, 0xffff0000, v82\n" \
"v_pk_fma_f32 v[70:71], v[74:75], v[74:75], v[70:71]\n" \
"v_pk_add_f32 v[54:55], v[54:55], v[82:83] op_sel_hi:[1,0] neg_lo:[0,1] neg_hi:[0,1]\n" \
"v_pk_fma_f32 v[70:71], v[76:77], v[76:77], v[70:71]\n" \
"v_pk_add_f32 v[56:57], v[56:57], v[82:83] op_sel_hi:[1,0] neg_lo:[0,1] neg_hi:[0,1]\n" \
"v_pk_fma_f32 v[70:71], v[78:79], v[78:79], v[70:71]\n" \
"v_pk_add_f32 v[58:59], v[58:59], v[82:83] op_sel_hi:[1,0] neg_lo:[0,1] neg_hi:[0,1]\n" \
"v_pk_fma_f32 v[70:71], v[80:81], v[80:81], v[70:71]\n" \
"v_pk_add_f32 v[60:61], v[60:61], v[82:83] op_sel_hi:[1,0] neg_lo:[0,1] neg_hi:[0,1]\n" \
"v_pk_add_f32 v[52:53], v[52:53], v[82:83] op_sel_hi:[1,0] neg_lo:[0,1] neg_hi:[0,1]\n" \
"v_add_f32 v70, v70, v71\n" \
"s_nop 1\n" \
"v_add_f32_dpp v70, v70, v70 row_ror:1 row_mask:0xf bank_mask:0xf\n" \
"s_nop 1\n" \
"v_add_f32_dpp v70, v70, v70 row_ror:2 row_mask:0xf bank_mask:0xf\n" \
"s_nop 1\n" \
"v_add_f32_dpp v70, v70, v70 row_ror:4 row_mask:0xf bank_mask:0xf\n" \
"s_nop 1\n" \
"v_add_f32_dpp v70, v70, v70 row_ror:8 row_mask:0xf bank_mask:0xf\n" \
"v_mov_b32 v71, v70\n" \
"s_nop 1\n" \
"v_permlane32_swap_b32 v70, v71\n" \
"v_add_f32 v70, v70, v71\n" \
"v_mov_b32 v71, v70\n" \
"s_nop 1\n" \
"v_permlane16_swap_b32 v70, v71\n" \
"v_add_f32 v70, v70, v71\n" \
"v_mul_f32 v70, 0x3acccccd, v70\n" \
"v_cvt_pk_bf16_f32 v70, v70, v70\n" \
"v_and_b32 v70, 0xffff0000, v70\n" \
"v_add_f32 v70, 0x3727c5ac, v70\n" \
"v_cvt_pk_bf16_f32 v70, v70, v70\n" \
"v_and_b32 v70, 0xffff0000, v70\n" \
"v_sqrt_f32 v70, v70\n" \
"v_cvt_pk_bf16_f32 v16, v52, v53\n" \
"v_cvt_pk_bf16_f32 v17, v54, v55\n" \
"v_cvt_pk_bf16_f32 v18, v56, v57\n" \
"v_cvt_pk_bf16_f32 v19, v58, v59\n" \
"v_cvt_pk_bf16_f32 v20, v60, v61\n" \
"v_cvt_pk_bf16_f32 v70, v70, v70\n" \
"v_and_b32 v70, 0xffff0000, v70\n" \
"v_rcp_f32 v70, v70\n" \
"v_lshlrev_b32 v52, 16, v16\n" \
"v_and_b32 v53, 0xffff0000, v16\n" \
"v_lshlrev_b32 v54, 16, v17\n" \
"v_and_b32 v55, 0xffff0000, v17\n" \
"v_lshlrev_b32 v56, 16, v18\n" \
"v_and_b32 v57, 0xffff0000, v18\n" \
"v_lshlrev_b32 v58, 16, v19\n" \
"v_and_b32 v59, 0xffff0000, v19\n" \
"v_lshlrev_b32 v60, 16, v20\n" \
"v_and_b32 v61, 0xffff0000, v20\n" \
"v_pk_mul_f32 v[52:53], v[52:53], v[70:71] op_sel_hi:[1,0]\n" \
"v_pk_mul_f32 v[54:55], v[54:55], v[70:71] op_sel_hi:[1,0]\n" \
"v_pk_mul_f32 v[56:57], v[56:57], v[70:71] op_sel_hi:[1,0]\n" \
"v_pk_mul_f32 v[58:59], v[58:59], v[70:71] op_sel_hi:[1,0]\n" \
"v_pk_mul_f32 v[60:61], v[60:61], v[70:71] op_sel_hi:[1,0]\n" \
"v_cvt_pk_bf16_f32 v16, v52, v53\n" \
"v_cvt_pk_bf16_f32 v17, v54, v55\n" \
"v_cvt_pk_bf16_f32 v18, v56, v57\n" \
"v_cvt_pk_bf16_f32 v19, v58, v59\n" \
"v_cvt_pk_bf16_f32 v20, v60, v61\n" \
"buffer_store_dwordx4 v[16:19], v4, s[40:43], s54 offen sc1\n" \
"buffer_store_dword v20, v5, s[40:43], s54 offen sc1\n" \
"buffer_load_dword v12, v2, s[40:43], 0 offen sc1\n" \
"s_add_u32 s53, s53, 0x200000\n" \
"s_xor_b32 s53, s53, s69\n" \
"s_add_u32 s54, s54, 0x200000\n" \
"s_xor_b32 s54, s54, s69\n" \
"s_add_u32 s57, s57, 0x200000\n" \
"s_xor_b32 s57, s57, s69\n" \
"v_mov_b32 v13, s52\n" \
"s_waitcnt vmcnt(1)\n" \
"s_mov_b64 exec, 15\n" \
"buffer_store_dword v13, v3, s[40:43], 0 offen sc1\n" \
"s_mov_b64 exec, -1\n" \
"s_waitcnt vmcnt(1)\n" \
"s_add_u32 s35, s35, 1\n" \
"s_cmp_lt_u32 s35, 64\n" \
"s_cbranch_scc1 RL_%=\n" \
"s_waitcnt vmcnt(0)\n" \
"s_barrier\n" \
"s_cmp_lg_u32 s50, 0\n" \
"s_cbranch_scc1 RE_%=\n" \
"v_mov_b32 v0, 1\n" \
"buffer_store_dword v0, off, s[40:43], 0 offset:256 sc1\n" \
"RE_%=:\n" \
"RCZ_%=:\n" \
"s_endpgm\n"
#define W_ASM_CLOB "s33", "s34", "s35", "s36", "s37", "s38", "s39", "s40", "s41", "s42", "s43", "s44", "s45", "s46", "s47", "s48", "s49", "s50", "s51", "s52", "s53", "s54", "s55", "s56", "s57", "s58", "s59", "s60", "s61", "s62", "s63", "s64", "s65", "s66", "s67", "s68", "s69", "s70", "s71", "s72", "s73", "s74", "s75", "s76", "s77", "s78", "s79", "s80", "s81", "s82", "s83", "s84", "s85", "s86", "s87", "s88", "s89", "s90", "s91", "s92", "s93", "s94", "s95", "s96", "s97", "s98", "s99", "vcc", "scc", "memory"
constexpr u32 MB = 1u << 20;
constexpr u32 OFF_W2 = 8 * MB, OFF_CTL = OFF_W2, CH = 16384;
constexpr int CTL_PAR = 0, CTL_TMO = 64, CTL_PAR0 = 96, CTL_TICK = 128;
constexpr u32 R_SEG = 2 * MB, R_DBG = 134 * MB;
constexpr u32 C_K = 0, C_V = 32 * CH, C_PUB = 64 * CH;
constexpr u32 C_O = 0, C_PART = 32 * CH, C_HN = 64 * CH, C_XN = 100 * CH;
constexpr u32 C_H = 72 * CH, C_ACT = 80 * CH;
constexpr u32 C_FKV = 105 * CH, C_FQA = C_FKV + 4096, C_FWO = 106 * CH, C_FF1 = C_FWO + 4096, C_FR = 110 * CH, C_FEND = 114 * CH, C_HINT = C_FR + 4096;
constexpr u32 SENT = 0xff81ff81u;

struct Params {
  const u16 *x, *mem0, *Wq, *Wk, *Wv, *Wo, *w1, *w3, *w2;
  u16* out; char* ws;
  int per, c1p, c1a, c1b, fq, scl, db, dc, inc, pad, c1q, dd;
};
#define RFL(x) __builtin_amdgcn_readfirstlane(x)

#define AFF 1
#define AFX 0
DI u32 load_votes(const Params& P) { return (u32)__builtin_amdgcn_ballot_w64(((const u32*)(P.ws + OFF_CTL))[CTL_PAR + (threadIdx.x & 31)] != 0); }
DI u32 load_c14(const Params& P, bool ring) {
  const u32 m = (u32)__builtin_amdgcn_ballot_w64(((const u32*)(P.ws + OFF_CTL))[CTL_PAR + 32 + (threadIdx.x & 31)] != 0);
  return ring ? (__builtin_popcount(m & 0x1fc0cu) > 4 ? 1u : 0u) : (__builtin_popcount(m & 0x3f1u) > 3 ? 1u : 0u);
}
DI unsigned long long load_par(const Params& P) {
  return __builtin_popcount(load_votes(P)) > 16 ? 0xaaaaaaaaaaaaaaaaull : 0x5555555555555555ull;
}
DI u32 load_par0(const Params& P) {
  const u32 m = load_votes(P), d = __builtin_popcount(m);
  return (d >= 24 ? 0xfu : d <= 8 ? 0u : m) ^ 0xau;
}
DI void role_x(const Params& P, int cls, int tix, lptr L, int tid) {
  const int wave = RFL(tid >> 6), lane = tid & 63;
  const int xid = cls * 16 + tix, h = xid >> 1, half = xid & 1;
  const u32 n = lane & 15, g = lane >> 4, q4 = n >> 2, pp = lane & 3, w = wave;
  const unsigned long long par = load_par(P);
  const u32 par0 = load_par0(P), xb = R_SEG + (u32)(par & 1) * 8192;
  {
    constexpr u32 L_VI = 10240, L_QI = 12288, L_ZW = 49152;
    const u32 l16 = lane * 16, hf = (n >> 1) & 1, i = 2 * (n >> 2) + (n & 1);
    const u32 rl = 16 * (g >> 1) + 4 * (g & 1) + (q4 & 1) + 8 * (q4 >> 1);
    u32x4 c[9];
    c[0] = (u32x4){hf ? 0xEEEEu : 0x4444u, l16, ((load_c14(P, false) ^ cls ^ AFX) & 1) * 16384 + ((g >> 1) * 4 + q4) * 128 + (g & 1) * 64 + pp * 16,
                   ((4 + 2 * w + (n >> 3)) * 4 + g) * 16384 + xid * 128 + (n & 7) * 16};
    c[1] = (u32x4){(4 * w + g) * 16384 + xid * 128 + n * 8, 0u,
                   L_QI + 2 * g * 288 + w * 64 + q4 * 16 + pp * 2, L_QI + i * 288 + g * 16};
    c[2] = (u32x4){L_VI + w * 256 + lane * 4, L_ZW + w * 28672 + l16, (2 * w + (lane >> 5)) * 1280 + (lane & 31) * 16, (4 * w + g) * 1280 + n * 16};
    const u32 r128 = w * 4 + g, r64 = w * 8 + (lane >> 3);
    c[3] = (u32x4){r128 * 8192 + ((n ^ (swz3<0, 2, 3>(r128) << 1)) << 4),
                   r64 * 8192 + (((lane & 7) ^ ((((r64 >> 2) & 1) | (((r64 >> 3) & 1) << 1)) << 1)) << 4),
                   off128<0, 2, 3>(rl, 32 * w + 4 * pp), off128<0, 2, 3>(rl, 32 * w + 16 + 4 * pp)};
    const u32 wr = 16 * (g & 1) + 4 * (g >> 1);
    c[4] = (u32x4){off64<2, 3>(rl, 16 * w + 4 * pp), wr * 512 + (64 * w + 4 * n) * 2, wr * 256 + (32 * w + 2 * n) * 2, 0u};
    const size_t pq = (size_t)(P.Wq + h * DH), pk = (size_t)(P.Wk + h * DH), pv = (size_t)(P.Wv + h * DH + half * 64), po = (size_t)(P.Wo + (size_t)(h * DH + half * 64) * DM);
    const size_t p1 = pq, p2 = pk;
    c[5] = (u32x4){(u32)p1, (u32)(p1 >> 32), (u32)p2, (u32)(p2 >> 32)};
    c[6] = (u32x4){(u32)pv, (u32)(pv >> 32), (u32)po, (u32)(po >> 32)};
    c[7] = (u32x4){xb + C_K + h * CH + w * 1024, xb + C_V + h * CH + half * 4096 + w * 1024, xb + C_PUB + h * CH + half * 4096 + w * 1024, (u32)P.scl + 40};
    c[8] = (u32x4){(u32)P.db, (u32)P.dc, (u32)half, (u32)P.dd};
#pragma unroll
    for (int k = 0; k < 9; ++k) lwr16(L + k * 4096 + tid * 16, c[k]);
    WAITL();
  }
  asm volatile(X_ASM_TEXT
               :
               : [ws] "s"(P.ws), [wave] "s"(wave), [par] "s"(par0), [dpar] "s"(~0ull), [fq] "s"(P.fq), [inc] "s"(P.inc), [lead] "s"(RFL(xid == 0 && wave == 0 ? 1 : 0)),
                 [per] "s"(P.per), [fkv] "s"(xb + C_FKV + h * 128), [dbg] "s"(R_DBG + (xid * 4 + wave) * 8192)
               : X_ASM_CLOB);
  __builtin_unreachable();
}
#define WARM 3
#define YWS 100
DI void warm(const u16* src, int ld, int rows, int chunks, int tid) {
  u32 acc = 0;
  for (int i = tid; i < rows * chunks; i += 256 * 8) {
    u32x4 t[8];
#pragma unroll
    for (int k = 0; k < 8; ++k) { const int e = min(i + k * 256, rows * chunks - 1); t[k] = *(const u32x4*)(src + (size_t)(e / chunks) * ld + (e % chunks) * 8); }
#pragma unroll
    for (int k = 0; k < 8; ++k) acc ^= t[k].x;
  }
  asm volatile("" :: "v"(acc));
}
DI void role_y(const Params& P, int cls, int tj, int tid) {
  const int wave = RFL(tid >> 6), lane = tid & 63;
  __builtin_amdgcn_s_sleep(YWS);
  if (WARM & 1) warm(P.Wo + (size_t)(cls * 16 + tj) * 64 * DM, DM, 64, 80, tid);
  if (wave == 3 || (wave == 2 && cls == 3 && tj == 3)) {
    const u32 kx = wave == 2 ? 63 : cls * 16 + tj;
    const rsrc_t rw = mkrsrc(P.ws + OFF_W2, 144 * MB);
    u32 so = R_SEG + 6 * MB + (u32)((load_par(P) >> 3) & 1) * 8192 + (kx >> 1) * CH + (kx & 1) * 4096;
    for (int s = 0; s < NSEG - 1; ++s) {
      BAR(); CFENCE();
      if (s >= 1 && s < NSEG - 3) {
        u32 acc = 0;
#pragma unroll
        for (int k = 0; k < 8; ++k) acc ^= bld16(rw, lane * 16 + (k & 3) * 1024, so + (k >> 2) * C_V).x;
        asm volatile("" :: "v"(acc));
      }
      so = (so + 2 * MB) ^ 8192;
    }
    return;
  }
  int y, c1;
  if (wave == 0) {
    if (tj < 8) { y = 16 + (tj >> 2) * 16 + (cls >> 1) * 8 + (tj & 3) * 2; c1 = tj < 4 ? P.c1q : P.c1a; }
    else { y = 48 + (cls >> 1) * 16 + (tj & 7) * 2; c1 = P.c1b; }
  }
  else if (wave == 1 && tj >= 8 && tj < 12) { y = (cls >> 1) * 8 + (tj - 8) * 2; c1 = P.c1p; }
  else if (wave == 1 && cls == 3 && tj == 3) { y = 78; c1 = P.c1b; }
  else return;
  y += (load_c14(P, true) ^ cls ^ (y >> 5) ^ AFX) & 1;
  const u32 n = lane & 15, g = lane >> 4, q4 = n >> 2, pp = lane & 3, u = lane & 7;
  const u32 par = load_par0(P);
  const u32 agst = (u32)((y >> 6) * 32768 + (y & 63) * 128) + (n >> 3) * 16384 + (2 * (u >> 1) + (g >> 1)) * 16 + (u & 1) * 8 + (g & 1) * 4;
  const u32 cc = 2 * (q4 & 1) + pp, one = pp < 2 ? ((q4 >> 1) ? 0x3f800000u : 0x00003f80u) : 0u;
  __builtin_amdgcn_s_sleep(30);
  {
    const rsrc_t rn = mkrsrc(P.ws, 8 * MB);
    const u32x4 z = {SENT, SENT, SENT, SENT};
#pragma unroll
    for (int k = 0; k < 16; ++k) bst16(rn, lane * 16 + (k & 7) * 1024, (2 + (k >> 3)) * 2 * MB + ((par >> (2 + (k >> 3))) & 1) * 8192 + y * 16384, z);
    bst4(rn, agst, (par & 1) * 8192 + 0x20000, SENT);
    if (y == 48) {
#pragma unroll
      for (int k = 0; k < 8; ++k) bst16(rn, max(k * 1024 + lane * 16, 128), (par & 1) * 8192 + 0x100000, z);
    }
    WAITV0();
  }
  asm volatile(Y_ASM_TEXT
               :
               : [ws] "s"(P.ws), [par] "s"(par), [yoff] "s"(RFL(y * 16384)), [sf] "s"(RFL(y == 48 ? P.scl : 0)), [ms] "s"(RFL(tj == 13 ? 0u : 0x7ff00000u)),
                 [hint] "s"(RFL(y == 48 ? R_SEG + (u32)((load_par(P) & 1) ^ 1) * 8192 + C_HINT : 0x7ff00000u)),
                 [per] "s"(P.per), [c1] "s"(RFL(c1)), [dbg] "s"(RFL(R_DBG + (256 + y) * 8192)),
                 [l16] "v"(lane * 16), [agst] "v"(agst), [sel0] "v"(cc == 0 ? one : 0u), [sel1] "v"(cc == 1 ? one : 0u),
                 [sel2] "v"(cc == 2 ? one : 0u), [sel3] "v"(cc == 3 ? one : 0u)
               : Y_ASM_CLOB);
  __builtin_unreachable();
}
DI u32x4 mkdesc(const void* p, u32 bytes) {
  const size_t a = (size_t)p;
  return (u32x4){(u32)RFL((u32)a), (u32)RFL((u32)(a >> 32)) & 0xffffu, bytes, 0x00020000u};
}
DI void dma16(u32x4 d, u32 voff, u32 soff, u32 lds) {
  u32 keep;
  asm volatile("s_mov_b32 %0, m0\n\ts_mov_b32 m0, %4\n\ts_nop 0\n\tbuffer_load_dwordx4 %1, %2, %3 offen sc1 lds\n\ts_mov_b32 m0, %0"
               : "=&s"(keep) : "v"(voff), "s"(d), "s"(soff), "s"(lds) : "memory");
}
#define SB() __builtin_amdgcn_sched_barrier(0)
#define WAITV(n) asm volatile("s_waitcnt vmcnt(" #n ")" ::: "memory")
DI u32 bld4(rsrc_t r, u32 vo, u32 so) { return __builtin_amdgcn_raw_buffer_load_b32(r, vo, so, SC1); }
DI u32x2 bld8(rsrc_t r, u32 vo, u32 so) { return __builtin_amdgcn_raw_buffer_load_b64(r, vo, so, SC1); }
DI void wait_ge(rsrc_t r, u32 fo, u32 want, int& budget) {
  while (true) {
    CFENCE();
    const u32 v = bld4(r, fo, 0);
    if (__builtin_amdgcn_ballot_w64(v < want) == 0) break;
    if (--budget < 0) { budget = -1; break; }
    __builtin_amdgcn_s_sleep(3);
  }
  CFENCE();
}
DI void signal(rsrc_t r, u32 fo, u32 val, int tid) { if (tid == 0) bst4(r, fo, 0, val); }
DI u32x4 frag2(lptr b, int off0, int off1) { const u32x2 lo = trrd(b + off0), hi = trrd(b + off1); return (u32x4){lo.x, lo.y, hi.x, hi.y}; }
DI u32x4 pk8(f32x4 a, f32x4 b) { return (u32x4){pk2(a[0], a[1]), pk2(a[2], a[3]), pk2(b[0], b[1]), pk2(b[2], b[3])}; }
constexpr f32x4 Z4 = {0.f, 0.f, 0.f, 0.f};
#define TMO(b) do { if ((b) < 0 && (tid & 63) == 0) bst4(rw, 4 * CTL_TMO, 0, 1u); } while (0)

#define ENDS 0
#define WST(k, s)
template <int C> DI float dppadd(float x) { return x + fbits(__builtin_amdgcn_update_dpp(0u, ubits(x), C, 0xf, 0xf, true)); }
DI float wsum(float x) { return xsum(dppadd<0x128>(dppadd<0x124>(dppadd<0x122>(dppadd<0x121>(x))))); }
DI void ln_row(float (&v)[10]) {
  float a = 0.f;
#pragma unroll
  for (int i = 0; i < 10; ++i) a += v[i];
  const float mean = wsum(a) * (1.0f / DM);
  float q = 0.f;
#pragma unroll
  for (int i = 0; i < 10; ++i) { const float d = v[i] - mean; q += d * d; }
  const float var = rbf(wsum(q) * (1.0f / DM));
  const float ri = 1.0f / rbf(sqrtf(rbf(var + 1e-5f))), mb = rbf(mean);
#pragma unroll
  for (int i = 0; i < 10; i += 2) { const u32 d = pk2(v[i] - mb, v[i + 1] - mb); v[i] = bflo(d) * ri; v[i + 1] = bfhi(d) * ri; }
}
#define W_RUN(NAME, NG, WID)                                                                                   \
  _Pragma("unroll") for (int k_ = 0; k_ < (NG); ++k_) lwr16(L + k_ * 4096 + tid * 16, c[k_]);                  \
  WAITL();                                                                                                     \
  asm volatile(NAME##_ASM_TEXT : : [ws] "s"(P.ws), [wave] "s"(wave), [dpar] "s"(~0ull), [dbg] "s"(RFL(R_DBG + 4 * MB + (WID) * 2048)) : W_ASM_CLOB); \
  __builtin_unreachable();
#define W_IDX const int wave = RFL(tid >> 6); const u32 lane = tid & 63, w = wave, n = lane & 15, g = lane >> 4, q4 = n >> 2, pp = lane & 3, l16 = lane * 16; \
  const u32 r128 = w * 4 + g, r64 = w * 8 + (lane >> 3); const unsigned long long par = load_par(P); const u32 xb = R_SEG + (u32)(par & 1) * 8192, wb = xb ^ 8192, rowo = q4 * CH + pp * 2048; (void)rowo; (void)r128; (void)r64; (void)q4; (void)pp;
DI u32x4 ptr2(const void* a, const void* b) { return (u32x4){(u32)(size_t)a, (u32)((size_t)a >> 32), (u32)(size_t)b, (u32)((size_t)b >> 32)}; }
template <int B1, int B2> DI u32 dv64(u32 r, u32 lane, u32 pitch) { return r * pitch + (((lane & 7) ^ ((((r >> B1) & 1) | (((r >> B2) & 1) << 1)) << 1)) << 4); }
constexpr u32x4 U0 = {0u, 0u, 0u, 0u};

DI void role_kv(const Params& P, int h, lptr L, int tid) {
  W_IDX
  u32x4 c[5];
  c[0] = (u32x4){l16, r128 * 8192 + ((n ^ (swz3<0, 1, 3>(r128) << 1)) << 4), lane * 8192, 0u};
#pragma unroll
  for (int t = 0; t < 4; ++t) c[1][t] = (w >> 1) * 16384 + off128<0, 1, 3>(8 * g + q4, 64 * (w & 1) + 16 * t + 4 * pp);
  c[2] = ptr2(P.Wk + h * DH, P.Wv + h * DH);
  c[3] = (u32x4){R_SEG + C_FKV + h * 128, xb + h * CH + (w < 2 ? C_K + w * 2048 : C_V + (w - 2) * 4096), xb + C_PUB + h * CH + w * 2048, wb + C_XN + w * 1024};
  c[4] = U0;
  W_RUN(KV, 5, h)
}
DI void role_qa(const Params& P, int h, lptr L, int tid) {
  W_IDX
  const u32 jt = w & 1, eh = w >> 1, ki = 131072 + w * 4352;
  u32x4 c[6];
  c[0] = (u32x4){l16, r128 * 8192 + ((n ^ (swz3<0, 1, 3>(r128) << 1)) << 4), off128<0, 1, 3>(8 * g + q4, 32 * w + 4 * pp), off128<0, 1, 3>(8 * g + q4, 32 * w + 16 + 4 * pp)};
  c[1] = (u32x4){0u, 81920 + w * 1024 + l16, 81920 + jt * 4096 + l16, 98304 + 8192 + eh * 4096 + l16};
  c[2] = (u32x4){ki + g * 1088 + n * 8, ki + pp * 1088 + (4 * g + q4) * 8, wb + C_FKV + h * 128, 16 * jt + n - 4 * g};
  c[3] = ptr2(P.Wq + h * DH, P.Wq);
  c[4] = (u32x4){wb + C_FQA + h * 128, xb + C_K + h * CH + w * 1024, xb + C_PUB + h * CH + eh * 4096, wb + C_O + h * CH + jt * 4096 + eh * 2048};
  c[5] = (u32x4){wb + C_XN + w * 1024, wb + C_HINT + (h >> 3) * CH, 0u, 0u};
  W_RUN(QA, 6, 32 + h)
}
DI void role_wo(const Params& P, int kk, int cc, lptr L, int tid) {
  W_IDX
  u32x4 c[5];
  c[0] = (u32x4){l16, r128 * 1280 + ((n ^ (swz3<0, 1, 2>(r128) << 1)) << 4), off128<0, 1, 2>(4 * g + q4, 32 * w + 8 * pp), off128<0, 1, 2>(4 * g + q4, 32 * w + 8 * pp + 4)};
  c[1] = (u32x4){0u, w ? 0x7ff00000u : wb + C_FQA + (kk * 8 + (lane >> 3)) * 128, n * 64 + g * 16, 0u};
  c[2] = ptr2(P.Wo + (size_t)kk * 1024 * DM + cc * 128, P.Wo);
  c[3] = (u32x4){wb + C_FWO + (kk * 5 + cc) * 128, wb + C_O + kk * 8 * CH + w * 1024, wb + C_PART + (kk * 5 + cc) * CH + w * 2048, 0u};
  c[4] = U0;
  W_RUN(WO, 5, 64 + kk * 5 + cc)
}
DI void role_f1(const Params& P, int j, int q, lptr L, int tid) {
  W_IDX
  u32x4 c[5];
  c[0] = (u32x4){l16, dv64<1, 3>(r64, lane, 3456), off64<1, 3>(8 * g + q4, 32 * (w >> 1) + 4 * pp), (w & 1) * 1024 + n * 64 + (g ^ ((0u - q4) & 3)) * 16};
  c[1] = (u32x4){w ? 0x7ff00000u : wb + C_FR + q * CH + (lane >> 1) * 128, g * CH + q4 * 2048 + ((lane & 3) ^ ((0u - g) & 3)) * 16, off64<1, 3>(8 * g + q4, 32 * (w >> 1) + 16 + 4 * pp), 0u};
  c[2] = ptr2(P.w1 + j * 64, P.w3 + j * 64);
  c[3] = (u32x4){wb + C_FF1 + j * 128, wb + C_HN + (w & 1) * 4 * CH + (w >> 1) * 64, wb + C_ACT + (j >> 1) * CH + ((j & 1) * 4 + w) * 1024, 0u};
  c[4] = U0;
  W_RUN(F1, 5, 88 + j)
}
DI void role_f2(const Params& P, int id, int q, lptr L, int tid) {
  W_IDX
  const u32 jt = q & 1, cc = jt ? id - 5 : id;
  u32x4 c[7];
  c[0] = (u32x4){l16, r128 * 1280 + ((n ^ (swz3<0, 1, 2>(r128) << 1)) << 4), wb + C_FF1 + q * CH + min(2 * (lane >> 2) + (w >> 1), 26u - (w >> 1)) * 128,
                 rowo + (cc * 128 + w * 32 + 8 * g) * 2};
#pragma unroll
  for (int t = 0; t < 4; ++t) c[1][t] = w * 8192 + off128<0, 1, 2>(4 * g + q4, 32 * (t ^ w) + 8 * pp);
  c[2] = (u32x4){((1 ^ w) * 8 + 1) * 1024 + l16, ((2 ^ w) * 8 + 2) * 1024 + l16, ((3 ^ w) * 8 + 3) * 1024 + l16, w * 8192 + l16};
  c[3] = (u32x4){0u, 0u, n * 1280 + (cc * 128 + w * 32 + 8 * g) * 2, 65536 + w * 16384 + l16};
  c[4] = ptr2(P.w2 + cc * 128, P.out);
  c[5] = (u32x4){jt * 16 * 1280, wb + C_ACT + w * 2048 + jt * 1024, (w < 2 ? 13u : 12u) * CH, wb + C_H + jt * 4 * CH};
  c[6] = U0;
  W_RUN(F2, 7, 115 + id)
}
DI void role_r(const Params& P, int j, int q, lptr L, int tid) {
  W_IDX
  const u32 row = j * 4 + w, ro = (row >> 4) * 1024 + (row & 15) * 64;
  u32x4 c[5];
  c[0] = (u32x4){g * CH + q4 * 2048 + ro + pp * 16, 4 * CH + g * 2048 + ro + n * 4, w ? 0x7ff00000u : wb + C_FWO + q * CH + min(lane >> 1, 19u) * 128, wb + C_FR + lane * CH + row * 128};
  c[1] = (u32x4){l16, 1024 + lane * 4, 0u, 0u};
  c[2] = ptr2(P.x, P.x);
  c[3] = (u32x4){wb + C_H + j * CH + w * 2048, wb + C_PART, wb + C_HN + j * CH + w * 2048, row * 1280};
  c[4] = U0;
  W_RUN(R, 5, 84 + (j & 3))
}
#define WSTR 128
#define FWS 100
#define XSW 0

constexpr int PRO_BLOCKS = 256, PRO_LDS = 96 * 1024;
__global__ void __launch_bounds__(1024) prologue(Params P) {
  extern __shared__ __attribute__((aligned(16))) char smem[];
  lptr L = (lptr)smem;
  const int bx = blockIdx.x, tid = threadIdx.x, wave = tid >> 6, lane = tid & 63;
  const int b = (bx & 7) == 0 ? 0 : 16 + bx - (bx >> 3);
  u32* ctl = (u32*)(P.ws + OFF_CTL);
  const u32x4 z = {SENT, SENT, SENT, SENT};
  if (b == 0) {
    const int i = bx >> 3, pg = i < 4 ? i : i < 10 ? 0 : i < 17 ? 2 + (i & 1) : (i - 16) * 4 + (i & 1);
    if (wave == 0) {
      char* p = P.ws + (size_t)pg * 2 * MB + 0x1f8000 + (i & 15) * 512;
      if (lane < 40) asm volatile("global_store_dwordx4 %0, %1, off sc1\n s_waitcnt vmcnt(0)" :: "v"(p + ((lane >> 3) & 3) * 8192 + (lane >> 5) * 256 + (lane & 7) * 16), "v"(z) : "memory");
      u32 t0 = 0, t1 = 0, t2 = 0, t3 = 0;
#pragma unroll 1
      for (int k = 4; k >= 0; --k) {
        u32 v;
        const u32 c1 = (u32)__builtin_amdgcn_s_memtime();
        asm volatile("s_waitcnt lgkmcnt(0)\n global_load_dword %0, %1, off sc1\n s_waitcnt vmcnt(0)" : "=v"(v) : "v"(p + (k & 3) * 8192 + (k >> 2) * 256) : "memory");
        const u32 c2 = (u32)__builtin_amdgcn_s_memtime();
        asm volatile("s_waitcnt lgkmcnt(0)" ::: "memory");
        t3 = t2; t2 = t1; t1 = t0; t0 = c2 - c1;
      }
      const u32 xc = xcc_id(), xh = (t1 + t3 < t0 + t2 ? 1u : 0u) ^ (xc >= 4 ? 1u : 0u);
      const u32 f14 = (xh ? t3 < t1 : t2 < t0) ? 1u : 0u;
      if (lane == 0) { ctl[CTL_PAR + i] = xh ^ (pg & 1); ctl[CTL_PAR + 32 + i] = f14 ^ 1u ^ (xc >= 4 ? 1u : xc & 1u); }
    }
  } else if (b < 145) {
    const int c = b - 17;
    const u16* xr = P.x + (size_t)(c * 16 + wave) * DM;
    const u32x4 xa = *(const u32x4*)(xr + lane * 8);
    const u32 xc = *(const u32*)(xr + 512 + lane * 2);
    float v[10] = {bflo(xa.x), bfhi(xa.x), bflo(xa.y), bfhi(xa.y), bflo(xa.z), bfhi(xa.z), bflo(xa.w), bfhi(xa.w), bflo(xc), bfhi(xc)};
    ln_row(v);
    lwr16(L + lane * 272 + wave * 16, (u32x4){pk2(v[0], v[1]), pk2(v[2], v[3]), pk2(v[4], v[5]), pk2(v[6], v[7])});
    lwr4(L + (64 + (lane >> 2)) * 272 + wave * 16 + (lane & 3) * 4, pk2(v[8], v[9]));
    __syncthreads();
    char* d = P.ws + OFF_W2 + R_SEG + (size_t)(c >> 1) * 2 * MB + C_XN;
    for (int e = tid; e < 1280; e += 1024) {
      const u32x4 t = lrd16(L + (e >> 4) * 272 + (e & 15) * 16);
      const u32 o = (c & 1) * 20480 + e * 16;
      *(u32x4*)(d + (o >> 13) * CH + (o & 8191)) = t; *(u32x4*)(d + 8192 + (o >> 13) * CH + (o & 8191)) = t;
    }
  } else {
    const int c = b - 145;
    if (c < 80) *(u32x4*)(P.ws + 2 * MB + c * 16384 + tid * 16) = z;
    else if (c < 82) { *(u32x4*)(P.ws + 65536 + (c - 80) * 32768 + tid * 16) = z; *(u32x4*)(P.ws + 65536 + 16384 + (c - 80) * 32768 + tid * 16) = z; }
    else if (c < 91) *(u32x4*)(P.ws + OFF_W2 + R_SEG + C_FKV + (c - 82) * 16384 + tid * 16) = U0;
    else if (c == 91) {
      if (tid < 640) {
        const int t = tid >> 6, l = tid & 63, i = 2 * ((l >> 3) & 3) + (l & 1), col = 64 * t + 32 * (l >> 5) + 8 * ((l >> 1) & 3);
        u32x4 v = *(const u32x4*)(P.mem0 + i * DM + col);
#pragma unroll
        for (int k = 0; k < 4; ++k) if (v[k] == SENT) v[k] |= 0x00400040u;
        const u32 o = (t >> 3) * 32768 + (t & 7) * 1024 + l * 16;
#pragma unroll
        for (int k = 0; k < 4; ++k) *(u32x4*)(P.ws + o + k * 8192) = v;
      }
    } else if (c == 92) {
      if (tid < 512) ctl[CTL_TICK + tid] = 0;
      if (tid < 32) ctl[CTL_TMO + tid] = 0;
      if (tid < 16) *(u32x4*)(P.ws + 0x100000 + (tid >> 3) * 8192 + (tid & 7) * 16) = z;
    }
  }
}

__global__ void __launch_bounds__(256, 1) mega(Params P) {
  extern __shared__ __attribute__((aligned(16))) char smem[];
  lptr L = (lptr)smem;
  const int tid = threadIdx.x;
  if (tid == 0) {
    u32* tk = (u32*)(P.ws + OFF_CTL) + CTL_TICK;
    int c = xcc_id() & 7, role = 0;
    for (int i = 0; i < 8; ++i) { const u32 t = atomicAdd(&tk[c * 64], 1u); if (t < 32) { role = c * 32 + t; break; } c = (c + 1) & 7; }
    lwr4(L, role);
  }
  __syncthreads();
  const int role = RFL(*(LAS int*)L);
  __syncthreads();
  const int cls = role >> 5, tix = role & 31;
  const int q = cls & 3;
  int kind, a;
  if (cls < 4) {
    if (tix < 16) { kind = 6; a = tix ^ XSW; } else if (role == 127) { kind = 3; a = 7; } else { kind = 7; a = tix - 16; }
  } else {
    if (tix < 8) { kind = 0; a = q * 8 + tix; } else if (tix < 16) { kind = 1; a = q * 8 + tix - 8; } else if (tix < 21) { kind = 2; a = tix - 16; }
    else if (tix == 21) { kind = 3; a = q; } else if (role == 252) { kind = 3; a = 4; } else if (tix < 29) { kind = 4; a = q * 7 + tix - 22; }
    else if (tix == 31 && q >= 2) { kind = 3; a = 3 + q; } else { kind = 5; a = (q & 1) * 5 + (q >> 1) * 3 + tix - 29; }
  }
  kind = RFL(kind); a = RFL(a);
  if ((WARM & 2) && cls >= 4 && tix >= 8) {
    const int i = (q * 24 + tix - 8) * 4 + (tid >> 6);
    const int l = (tid & 63) * (WSTR / 2);
    constexpr int PER = 32 * WSTR;
    u32 acc = 0;
#pragma unroll 2
    for (int k = i; k < 640 * 4096 / PER; k += 384) {
      const size_t o = (size_t)k * PER + l;
      acc ^= *(const u32*)(P.Wk + o) ^ *(const u32*)(P.Wv + o) ^ *(const u32*)(P.Wq + o) ^ *(const u32*)(P.Wo + o);
    }
    asm volatile("" :: "v"(acc));
    __builtin_amdgcn_s_sleep(FWS);
#pragma unroll 2
    for (int k = i; k < 640 * 1728 / PER; k += 384) {
      const size_t o = (size_t)k * PER + l;
      acc ^= *(const u32*)(P.w1 + o) ^ *(const u32*)(P.w3 + o) ^ *(const u32*)(P.w2 + o);
    }
    asm volatile("" :: "v"(acc));
  }
  if (kind == 6) role_x(P, cls, a, L, tid);
  else if (kind == 7) role_y(P, cls, a, tid);
  else if (kind == 0) role_kv(P, a, L, tid);
  else if (kind == 1) role_qa(P, a, L, tid);
  else if (kind == 2) role_wo(P, q, a, L, tid);
  else if (kind == 3) role_r(P, a, q, L, tid);
  else if (kind == 4) role_f1(P, a, q, L, tid);
  else if (kind == 5) role_f2(P, a, q, L, tid);
}

#define T_C1P 2760
#define T_C1A 2950
#define T_C1B 3090
#define T_PER 3770
#define T_DB 64
#define T_C1Q 2840
#define T_DD 64
#define T_DC 88
extern "C" void kernel_launch(void* const* d_in, const int* in_sizes, int n_in, void* d_out, int out_size, void* d_ws, size_t ws_size, hipStream_t stream) {
  Params P;
  P.x = (const u16*)d_in[0]; P.mem0 = (const u16*)d_in[1]; P.Wq = (const u16*)d_in[2]; P.Wk = (const u16*)d_in[3];
  P.Wv = (const u16*)d_in[4]; P.Wo = (const u16*)d_in[5]; P.w1 = (const u16*)d_in[8]; P.w3 = (const u16*)d_in[9]; P.w2 = (const u16*)d_in[10];
  P.out = (u16*)d_out;
  P.ws = (char*)(((size_t)d_ws + 2 * MB - 1) & ~(size_t)(2 * MB - 1));
  P.per = T_PER; P.c1p = T_C1P; P.c1a = T_C1A; P.c1b = T_C1B; P.fq = 2550; P.scl = 1050; P.db = (T_DB / 8) * 4; P.dc = (T_DC / 8) * 4; P.inc = 24; P.pad = 0; P.c1q = T_C1Q; P.dd = (T_DD / 8) * 4;
  (void)hipFuncSetAttribute((const void*)mega, hipFuncAttributeMaxDynamicSharedMemorySize, 160 * 1024);
  (void)hipFuncSetAttribute((const void*)prologue, hipFuncAttributeMaxDynamicSharedMemorySize, PRO_LDS);
  prologue<<<PRO_BLOCKS, 1024, PRO_LDS, stream>>>(P);
  mega<<<256, 256, 160 * 1024, stream>>>(P);
}
```
